```python
import torch

def _softmax(x, axis=-1):
    return torch.softmax(x, dim=axis)
def _gelu(x):
    return torch.nn.functional.gelu(x, approximate="tanh")
def _silu(x):
    return torch.nn.functional.silu(x)
def _sigmoid(x):
    return torch.sigmoid(x)
def _layernorm(x, axis=-1, eps=1e-5):
    m = x.mean(dim=axis, keepdim=True)
    v = x.var(dim=axis, keepdim=True, unbiased=False)
    return (x - m) / torch.sqrt(v + eps)
def _scatter_add(tgt, idx, src, axis=0):
    return tgt.clone().index_add_(axis, idx, src)
def _segment_sum(src, seg, n):
    out = torch.zeros((n,) + src.shape[1:], dtype=src.dtype)
    return out.index_add_(0, seg, src)
def _pad(x, pw):
    flat = tuple(v for pair in reversed(pw) for v in pair)
    return torch.nn.functional.pad(x, flat)

kernel_name = 'sequential_preLN__linear_attn_block__mixer_s_mlp__prompt'

def setup_inputs():

    _w = (lambda a: (lambda t: t.to(torch.bfloat16) if t.is_floating_point() else t)(torch.as_tensor(a)))
    def _A_setup():
        B, S, D = 1, 2048, 576
        return {
            "x": _w((lambda *s: torch.randn(*s, dtype=torch.bfloat16) * 0.05)(B, S, D)),
            "wqkv": _w((lambda *s: torch.randn(*s, dtype=torch.bfloat16) * 0.05)(D, 3 * D)),
            "wo": _w((lambda *s: torch.randn(*s, dtype=torch.bfloat16) * 0.05)(D, D)),
        }
    _A_in = _A_setup()

    def _B_setup():
        B, S, D, F = 1, 2048, 576, 4608
        return {
            "x":  _w((lambda *s: torch.randn(*s, dtype=torch.bfloat16) * 0.05)(B, S, D)),
            "w1": _w((lambda *s: torch.randn(*s, dtype=torch.bfloat16) * 3.2)(D, D) * (D ** -0.5)),
            "w2": _w((lambda *s: torch.randn(*s, dtype=torch.bfloat16) * 3.2)(D, D) * (D ** -0.5)),
            "w3": _w((lambda *s: torch.randn(*s, dtype=torch.bfloat16) * 3.2)(D, F) * (D ** -0.5)),
            "w4": _w((lambda *s: torch.randn(*s, dtype=torch.bfloat16) * 1.6)(F, D) * (F ** -0.5)),
        }
    _B_in = _B_setup()

    return {
        "A_x": _A_in["x"],
        "A_wqkv": _A_in["wqkv"],
        "A_wo": _A_in["wo"],
        "B_x": _B_in["x"],
        "B_w1": _B_in["w1"],
        "B_w2": _B_in["w2"],
        "B_w3": _B_in["w3"],
        "B_w4": _B_in["w4"],
    }

def reference(A_x, A_wqkv, A_wo, B_x, B_w1, B_w2, B_w3, B_w4):
    def _A_ref(x, wqkv, wo):
        B, S, D = x.shape
        H = 32; Dh = D // H
        qkv = (lambda x, *d: x.permute(*d))((x @ wqkv).reshape(B, S, 3, H, Dh), 2, 0, 3, 1, 4)
        q, k, v = qkv[0], qkv[1], qkv[2]
        phi = lambda t: torch.where(t > 0, t + 1, torch.exp(t)) + 1e-6
        pq, pk = phi(q), phi(k)
        kv = torch.einsum("bhsd,bhse->bhde", pk, v)
        z = torch.einsum("bhsd->bhd", pk)
        num = torch.einsum("bhqd,bhde->bhqe", pq, kv)
        den = torch.einsum("bhqd,bhd->bhq", pq, z)[..., None] + 1e-6
        o = num / den
        return (lambda x, *d: x.permute(*d))(o, 0, 2, 1, 3).reshape(B, S, D) @ wo

    def _B_ref(x, w1, w2, w3, w4):
        B, S, D = x.shape
        h = _gelu(_layernorm(x, axis=-1) @ w1)
        c = D // 4
        g0 = _pad(h[:, :, 0*c:1*c], ((0, 0), (1, 0), (0, 0)))[:, :S, :]
        g1 = _pad(h[:, :, 1*c:2*c], ((0, 0), (0, 1), (0, 0)))[:, 1:, :]
        g2 = _pad(h[:, :, 2*c:3*c], ((0, 0), (2, 0), (0, 0)))[:, :S, :]
        g3 = _pad(h[:, :, 3*c:   ], ((0, 0), (0, 2), (0, 0)))[:, 2:, :]
        hs = torch.concatenate([g0, g1, g2, g3], axis=-1) @ w2
        x1 = x + hs
        h2 = _gelu(_layernorm(x1, axis=-1) @ w3) @ w4
        return x1 + h2

    x = A_x
    h = x + _A_ref(_layernorm(x, axis=-1), A_wqkv, A_wo)
    h2 = h + _B_ref(_layernorm(h, axis=-1), B_w1, B_w2, B_w3, B_w4)
    return h2
```

```cpp
#include <hip/hip_runtime.h>
#include <hip/hip_bf16.h>
typedef __hip_bfloat16 bf16;
constexpr int S=2048, D=576, H=32, DH=18, F=4608;

__device__ float b2f(bf16 v){ return __bfloat162float(v);}

__global__ void ln_k(const float* in, float* out){
  int r=blockIdx.x; __shared__ float red[256];
  float s=0; for(int i=threadIdx.x;i<D;i+=256) s+=in[r*D+i];
  red[threadIdx.x]=s; __syncthreads();
  for(int o=128;o>0;o>>=1){ if(threadIdx.x<o) red[threadIdx.x]+=red[threadIdx.x+o]; __syncthreads(); }
  float m=red[0]/D; __syncthreads();
  s=0; for(int i=threadIdx.x;i<D;i+=256){ float d=in[r*D+i]-m; s+=d*d; }
  red[threadIdx.x]=s; __syncthreads();
  for(int o=128;o>0;o>>=1){ if(threadIdx.x<o) red[threadIdx.x]+=red[threadIdx.x+o]; __syncthreads(); }
  float rs=1.f/sqrtf(red[0]/D+1e-5f);
  for(int i=threadIdx.x;i<D;i+=256) out[r*D+i]=(in[r*D+i]-m)*rs;
}
__global__ void cvt_k(const bf16* in, float* out, int n){ int i=blockIdx.x*256+threadIdx.x; if(i<n) out[i]=b2f(in[i]); }
template<int ACT> __global__ void gemm_k(const float* A, const bf16* W, float* C, int M,int N,int K){
  __shared__ float As[16][17], Ws[16][17];
  int tx=threadIdx.x&15, ty=threadIdx.x>>4; int row=blockIdx.y*16+ty, col=blockIdx.x*16+tx; float acc=0;
  for(int k0=0;k0<K;k0+=16){ As[ty][tx]=A[row*K+k0+tx]; Ws[ty][tx]=b2f(W[(k0+ty)*N+col]); __syncthreads();
    for(int k=0;k<16;k++) acc+=As[ty][k]*Ws[k][tx]; __syncthreads(); }
  if(ACT==1){ float u=0.7978845608f*(acc+0.044715f*acc*acc*acc); acc=0.5f*acc*(1.f+tanhf(u)); }
  C[row*N+col]=acc;
}
__device__ float phi(float t){ return (t>0? t+1.f: expf(t))+1e-6f; }
__global__ void kv_k(const float* qkv, float* kv, float* z){
  int h=blockIdx.x; int t=threadIdx.x; if(t>=DH*DH+DH) return;
  float acc=0;
  if(t<DH*DH){ int d=t/DH,e=t%DH; for(int s=0;s<S;s++) acc+=phi(qkv[s*3*D+D+h*DH+d])*qkv[s*3*D+2*D+h*DH+e]; kv[h*DH*DH+t]=acc; }
  else { int d=t-DH*DH; for(int s=0;s<S;s++) acc+=phi(qkv[s*3*D+D+h*DH+d]); z[h*DH+d]=acc; }
}
__global__ void o_k(const float* qkv,const float* kv,const float* z,float* o){ int i=blockIdx.x*256+threadIdx.x; if(i>=S*D) return;
  int s=i/D,c=i%D,h=c/DH,e=c%DH; float num=0,den=0; for(int d=0;d<DH;d++){ float p=phi(qkv[s*3*D+h*DH+d]); num+=p*kv[h*DH*DH+d*DH+e]; den+=p*z[h*DH+d]; }
  o[i]=num/(den+1e-6f);
}
__global__ void add_k(const float* a,const float* b,float* c,int n){ int i=blockIdx.x*256+threadIdx.x; if(i<n) c[i]=a[i]+b[i]; }
__global__ void shift_k(const float* a,float* c){ int i=blockIdx.x*256+threadIdx.x; if(i>=S*D) return; int s=i/D,d=i%D; int g=d/144; int sh=(g==0)?-1:(g==1)?1:(g==2)?-2:2; int ss=s+sh; c[i]=(ss>=0&&ss<S)?a[ss*D+d]:0.f; }
__global__ void fin_k(const float* h,const float* x1,const float* h2,bf16* out){ int i=blockIdx.x*256+threadIdx.x; if(i<S*D) out[i]=__float2bfloat16(h[i]+x1[i]+h2[i]); }

extern "C" void kernel_launch(void* const* d_in, const int* in_sizes, int n_in, void* d_out, int out_size, void* d_ws, size_t ws_size, hipStream_t stream){
  const bf16* x=(const bf16*)d_in[0]; const bf16* wqkv=(const bf16*)d_in[1]; const bf16* wo=(const bf16*)d_in[2];
  const bf16* w1=(const bf16*)d_in[4]; const bf16* w2=(const bf16*)d_in[5]; const bf16* w3=(const bf16*)d_in[6]; const bf16* w4=(const bf16*)d_in[7];
  float* ws=(float*)d_ws; int n=S*D; int nb=(n+255)/256;
  float* xf=ws; float* xn=xf+n; float* qkv=xn+n; float* kv=qkv+3*n; float* z=kv+16384; float* o=z+1024; float* a=o+n; float* h=a+n; float* hn=h+n; float* hnn=hn+n;
  float* g=hnn+n; float* gs=g+n; float* hs=gs+n; float* x1=hs+n; float* x1n=x1+n; float* hh=x1n+n; float* h2=hh+(size_t)S*F;
  cvt_k<<<nb,256,0,stream>>>(x,xf,n);
  ln_k<<<S,256,0,stream>>>(xf,xn);
  gemm_k<0><<<dim3(3*D/16,S/16),256,0,stream>>>(xn,wqkv,qkv,S,3*D,D);
  kv_k<<<H,384,0,stream>>>(qkv,kv,z);
  o_k<<<nb,256,0,stream>>>(qkv,kv,z,o);
  gemm_k<0><<<dim3(D/16,S/16),256,0,stream>>>(o,wo,a,S,D,D);
  add_k<<<nb,256,0,stream>>>(xf,a,h,n);
  ln_k<<<S,256,0,stream>>>(h,hn);
  ln_k<<<S,256,0,stream>>>(hn,hnn);
  gemm_k<1><<<dim3(D/16,S/16),256,0,stream>>>(hnn,w1,g,S,D,D);
  shift_k<<<nb,256,0,stream>>>(g,gs);
  gemm_k<0><<<dim3(D/16,S/16),256,0,stream>>>(gs,w2,hs,S,D,D);
  add_k<<<nb,256,0,stream>>>(hn,hs,x1,n);
  ln_k<<<S,256,0,stream>>>(x1,x1n);
  gemm_k<1><<<dim3(F/16,S/16),256,0,stream>>>(x1n,w3,hh,S,F,D);
  gemm_k<0><<<dim3(D/16,S/16),256,0,stream>>>(hh,w4,h2,S,D,F);
  fin_k<<<nb,256,0,stream>>>(h,x1,h2,(bf16*)d_out);
}
```

```cpp
#include <hip/hip_runtime.h>
#include <hip/hip_bf16.h>

namespace mk {
typedef unsigned v4u __attribute__((ext_vector_type(4)));
typedef unsigned v2u __attribute__((ext_vector_type(2)));
typedef float f4 __attribute__((ext_vector_type(4)));
typedef float f2 __attribute__((ext_vector_type(2)));
typedef short s8 __attribute__((ext_vector_type(8)));
typedef short s4 __attribute__((ext_vector_type(4)));
typedef __bf16 bf2 __attribute__((ext_vector_type(2)));
typedef unsigned long long u64;
typedef __amdgpu_buffer_rsrc_t rsrc_t;
#define LAS __attribute__((address_space(3)))
#define DEV __device__ __forceinline__

#define MAPMODE 3

constexpr int W_EPOCH = 0;
constexpr int W_FKV = 1024;
constexpr int W_FPT = W_FKV + 2048;
constexpr int W_FO = 8192;
constexpr int W_FH = W_FO + 4096;
constexpr int W_FG = W_FH + 4096;
constexpr int W_FX = W_FG + 4096;
constexpr int W_FP = 32768;
constexpr int W_RS = W_FP + 32 * 36 * 128;
constexpr int W_KVP = W_RS + 18 * 1024;
constexpr int ACT_B = 73728;
constexpr int ACT_ALL = 32 * ACT_B;
constexpr int W_AO = W_KVP + 32 * 32 * 768;
constexpr int W_AH = W_AO + ACT_ALL;
constexpr int W_AG = W_AH + ACT_ALL;
constexpr int W_AX = W_AG + ACT_ALL;
constexpr int PT_M = 648 * 1024;
constexpr int W_PT = W_AX + ACT_ALL;
constexpr int W_PART = W_PT + 19 * PT_M;
constexpr int W_END = W_PART + 32 * 8 * 36 * 2048;
constexpr int W_DBG = W_END;
constexpr int OOB = 0x40000000;
constexpr int SC1 = 16;

constexpr int L_B1 = 0;
constexpr int L_B2 = ACT_B;
constexpr int L_SP = 2 * ACT_B;
constexpr int L_PTS = L_SP;
constexpr int L_RX = L_SP + 8192;
constexpr int L_QX = L_RX + 2304;
constexpr int L_OX = L_QX + 1024;
constexpr int L_ST = L_OX + 1024;
constexpr int L_RSX = L_ST + 1024;
constexpr int L_END = L_RSX + 64;
static_assert(L_END <= 163840, "lds");

struct Params {
  const void *x, *wqkv, *wo, *w1, *w2, *w3, *w4;
  void *out, *ws;
};

DEV f4 mfma(v4u a, v4u b, f4 c) {
  return __builtin_amdgcn_mfma_f32_16x16x32_bf16(__builtin_bit_cast(s8, a), __builtin_bit_cast(s8, b), c, 0, 0, 0);
}
DEV unsigned pk(float a, float b) {
  f2 v = {a, b};
  return __builtin_bit_cast(unsigned, __builtin_convertvector(v, bf2));
}
DEV float lo2f(unsigned u) { return __builtin_bit_cast(float, u << 16); }
DEV float hi2f(unsigned u) { return __builtin_bit_cast(float, u & 0xffff0000u); }
DEV float dot2(unsigned a, unsigned b, float c) {
  return __builtin_amdgcn_fdot2_f32_bf16(__builtin_bit_cast(bf2, a), __builtin_bit_cast(bf2, b), c, false);
}
DEV rsrc_t mkrs(const void *p, int bytes) { return __builtin_amdgcn_make_buffer_rsrc((void *)p, 0, bytes, 0x00020000); }
DEV v4u ldg(rsrc_t r, int v, int s) { return __builtin_amdgcn_raw_buffer_load_b128(r, v, s, 0); }
DEV v4u ldc(rsrc_t r, int v, int s) { return __builtin_amdgcn_raw_buffer_load_b128(r, v, s, SC1); }
DEV void stc(v4u d, rsrc_t r, int v, int s) { __builtin_amdgcn_raw_buffer_store_b128(d, r, v, s, SC1); }
DEV void dma(rsrc_t r, int v, int s, int lds) {
  asm volatile("s_mov_b32 m0, %2\n\ts_nop 3\n\tbuffer_load_dwordx4 %0, %1, %3 offen lds" ::"v"(v), "s"(r), "s"(lds), "s"(s) : "memory");
}
DEV void dmac(rsrc_t r, int v, int s, int lds) {
  asm volatile("s_mov_b32 m0, %2\n\ts_nop 3\n\tbuffer_load_dwordx4 %0, %1, %3 offen sc1 lds" ::"v"(v), "s"(r), "s"(lds), "s"(s) : "memory");
}
template <int N> DEV void vmwait() { asm volatile("s_waitcnt vmcnt(%0)" ::"i"(N) : "memory"); }
DEV void bar() { asm volatile("s_waitcnt lgkmcnt(0)\n\ts_barrier" ::: "memory"); }
DEV v4u lds128(LAS char *L, int a) { return *(LAS v4u *)(L + a); }
DEV v2u lds64(LAS char *L, int a) { return *(LAS v2u *)(L + a); }
DEV v2u ldstr(LAS char *L, int a) { return __builtin_bit_cast(v2u, __builtin_amdgcn_ds_read_tr16_b64_v4i16((LAS s4 *)(L + a))); }
DEV v4u trfrag(LAS char *L, int a, int d) {
  v2u p = ldstr(L, a), q = ldstr(L, a + d);
  return v4u{p[0], p[1], q[0], q[1]};
}
template <int CTRL> DEV float dppadd(float v) {
  return v + __builtin_bit_cast(float, __builtin_amdgcn_update_dpp(0, __builtin_bit_cast(int, v), CTRL, 0xf, 0xf, true));
}
DEV float red8(float v) {
  v = dppadd<0xB1>(v);
  v = dppadd<0x4E>(v);
  return dppadd<0x141>(v);
}
DEV float phi(float t) {
  const float e = __builtin_amdgcn_fmed3f(__builtin_amdgcn_exp2f(t * 1.44269504089f), 0.f, 1.f);
  return (fmaxf(t, 0.f) + e) + 1e-6f;
}
DEV float gelu(float x) {
  const float c1 = -2.302208198f, c2 = -0.1029432397f;
  const float e = __builtin_amdgcn_exp2f(x * __builtin_fmaf(x * x, c2, c1));
  return x * __builtin_amdgcn_rcpf(1.f + e);
}
DEV v4u pairup(v2u a, v2u b) {
  auto s0 = __builtin_amdgcn_permlane16_swap(a[0], b[0], false, false);
  auto s1 = __builtin_amdgcn_permlane16_swap(a[1], b[1], false, false);
  return v4u{s0[0], s1[0], s0[1], s1[1]};
}
DEV v2u pk4(f4 v) { return v2u{pk(v[0], v[1]), pk(v[2], v[3])}; }

constexpr unsigned SPIN_CAP = 20000;
DEV void waitflag(rsrc_t rs, int voff, bool act, v2u tag, bool &dead) {
  if (dead) return;
  for (unsigned s = 0;; ++s) {
    asm volatile("" ::: "memory");
    const v2u f = __builtin_amdgcn_raw_buffer_load_b64(rs, voff, 0, SC1);
    if (__all(!act || (f[0] == tag[0] && f[1] == tag[1]))) break;
    if (s > SPIN_CAP) { dead = true; break; }
  }
  asm volatile("" ::: "memory");
}
DEV void waitall(rsrc_t rs, int base, int lane, v2u tag, bool &dead) {
  if (dead) return;
  for (unsigned s = 0;; ++s) {
    asm volatile("" ::: "memory");
    const v4u f = ldc(rs, base + lane * 16, 0), g = ldc(rs, base + lane * 16, 1024);
    const unsigned d = ((f[0] ^ tag[0]) | (f[1] ^ tag[1])) | ((f[2] ^ tag[0]) | (f[3] ^ tag[1])) | ((g[0] ^ tag[0]) | (g[1] ^ tag[1])) |
                       ((g[2] ^ tag[0]) | (g[3] ^ tag[1]));
    if (__all(d == 0)) break;
    if (s > SPIN_CAP) { dead = true; break; }
  }
  asm volatile("" ::: "memory");
}
DEV void setflag(rsrc_t rs, int off, v2u tag) { __builtin_amdgcn_raw_buffer_store_b64(tag, rs, off, 0, SC1); }

struct PtUnit { rsrc_t rs; int pitch, s0, s1, dst; };
DEV PtUnit pt_setup(const Params &p, int unit) {
  const int mat = unit / 162, rem = unit - mat * 162, u = rem / 9, c = rem - u * 9;
  PtUnit r;
  const void *src = mat == 0 ? p.wo : mat == 1 ? p.w1 : mat == 2 ? p.w2 : mat < 11 ? p.w3 : p.w4;
  r.rs = mkrs(src, mat < 3 ? 576 * 1152 : 576 * 9216);
  r.pitch = (mat >= 3 && mat < 11) ? 9216 : 1152;
  int r0 = 8 * u, r1 = 8 * u + 4, cb = 128 * c;
  if (mat == 0) {
    const int hi = u >= 9 ? 72 : 0, c9 = u >= 9 ? u - 9 : u, h = c9 >> 1;
    r0 = hi + (c9 < 8 ? 18 * h + 2 * (h & 1) + 8 * (c9 & 1) : 16);
    r1 = c9 < 8 ? r0 + 4 : hi + 52;
  } else if (mat >= 11) {
    r0 += 576 * (mat - 11), r1 += 576 * (mat - 11);
  } else if (mat >= 3) {
    cb += 1152 * (mat - 3);
  }
  r.s0 = r0 * r.pitch + cb, r.s1 = r1 * r.pitch + cb;
  r.dst = W_PT + mat * PT_M + (4 * c * 18 + u) * 1024;
  return r;
}
DEV void pt_load(const PtUnit &t, int lane, int lds) {
  const int q = lane >> 4;
  const int v = (((lane >> 3) & 1) * 144 + q) * t.pitch + (((lane & 7) ^ (2 * q)) << 4);
  dma(t.rs, v, t.s0, lds);
  dma(t.rs, v, t.s1, lds + 1024);
  dma(t.rs, v, t.s0 + 288 * t.pitch, lds + 2048);
  dma(t.rs, v, t.s1 + 288 * t.pitch, lds + 3072);
}
DEV void pt_store(const PtUnit &t, LAS char *L, rsrc_t ws, int lane, int lds) {
  const int g = lane >> 4, q = (lane >> 2) & 3, pp = lane & 3;
  const int a = lds + 128 * (16 * (g >> 1) + 2 * q + (g & 1)) + 8 * pp;
#pragma unroll
  for (int y = 0; y < 4; ++y) stc(trfrag(L, a + 32 * (y ^ q), 1024), ws, t.dst + lane * 16, y * 18 * 1024);
}

#define STAMP(n)

template <bool GRAM, int NY, class PF>
DEV void stage_gemm(LAS char *L, rsrc_t ws, int src_v, int w, const int (&xa)[4], const v4u (&Wm)[18], const v4u (&We)[18], f4 (&acc)[4],
                    f4 &accE, f4 &accG, PF pf) {
#pragma unroll
  for (int u = 0; u < 18; ++u) dmac(ws, src_v, u * 1024, L_B1 + (18 * w + u) * 1024);
  pf();
#pragma unroll
  for (int m = 0; m < 4; ++m) acc[m] = f4{0, 0, 0, 0};
  accE = f4{0, 0, 0, 0}, accG = f4{0, 0, 0, 0};
#pragma unroll
  for (int c = 0; c < 3; ++c) {
    if (c == 0) vmwait<12 + NY>();
    if (c == 1) vmwait<6 + NY>();
    if (c == 2) vmwait<NY>();
    bar();
#pragma unroll
    for (int u = 6 * c; u < 6 * c + 6; ++u) {
      v4u X[4];
#pragma unroll
      for (int m = 0; m < 4; ++m) X[m] = lds128(L, xa[m] + u * 1024);
      accE = mfma(We[u], X[0], accE);
      if (GRAM) accG = mfma(X[0], X[0], accG);
#pragma unroll
      for (int m = 0; m < 4; ++m) acc[m] = mfma(Wm[u], X[m], acc[m]);
    }
  }
}
DEV void load_w(rsrc_t ws, int vm, int ve, v4u (&Wm)[18], v4u (&We)[18]) {
#pragma unroll
  for (int u = 0; u < 18; ++u) Wm[u] = ldc(ws, vm, u * 1024);
#pragma unroll
  for (int u = 0; u < 18; ++u) We[u] = ldc(ws, ve, u * 1024);
  __builtin_amdgcn_sched_barrier(0);
}
DEV void publish(rsrc_t ws, int base, int pv0, int pv1, int pve, const v2u (&a)[4], v2u e) {
  stc(pairup(a[0], a[1]), ws, pv0, base);
  stc(pairup(a[2], a[3]), ws, pv1, base);
  stc(pairup(e, e), ws, pve, base);
}

__global__ void __launch_bounds__(256, 1) fused(Params p) {
  extern __shared__ __attribute__((aligned(16))) char smem[];
  LAS char *L = (LAS char *)smem;
  const int tid = threadIdx.x, lane = tid & 63, w = __builtin_amdgcn_readfirstlane(tid >> 6);
  const int b = blockIdx.x, xc = b & 7, qc = b >> 3;
  int i, j;
  if (MAPMODE == 1) i = qc, j = xc;
  else if (MAPMODE == 2) i = xc * 4 + (qc >> 3), j = qc & 7;
  else if (MAPMODE == 3) i = (xc >> 1) * 8 + (qc >> 2), j = (xc & 1) * 4 + (qc & 3);
  else i = (xc >> 2) * 16 + (qc >> 1), j = (xc & 3) * 2 + (qc & 1);
  const int t = lane & 15, g = lane >> 4;
  const rsrc_t ws = mkrs(p.ws, W_END), rx = mkrs(p.x, 2048 * 1152), rq = mkrs(p.wqkv, 576 * 3456), ro = mkrs(p.out, 2048 * 1152);
  bool dead = false;
  STAMP(0);

  const v2u ep = __builtin_amdgcn_raw_buffer_load_b64(ws, W_EPOCH, 0, SC1);
  v4u xr[18];
  {
    const int v = (64 * i + 16 * w + (lane >> 3)) * 1152 + (lane & 7) * 16;
#pragma unroll
    for (int a = 0; a < 18; ++a) xr[a] = ldg(rx, v, (a & 1) * 9216 + (a >> 1) * 128);
  }
  v2u xres[4], xrese;
#pragma unroll
  for (int m = 0; m < 4; ++m)
    xres[m] = __builtin_amdgcn_raw_buffer_load_b64(rx, (64 * i + 16 * (m ^ w) + t) * 1152 + (64 * j + 16 * w + 4 * g) * 2, 0, 0);
  xrese = __builtin_amdgcn_raw_buffer_load_b64(rx, (64 * i + 16 * w + t) * 1152 + (512 + 8 * j + 4 * (g & 1)) * 2, 0, 0);
  __builtin_amdgcn_sched_barrier(0);
  if (w < 2) {
    const int unit = 2 * b + w;
    if (unit < 486) {
      const PtUnit pt = pt_setup(p, unit);
      pt_load(pt, lane, L_PTS + 4096 * w);
      vmwait<0>();
      pt_store(pt, L, ws, lane, L_PTS + 4096 * w);
    }
  } else if (w == 2) {
    if (b < 72) {
      const rsrc_t r2 = mkrs(p.w2, 576 * 1152);
      float s = 0.f;
#pragma unroll
      for (int a = 0; a < 9; ++a) {
        const v4u d = ldg(r2, (8 * b + (lane >> 3)) * 1152 + (lane & 7) * 16, a * 128);
#pragma unroll
        for (int e = 0; e < 4; ++e) s = dot2(d[e], 0x3f803f80u, s);
      }
      s = red8(s) * (1.f / 576.f);
      const unsigned h = pk(s, 0.f), l2 = pk(s - lo2f(h), 0.f);
      if ((lane & 7) == 0) {
        *(LAS unsigned short *)(L + L_RSX + (lane >> 3) * 2) = (unsigned short)h;
        *(LAS unsigned short *)(L + L_RSX + 16 + (lane >> 3) * 2) = (unsigned short)l2;
      }
      asm volatile("s_waitcnt lgkmcnt(0)" ::: "memory");
      const int gg = b / 18, uu = b - 18 * gg;
      if (lane < 2) stc(lds128(L, L_RSX + lane * 16), ws, W_RS + uu * 1024 + (2 * gg + lane) * 16, 0);
    }
  } else if (lane < 32) {
    const unsigned c = (lane & 4) ? 0u : 0x3f803f80u;
    *(LAS v2u *)(L + L_RX + (lane >> 3) * 576 + 512 + (lane & 7) * 8) = v2u{c, c};
  }
  int kvo[5], qo[5];
#pragma unroll
  for (int n = 0; n < 5; ++n) {
    const int s = 64 * n + lane, rr = s / 18, c = s - 18 * rr, c9 = c < 9 ? c : c - 9;
    const int row = (rr & 3) | ((rr & 4) << 1) | ((rr & 8) >> 1);
    kvo[n] = (16 * w + row) * 3456 + (c < 9 ? 1152 : 2304) + 144 * j + 16 * c9;
    qo[n] = (64 * (w >> 1) + 16 * (w & 1) + row + (c < 9 ? 0 : 32)) * 3456 + 144 * j + 16 * c9;
  }
  auto fill = [&](const int(&o)[5], int srow, int slot) {
    const int lb = L_B2 + slot * 18432 + w * 4608;
#pragma unroll
    for (int n = 0; n < 4; ++n) dma(rq, o[n], srow * 3456, lb + n * 1024);
    if (lane < 32) dma(rq, o[4], srow * 3456, lb + 4096);
  };
  const int rot = i % 9;
  auto chunk = [&](int c) { int v = c + rot; return v >= 9 ? v - 9 : v; };
#pragma unroll
  for (int c = 0; c < 4; ++c) fill(kvo, 64 * chunk(c), c);
  STAMP(1);
  {
    float s1[2] = {0.f, 0.f}, s2[2] = {0.f, 0.f};
#pragma unroll
    for (int a = 0; a < 18; ++a)
#pragma unroll
      for (int e = 0; e < 4; ++e) s1[a & 1] = dot2(xr[a][e], 0x3f803f80u, s1[a & 1]), s2[a & 1] = dot2(xr[a][e], xr[a][e], s2[a & 1]);
    float rs_[2], nm[2];
#pragma unroll
    for (int h = 0; h < 2; ++h) {
      const float mu = red8(s1[h]) * (1.f / 576.f), var = fmaxf(red8(s2[h]) * (1.f / 576.f) - mu * mu, 0.f);
      rs_[h] = __builtin_amdgcn_rsqf(var + 1e-5f), nm[h] = -mu * rs_[h];
    }
    const int f = (lane & 3) | ((lane & 4) ? 12 : 0);
#pragma unroll
    for (int a = 0; a < 18; ++a) {
      v4u y;
#pragma unroll
      for (int e = 0; e < 4; ++e)
        y[e] = pk(__builtin_fmaf(lo2f(xr[a][e]), rs_[a & 1], nm[a & 1]), __builtin_fmaf(hi2f(xr[a][e]), rs_[a & 1], nm[a & 1]));
      *(LAS v4u *)(L + L_B1 + (8 * (a >> 1) + (lane & 7)) * 1024 + (16 * w + (((lane >> 3) + 8 * (a & 1)) ^ f)) * 16) = y;
    }
  }
  const u64 tag64 = (((u64)ep[1] << 32) | ep[0]) + 0x9E3779B97F4A7C15ull;
  const v2u tag = {(unsigned)tag64, (unsigned)(tag64 >> 32)};
  STAMP(2);
  int xna[2][4];
#pragma unroll
  for (int pr = 0; pr < 2; ++pr)
#pragma unroll
    for (int m = 0; m < 4; ++m) xna[pr][m] = L_B1 + (4 * pr + g) * 1024 + (16 * (m ^ w) + (t ^ (g | (pr ? 12 : 0)))) * 16;
  const int q4 = (lane >> 2) & 3, p4 = lane & 3;
  const int wrow = L_B2 + 288 * (16 * (g >> 1) + 4 * (g & 1) + q4);
  const int wmain = wrow + 2 * (18 * w + 2 * (w & 1)) + 8 * p4;
  const int wrem = wrow + ((p4 & 1) ? 104 : 32);
  f4 aK[4], aV[4], aR = {0, 0, 0, 0};
#pragma unroll
  for (int m = 0; m < 4; ++m) aK[m] = f4{0, 0, 0, 0}, aV[m] = f4{0, 0, 0, 0};
#pragma unroll
  for (int c = 0; c < 9; ++c) {
    if (c == 0) vmwait<15>(); else vmwait<10>();
    bar();
    if (c >= 1) {
      if (c + 3 < 9) fill(kvo, 64 * chunk(c + 3), (c + 3) & 3);
      else fill(qo, 128 * (c + 3 - 9), (c + 3) & 3);
    }
    const int xb = chunk(c) * 8192, sb = (c & 3) * 18432;
#pragma unroll
    for (int pr = 0; pr < 2; ++pr) {
      const v4u wk = trfrag(L, wmain + sb + pr * 9216, 2304), wv = trfrag(L, wmain + 144 + sb + pr * 9216, 2304);
      const v4u wr = trfrag(L, wrem + ((p4 & 2) ? 144 : 0) + sb + pr * 9216, 2304);
#pragma unroll
      for (int m = 0; m < 4; ++m) {
        const v4u X = lds128(L, xna[pr][m] + xb);
        if (m == 0) aR = mfma(X, wr, aR);
        aK[m] = mfma(X, wk, aK[m]);
        aV[m] = mfma(X, wv, aV[m]);
      }
    }
  }
  STAMP(3);
  {
    f4 r;
#pragma unroll
    for (int e = 0; e < 4; ++e) r[e] = t < 8 ? phi(aR[e]) : aR[e];
    *(LAS v2u *)(L + L_RX + w * 576 + (t * 4 + g) * 8) = pk4(r);
  }
  bar();
  fill(qo, 128 * 3, 0);
  {
    const int cA = t < 4 ? 2 * w + (t & 1) : t < 6 ? 8 + 2 * w + (t & 1) : t == 6 ? 16 : 17;
    const int cB = t < 2 ? 8 + 2 * w + t : (t & 3) == 3 ? 16 : 17;
    f4 T0 = {0, 0, 0, 0}, Y1 = T0, Y2 = T0, Y3 = T0;
#pragma unroll
    for (int P = 0; P < 2; ++P) {
      v2u k0, k1;
      f4 a, c;
#pragma unroll
      for (int e = 0; e < 4; ++e) a[e] = phi(aK[2 * P][e]), c[e] = phi(aK[2 * P + 1][e]);
      k0 = pk4(a), k1 = pk4(c);
      const v2u v0 = pk4(aV[2 * P]), v1 = pk4(aV[2 * P + 1]);
      const int m0 = ((2 * P) ^ w) * 576, m1 = ((2 * P + 1) ^ w) * 576;
      const v2u ra0 = lds64(L, L_RX + m0 + (cA * 4 + g) * 8), ra1 = lds64(L, L_RX + m1 + (cA * 4 + g) * 8);
      const v2u rb0 = lds64(L, L_RX + m0 + (cB * 4 + g) * 8), rb1 = lds64(L, L_RX + m1 + (cB * 4 + g) * 8);
      const v4u PK = {k0[0], k0[1], k1[0], k1[1]}, V = {v0[0], v0[1], v1[0], v1[1]};
      const v4u RA = {ra0[0], ra0[1], ra1[0], ra1[1]}, RB = {rb0[0], rb0[1], rb1[0], rb1[1]};
      T0 = mfma(PK, V, T0), Y1 = mfma(RA, V, Y1), Y2 = mfma(RA, RB, Y2), Y3 = mfma(RA, PK, Y3);
    }
    const v2u tt = pk4(T0);
    const unsigned x01 = g == 0 ? pk(Y1[0], Y1[1]) : pk(Y3[0], Y3[1]), x23 = g == 0 ? pk(Y2[2], Y2[3]) : pk(Y3[2], Y3[3]);
    const v4u sw = pairup(tt, tt);
    const v4u gr = {tt[0], tt[1], g == 2 ? sw[2] : x01, g == 2 ? sw[3] : x23};
    stc(gr, ws, g < 3 ? W_KVP + ((4 * j + w) * 32 + i) * 768 + lane * 16 : OOB, 0);
  }
  vmwait<0>();
  bar();
  if (tid == 0) setflag(ws, W_FKV + (j * 32 + i) * 8, tag);
  STAMP(4);
  f4 aQ[4], aQR = {0, 0, 0, 0};
#pragma unroll
  for (int m = 0; m < 4; ++m) aQ[m] = f4{0, 0, 0, 0};
#pragma unroll
  for (int c = 0; c < 5; ++c) {
    if (c == 1) {
      bar();
      if (w < 2) fill(qo, 128 * 4, 1);
    }
    if (c == 4) {
      vmwait<0>();
      bar();
    }
    const int sb = ((c + 1) & 3) * 18432;
#pragma unroll
    for (int s = 0; s < (c == 4 ? 2 : 4); ++s) {
      const int u = 4 * c + s, o = sb + (s >> 1) * 9216 + (s & 1) * 144;
      const v4u wq = trfrag(L, wmain + o, 2304), wr = trfrag(L, wrem + o, 2304);
#pragma unroll
      for (int m = 0; m < 4; ++m) {
        const v4u X = lds128(L, xna[u & 1][m] + (u >> 1) * 8192);
        if (m == 0) aQR = mfma(wr, X, aQR);
        aQ[m] = mfma(wq, X, aQ[m]);
      }
    }
  }
  if (g < 2) {
    *(LAS unsigned *)(L + L_QX + ((w * 4 + 2 * g) * 16 + t) * 4) = pk(phi(aQR[0]), phi(aQR[1]));
    *(LAS unsigned *)(L + L_QX + ((w * 4 + 2 * g + 1) * 16 + t) * 4) = pk(phi(aQR[2]), phi(aQR[3]));
  }
  bar();
  v4u PQ[4];
#pragma unroll
  for (int m = 0; m < 4; ++m)
    PQ[m] = v4u{pk(phi(aQ[m][0]), phi(aQ[m][1])), pk(phi(aQ[m][2]), phi(aQ[m][3])), *(LAS unsigned *)(L + L_QX + (((m ^ w) * 4 + w) * 16 + t) * 4), 0u};
  STAMP(5);
  waitall(ws, W_FKV, lane, tag, dead);
  if (b == 0 && tid == 0) setflag(ws, W_EPOCH, tag);
  STAMP(6);
  v4u Wm[18], We[18];
  const int wvm = W_PT + (4 * j + w) * 18432 + lane * 16;
  const int wve = t < 8 ? W_PT + (32 + (j >> 1)) * 18432 + (16 * g + 8 * (j & 1) + t) * 16 : OOB;
  const int pv0 = i * ACT_B + (g >> 1) * 1024 + (16 * ((g & 1) ^ w) + t) * 16, pv1 = i * ACT_B + (g >> 1) * 1024 + (16 * ((2 + (g & 1)) ^ w) + t) * 16;
  {
    v4u P[32];
    const int v = g < 3 ? W_KVP + (4 * j + w) * 32 * 768 + lane * 16 : OOB;
#pragma unroll
    for (int n = 0; n < 32; ++n) P[n] = ldc(ws, v, n * 768);
    __builtin_amdgcn_sched_barrier(0);
    const int k0 = t < 12 ? 8 * (t >> 2) + (t & 3) : 20 + (t & 3), k1 = t < 4 ? 4 + t : -1, k2 = t < 2 ? 12 + t : (t & 3) == 3 ? 14 : -1;
    v4u Z0, Z1, Z2;
#pragma unroll
    for (int e = 0; e < 4; ++e) {
      const int k = 8 * g + 2 * e;
      Z0[e] = k0 == k ? 0x3f80u : k0 == k + 1 ? 0x3f800000u : 0u;
      Z1[e] = k1 == k ? 0x3f80u : k1 == k + 1 ? 0x3f800000u : 0u;
      Z2[e] = k2 == k ? 0x3f80u : k2 == k + 1 ? 0x3f800000u : 0u;
    }
    f4 S0 = {0, 0, 0, 0}, S1 = S0, S2 = S0;
#pragma unroll
    for (int n = 0; n < 32; ++n) {
      S0 = mfma(Z0, P[n], S0), S1 = mfma(Z1, P[n], S1), S2 = mfma(P[n], Z2, S2);
      if (n == 8) load_w(ws, wvm, wve, Wm, We);
    }
    const v4u Am = {pk(S0[0], S0[1]), pk(S0[2], S0[3]), pk(S1[0], S1[1]), 0u}, Ar = {pk(S2[0], S2[1]), pk(S2[2], S2[3]), pk(S1[2], S1[3]), 0u};
    v2u o[4];
#pragma unroll
    for (int m = 0; m < 4; ++m) {
      const f4 om = mfma(Am, PQ[m], f4{0, 0, 0, 0}), orr = mfma(Ar, PQ[m], f4{0, 0, 0, 0});
      const float inv = __builtin_amdgcn_rcpf(orr[3] + 1e-6f);
      o[m] = pk4(om * inv);
      if (g == 0) *(LAS unsigned *)(L + L_OX + ((16 * (m ^ w) + t) * 4 + w) * 4) = pk(orr[0] * inv, orr[1] * inv);
    }
    stc(pairup(o[0], o[1]), ws, pv0, W_AO + (9 * j + 2 * w) * 1024);
    stc(pairup(o[2], o[3]), ws, pv1, W_AO + (9 * j + 2 * w) * 1024);
    bar();
    if (lane < 16) stc(lds128(L, L_OX + (16 * w + lane) * 16), ws, i * ACT_B + (16 * w + lane) * 16, W_AO + (9 * j + 8) * 1024);
  }
  vmwait<0>();
  bar();
  if (tid == 0) setflag(ws, W_FO + i * 128 + j * 8, tag);
  STAMP(7);
  {
    PtUnit pt[3];
#pragma unroll
    for (int r = 0; r < 3; ++r) {
      const int unit = 486 + r * 1024 + 4 * b + w;
      if (unit < 3078) pt[r] = pt_setup(p, unit), pt_load(pt[r], lane, L_B2 + (w * 3 + r) * 4096);
    }
    vmwait<0>();
#pragma unroll
    for (int r = 0; r < 3; ++r) {
      const int unit = 486 + r * 1024 + 4 * b + w;
      if (unit < 3078) pt_store(pt[r], L, ws, lane, L_B2 + (w * 3 + r) * 4096);
    }
  }
  STAMP(8);
  int xa[4];
#pragma unroll
  for (int m = 0; m < 4; ++m) xa[m] = L_B1 + 18 * g * 1024 + (16 * (m ^ w) + t) * 16;
  const int pvm0 = pv0 + (8 * j + 2 * w) * 1024, pvm1 = pv1 + (8 * j + 2 * w) * 1024;
  const int pve = g == 0 ? i * ACT_B + (64 + j) * 1024 + (16 * w + t) * 16 : OOB;
  const int gsrc = i * ACT_B + 18 * w * 1024 + lane * 16;
  f4 acc[4], accE, accG;
  v4u Um[18], Ue[18];
  v2u hb[4], hbe;
  waitflag(ws, W_FO + i * 128 + (lane & 7) * 8, true, tag, dead);
  STAMP(9);
  stage_gemm<false, 36>(L, ws, W_AO + gsrc, w, xa, Wm, We, acc, accE, accG, [&] { load_w(ws, wvm + PT_M, wve + PT_M, Um, Ue); });
#pragma unroll
  for (int m = 0; m < 4; ++m)
    hb[m] = v2u{pk(lo2f(xres[m][0]) + acc[m][0], hi2f(xres[m][0]) + acc[m][1]), pk(lo2f(xres[m][1]) + acc[m][2], hi2f(xres[m][1]) + acc[m][3])};
  hbe = v2u{pk(lo2f(xrese[0]) + accE[0], hi2f(xrese[0]) + accE[1]), pk(lo2f(xrese[1]) + accE[2], hi2f(xrese[1]) + accE[3])};
  publish(ws, W_AH, pvm0, pvm1, pve, hb, hbe);
  vmwait<0>();
  bar();
  if (tid == 0) setflag(ws, W_FH + i * 128 + j * 8, tag), setflag(ws, W_FPT + b * 8, tag);
  STAMP(10);
  f4 cs = {0, 0, 0, 0}, cse = cs;
  {
    const v4u ones = {0x3f803f80u, 0x3f803f80u, 0x3f803f80u, 0x3f803f80u};
    const unsigned om = t == 8 ? 0x3f803f80u : 0u;
#pragma unroll
    for (int u = 0; u < 18; ++u) {
      cs = mfma(Um[u], ones, cs), cse = mfma(Ue[u], ones, cse);
      Ue[u] |= om;
    }
  }
  waitflag(ws, W_FH + i * 128 + (lane & 7) * 8, true, tag, dead);
  STAMP(11);
  const int wve2 = t < 8 ? wve + 2 * PT_M : t < 10 ? W_RS + (2 * g + t - 8) * 16 : OOB;
  stage_gemm<true, 36>(L, ws, W_AH + gsrc, w, xa, Um, Ue, acc, accE, accG, [&] { load_w(ws, wvm + 2 * PT_M, wve2, Wm, We); });
  f4 hn[4], hne;
  {
    const float sum = __builtin_bit_cast(float, __builtin_amdgcn_ds_bpermute((32 + t) * 4, __builtin_bit_cast(int, accE[0])));
    const float sq = (t & 2) ? ((t & 1) ? accG[3] : accG[2]) : ((t & 1) ? accG[1] : accG[0]);
    const float mu = sum * (1.f / 576.f), var = fmaxf(sq * (1.f / 576.f) - mu * mu, 0.f);
    const float r1 = __builtin_amdgcn_rsqf(var + 1e-5f), r2 = __builtin_amdgcn_rsqf(var * r1 * r1 + 1e-5f);
    if (g == (t >> 2)) *(LAS f4 *)(L + L_ST + (16 * w + t) * 16) = f4{mu, r1, r1 * r2, 0.f};
    bar();
    v2u gb[4], gbe;
#pragma unroll
    for (int m = 0; m < 4; ++m) {
      const f4 st = *(LAS f4 *)(L + L_ST + (16 * (m ^ w) + t) * 16);
      f4 y;
#pragma unroll
      for (int e = 0; e < 4; ++e) y[e] = gelu(st[2] * __builtin_fmaf(-st[0], cs[e], acc[m][e]));
      gb[m] = pk4(y);
      hn[m] = (f4{lo2f(hb[m][0]), hi2f(hb[m][0]), lo2f(hb[m][1]), hi2f(hb[m][1])} - st[0]) * st[1];
      if (m == 0) {
#pragma unroll
        for (int e = 0; e < 4; ++e) y[e] = gelu(st[2] * __builtin_fmaf(-st[0], cse[e], accE[e]));
        gbe = pk4(y);
        hne = (f4{lo2f(hbe[0]), hi2f(hbe[0]), lo2f(hbe[1]), hi2f(hbe[1])} - st[0]) * st[1];
      }
    }
    publish(ws, W_AG, pvm0, pvm1, pve, gb, gbe);
  }
  vmwait<0>();
  bar();
  if (tid == 0) setflag(ws, W_FG + i * 128 + j * 8, tag);
  STAMP(12);
  {
    const int ib = i - 1 + (lane >> 3);
    waitflag(ws, W_FG + ib * 128 + (lane & 7) * 8, lane < 24 && ib >= 0 && ib < 32, tag, dead);
  }
  STAMP(13);
  f4 res[4], rese;
  {
    const int tok = 64 * i + lane + ((w & 1) ? 1 : -1) * (1 + (w >> 1));
    const int src = (tok >= 0 && tok < 2048) ? W_AG + (tok >> 6) * ACT_B + 18 * w * 1024 + (tok & 63) * 16 : OOB;
    stage_gemm<false, 0>(L, ws, src, w, xa, Wm, We, acc, accE, accG, [&] {});
    if (g == 2) *(LAS float *)(L + L_ST + (16 * w + t) * 4) = accE[0] + accE[1];
    bar();
    v2u xb[4], xbe;
#pragma unroll
    for (int m = 0; m < 4; ++m) {
      const float mu = *(LAS float *)(L + L_ST + (16 * (m ^ w) + t) * 4);
      const f4 x1 = hn[m] + acc[m];
      xb[m] = pk4(x1 - mu);
      res[m] = x1 + f4{lo2f(hb[m][0]), hi2f(hb[m][0]), lo2f(hb[m][1]), hi2f(hb[m][1])};
      if (m == 0) {
        const f4 x1e = hne + accE;
        xbe = pk4(x1e - mu);
        rese = x1e + f4{lo2f(hbe[0]), hi2f(hbe[0]), lo2f(hbe[1]), hi2f(hbe[1])};
      }
    }
    publish(ws, W_AX, pvm0, pvm1, pve, xb, xbe);
  }
  vmwait<0>();
  bar();
  if (tid == 0) setflag(ws, W_FX + i * 128 + j * 8, tag);
  STAMP(14);
  waitall(ws, W_FPT, lane, tag, dead);
  waitflag(ws, W_FX + i * 128 + (lane & 7) * 8, true, tag, dead);
  STAMP(15);
#pragma unroll
  for (int u = 0; u < 18; ++u) dmac(ws, W_AX + gsrc, u * 1024, L_B1 + (18 * w + u) * 1024);
  vmwait<0>();
  bar();
  float r3[4];
  {
    accG = f4{0, 0, 0, 0};
#pragma unroll
    for (int u = 0; u < 18; ++u) {
      const v4u X = lds128(L, xa[0] + u * 1024);
      accG = mfma(X, X, accG);
    }
    const float sq = (t & 2) ? ((t & 1) ? accG[3] : accG[2]) : ((t & 1) ? accG[1] : accG[0]);
    if (g == (t >> 2)) *(LAS float *)(L + L_ST + 512 + (16 * w + t) * 4) = __builtin_amdgcn_rsqf(sq * (1.f / 576.f) + 1e-5f);
    bar();
#pragma unroll
    for (int m = 0; m < 4; ++m) r3[m] = *(LAS float *)(L + L_ST + 512 + (16 * (m ^ w) + t) * 4);
  }
#pragma unroll 1
  for (int tt = 0; tt < 9; ++tt) {
#pragma unroll
    for (int u = 0; u < 18; ++u) Wm[u] = ldc(ws, W_PT + (3 + j) * PT_M + (9 * w + tt) * 18432 + lane * 16, u * 1024);
#pragma unroll
    for (int m = 0; m < 4; ++m) acc[m] = f4{0, 0, 0, 0};
#pragma unroll
    for (int u = 0; u < 18; ++u)
#pragma unroll
      for (int m = 0; m < 4; ++m) acc[m] = mfma(Wm[u], lds128(L, xa[m] + u * 1024), acc[m]);
#pragma unroll
    for (int m = 0; m < 4; ++m) {
      f4 y;
#pragma unroll
      for (int e = 0; e < 4; ++e) y[e] = gelu(acc[m][e] * r3[m]);
      *(LAS v2u *)(L + L_B2 + (18 * w + 2 * tt + (g >> 1)) * 1024 + (16 * (m ^ w) + t) * 16 + 8 * (g & 1)) = pk4(y);
    }
  }
  bar();
  STAMP(16);
#pragma unroll 1
  for (int s = 0; s < 9; ++s) {
    const int T = s == 0 ? 32 + w : 4 * ((j + s) & 7) + w;
#pragma unroll
    for (int u = 0; u < 18; ++u) Wm[u] = ldc(ws, W_PT + (11 + j) * PT_M + T * 18432 + lane * 16, u * 1024);
#pragma unroll
    for (int m = 0; m < 4; ++m) acc[m] = f4{0, 0, 0, 0};
#pragma unroll
    for (int u = 0; u < 18; ++u)
#pragma unroll
      for (int m = 0; m < 4; ++m) acc[m] = mfma(Wm[u], lds128(L, xa[m] + ACT_B + u * 1024), acc[m]);
    if (s < 8) {
      const int d = W_PART + ((i * 8 + j) * 36 + T) * 2048 + lane * 16;
      const v2u a0 = pk4(acc[0]), a1 = pk4(acc[1]), a2 = pk4(acc[2]), a3 = pk4(acc[3]);
      stc(v4u{a0[0], a0[1], a1[0], a1[1]}, ws, d, 0);
      stc(v4u{a2[0], a2[1], a3[0], a3[1]}, ws, d, 1024);
      vmwait<0>();
      if (lane == 0) setflag(ws, W_FP + (i * 36 + T) * 128 + j * 8, tag);
    }
  }
  STAMP(17);
  {
    const int Tm = 4 * j + w, Te = 32 + (j >> 1);
    waitflag(ws, W_FP + (i * 36 + ((lane & 8) ? Te : Tm)) * 128 + (lane & 7) * 8, lane < 16 && lane != j, tag, dead);
    STAMP(18);
#pragma unroll
    for (int n = 1; n < 8; ++n) {
      const int d = W_PART + ((i * 8 + ((j + n) & 7)) * 36 + Tm) * 2048 + lane * 16;
      const v4u a = ldc(ws, d, 0), c = ldc(ws, d, 1024);
      acc[0] += f4{lo2f(a[0]), hi2f(a[0]), lo2f(a[1]), hi2f(a[1])};
      acc[1] += f4{lo2f(a[2]), hi2f(a[2]), lo2f(a[3]), hi2f(a[3])};
      acc[2] += f4{lo2f(c[0]), hi2f(c[0]), lo2f(c[1]), hi2f(c[1])};
      acc[3] += f4{lo2f(c[2]), hi2f(c[2]), lo2f(c[3]), hi2f(c[3])};
    }
    const int mp = w ^ (j >> 1);
#pragma unroll
    for (int n = 0; n < 8; ++n) {
      const v2u a = __builtin_amdgcn_raw_buffer_load_b64(
          ws, W_PART + ((i * 8 + n) * 36 + Te) * 2048 + (mp >> 1) * 1024 + (16 * (2 * (j & 1) + (g & 1)) + t) * 16 + (mp & 1) * 8, 0, SC1);
      rese += f4{lo2f(a[0]), hi2f(a[0]), lo2f(a[1]), hi2f(a[1])};
    }
    v2u ob[4];
#pragma unroll
    for (int m = 0; m < 4; ++m) ob[m] = pk4(res[m] + acc[m]);
    const v2u oe = pk4(rese);
    const int oc = (64 * j + 16 * w + 8 * (g >> 1)) * 2;
    __builtin_amdgcn_raw_buffer_store_b128(pairup(ob[0], ob[1]), ro, (64 * i + 16 * ((g & 1) ^ w) + t) * 1152 + oc, 0, 0);
    __builtin_amdgcn_raw_buffer_store_b128(pairup(ob[2], ob[3]), ro, (64 * i + 16 * ((2 + (g & 1)) ^ w) + t) * 1152 + oc, 0, 0);
    __builtin_amdgcn_raw_buffer_store_b128(pairup(oe, oe), ro, g == 0 ? (64 * i + 16 * w + t) * 1152 + (512 + 8 * j) * 2 : OOB, 0, 0);
  }
  STAMP(19);
}
}

extern "C" void kernel_launch(void *const *d_in, const int *in_sizes, int n_in, void *d_out, int out_size, void *d_ws, size_t ws_size,
                              hipStream_t stream) {
  mk::Params p;
  p.x = d_in[0], p.wqkv = d_in[1], p.wo = d_in[2], p.w1 = d_in[4], p.w2 = d_in[5], p.w3 = d_in[6], p.w4 = d_in[7];
  p.out = d_out, p.ws = d_ws;
  (void)hipFuncSetAttribute((const void *)mk::fused, hipFuncAttributeMaxDynamicSharedMemorySize, 163840);
  mk::fused<<<256, 256, 163840, stream>>>(p);
}
```

```cpp
#include <hip/hip_runtime.h>

namespace mk {
constexpr int W_AO = 1353728, W_AH = 3713024, W_AG = 6072320, W_AX = 8431616, W_DBG = 42272768;
struct Params {
  const void *x, *wqkv, *wo, *w1, *w2, *w3, *w4;
  void *out, *ws;
};

__global__ void __launch_bounds__(256, 1) fused(Params p) {
  unsigned tid = threadIdx.x, bid = blockIdx.x;
  auto ka = __builtin_amdgcn_kernarg_segment_ptr();
  asm volatile(
      "s_load_dwordx16 s[20:35], s[0:1], 0\n"
      "s_load_dwordx2 s[36:37], s[0:1], 64\n"
      "v_mov_b32 v5, 1073741824\n"
      "s_getpc_b64 s[60:61]\n"
      ".LCODE0_%=:\n"
      "s_and_b32 s61, s61, 0xffff\n"
      "s_mov_b32 s62, .LCODE1_%=-.LCODE0_%=\n"
      "s_mov_b32 s63, 0x20000\n"
      "s_lshr_b32 s46, s2, 3\n"
      "s_mul_i32 s47, s46, 1536\n"
      "v_cmp_gt_u32 vcc, 12, v0\n"
      "v_lshlrev_b32 v16, 7, v0\n"
      "v_lshrrev_b32 v12, 6, v0\n"
      "v_cndmask_b32 v16, v5, v16, vcc\n"
      "v_add_u32 v9, s47, v16\n"
      "buffer_load_dword v8, v9, s[60:63], 0 offen\n"
      "v_readfirstlane_b32 s40, v12\n"
      "v_and_b32 v4, 63, v0\n"
      "s_and_b32 s45, s2, 7\n"
      "s_lshr_b32 s38, s45, 1\n"
      "s_lshl_b32 s38, s38, 3\n"
      "s_lshr_b32 s47, s46, 2\n"
      "s_add_u32 s38, s38, s47\n"
      "s_and_b32 s39, s45, 1\n"
      "s_lshl_b32 s39, s39, 2\n"
      "s_and_b32 s47, s46, 3\n"
      "s_add_u32 s39, s39, s47\n"
      "s_lshl_b32 s45, s39, 1\n"
      "s_add_u32 s45, s45, s38\n"
      "s_mul_i32 s46, s45, 57\n"
      "s_lshr_b32 s46, s46, 9\n"
      "s_mul_i32 s46, s46, 9\n"
      "s_sub_u32 s45, s45, s46\n"
      "s_lshl_b32 s53, s45, 7\n"
      "s_lshl_b32 s45, s38, 6\n"
      "s_lshl_b32 s46, s40, 4\n"
      "s_add_u32 s45, s45, s46\n"
      "s_lshl_b32 s72, s40, 11\n"
      "v_lshrrev_b32 v12, 3, v4\n"
      "v_and_b32 v13, 6, v12\n"
      "v_xor_b32 v13, v13, v4\n"
      "v_and_b32 v13, 7, v13\n"
      "v_add_u32 v14, s45, v12\n"
      "v_mul_u32_u24 v14, 0x480, v14\n"
      "v_lshl_add_u32 v14, v13, 4, v14\n"
      "v_add_u32 v15, 8192, v14\n"
      "v_lshlrev_b32 v1, 4, v4\n"
      "v_and_b32 v2, 15, v4\n"
      "v_lshrrev_b32 v3, 4, v4\n"
      "v_cmp_gt_u32 vcc, 16, v4\n"
      "v_lshlrev_b32 v16, 7, v4\n"
      "s_lshl_b32 s45, s2, 13\n"
      "v_cndmask_b32 v16, v5, v16, vcc\n"
      "s_add_u32 s45, s45, s72\n"
      "v_cmp_eq_u32 vcc, 0, v0\n"
      "v_add_u32 v23, s45, v16\n"
      "s_mov_b32 s41, 20000\n"
      "v_cndmask_b32 v5, v5, v0, vcc\n"
      "v_cmp_gt_u32 vcc, 16, v0\n"
      "v_lshlrev_b32 v10, 3, v0\n"
      "s_nop 0\n"
      "v_cndmask_b32 v10, v5, v10, vcc\n"
      "s_add_u32 s64, s53, 128\n"
      "s_cmp_eq_u32 s64, 1152\n"
      "s_cselect_b32 s64, 0, s64\n"
      "s_add_u32 s65, s64, 128\n"
      "s_cmp_eq_u32 s65, 1152\n"
      "s_cselect_b32 s65, 0, s65\n"
      "s_add_u32 s66, s65, 128\n"
      "s_cmp_eq_u32 s66, 1152\n"
      "s_cselect_b32 s66, 0, s66\n"
      "s_add_u32 s67, s66, 128\n"
      "s_cmp_eq_u32 s67, 1152\n"
      "s_cselect_b32 s67, 0, s67\n"
      "s_add_u32 s68, s67, 128\n"
      "s_cmp_eq_u32 s68, 1152\n"
      "s_cselect_b32 s68, 0, s68\n"
      "s_add_u32 s69, s68, 128\n"
      "s_cmp_eq_u32 s69, 1152\n"
      "s_cselect_b32 s69, 0, s69\n"
      "s_add_u32 s70, s69, 128\n"
      "s_cmp_eq_u32 s70, 1152\n"
      "s_cselect_b32 s70, 0, s70\n"
      "s_add_u32 s71, s70, 128\n"
      "s_cmp_eq_u32 s71, 1152\n"
      "s_cselect_b32 s71, 0, s71\n"
      "s_mul_i32 s78, s40, 5120\n"
      "s_add_u32 s78, s78, 73728\n"
      "s_add_u32 s77, s78, 61440\n"
      "s_lshl_b32 s45, s2, 1\n"
      "s_add_u32 s45, s45, s40\n"
      "s_cmp_lt_u32 s40, 2\n"
      "s_cselect_b32 s45, s45, 486\n"
      "s_mul_i32 s46, s45, 6473\n"
      "s_lshr_b32 s46, s46, 20\n"
      "s_mul_i32 s47, s46, 162\n"
      "s_sub_u32 s47, s45, s47\n"
      "s_mul_i32 s48, s47, 57\n"
      "s_lshr_b32 s48, s48, 9\n"
      "s_mul_i32 s49, s48, 9\n"
      "s_sub_u32 s49, s47, s49\n"
      "s_mul_i32 s76, s46, 663552\n"
      "s_mul_i32 s50, s49, 72\n"
      "s_add_u32 s50, s50, s48\n"
      "s_lshl_b32 s50, s50, 10\n"
      "s_add_u32 s76, s76, s50\n"
      "s_lshl_b32 s74, s48, 3\n"
      "s_lshl_b32 s49, s49, 7\n"
      "s_add_u32 s75, s74, 4\n"
      "s_cmp_ge_u32 s48, 9\n"
      "s_cselect_b32 s50, 72, 0\n"
      "s_cselect_b32 s51, 9, 0\n"
      "s_sub_u32 s48, s48, s51\n"
      "s_lshr_b32 s51, s48, 1\n"
      "s_mul_i32 s47, s51, 18\n"
      "s_and_b32 s51, s48, 2\n"
      "s_add_u32 s47, s47, s51\n"
      "s_and_b32 s51, s48, 1\n"
      "s_lshl_b32 s51, s51, 3\n"
      "s_add_u32 s47, s47, s51\n"
      "s_add_u32 s51, s47, 4\n"
      "s_cmp_eq_u32 s48, 8\n"
      "s_cselect_b32 s47, 16, s47\n"
      "s_cselect_b32 s51, 52, s51\n"
      "s_add_u32 s47, s47, s50\n"
      "s_add_u32 s51, s51, s50\n"
      "s_cmp_eq_u32 s46, 0\n"
      "s_cselect_b32 s74, s47, s74\n"
      "s_cselect_b32 s75, s51, s75\n"
      "s_mov_b32 s73, 1152\n"
      "s_mov_b32 s18, 663552\n"
      "s_mov_b32 s19, 0x20000\n"
      "s_mul_i32 s74, s74, s73\n"
      "s_mul_i32 s75, s75, s73\n"
      "s_add_u32 s74, s74, s49\n"
      "s_add_u32 s75, s75, s49\n"
      "s_cmp_lt_u32 s45, 486\n"
      "s_cselect_b32 s18, s18, 0\n"
      "s_cselect_b32 s76, s76, 1073741824\n"
      "s_cselect_b32 s74, s74, 0\n"
      "s_cselect_b32 s75, s75, 0\n"
      "v_bfe_u32 v26, v4, 3, 1\n"
      "v_mul_u32_u24 v26, 144, v26\n"
      "v_add_u32 v26, v26, v3\n"
      "v_mul_lo_u32 v11, v26, s73\n"
      "v_and_b32 v26, 7, v4\n"
      "v_lshlrev_b32 v27, 1, v3\n"
      "v_xor_b32 v26, v26, v27\n"
      "v_lshl_add_u32 v11, v26, 4, v11\n"
      "s_mul_i32 s73, s73, 288\n"
      "s_lshl_b32 s79, s2, 1\n"
      "s_add_u32 s79, s79, s40\n"
      "s_sub_u32 s79, s79, 2\n"
      "s_cmp_lt_u32 s40, 2\n"
      "s_cselect_b32 s79, 384, s79\n"
      "s_cmp_lt_u32 s79, 384\n"
      "s_cbranch_scc0 .LRS1_%=\n"
      "s_sub_u32 s47, s79, 192\n"
      "s_cselect_b32 s47, s79, s47\n"
      "s_cselect_b32 s46, 0, 2\n"
      "s_mul_i32 s74, s47, 3456\n"
      "s_add_u32 s75, s74, 1024\n"
      "s_mov_b32 s73, 2048\n"
      "s_mov_b32 s18, 663552\n"
      "v_mov_b32 v11, v1\n"
      ".LRS1_%=:\n"
      "s_waitcnt lgkmcnt(0)\n"
      "s_mov_b32 s8, s20\n"
      "s_and_b32 s9, s21, 0xffff\n"
      "s_mov_b32 s10, 2359296\n"
      "s_mov_b32 s11, 0x20000\n"
      "s_add_u32 m0, s72, 0\n"
      "s_nop 0\n"
      "buffer_load_dwordx4 v14, s[8:11], s53 offen lds\n"
      "buffer_load_dwordx4 v15, s[8:11], s53 offen offset:1024 lds\n"
      "s_mov_b32 s12, s22\n"
      "s_and_b32 s13, s23, 0xffff\n"
      "s_mov_b32 s14, 1990656\n"
      "s_mov_b32 s15, 0x20000\n"
      "buffer_load_dword v8, v23, s[12:15], 0 offen\n"
      "s_cmp_eq_u32 s46, 0\n"
      "s_cselect_b64 s[16:17], s[24:25], s[26:27]\n"
      "s_cmp_eq_u32 s46, 2\n"
      "s_cselect_b64 s[16:17], s[28:29], s[16:17]\n"
      "s_and_b32 s17, s17, 0xffff\n"
      "s_add_u32 m0, s72, 8192\n"
      "s_nop 0\n"
      "buffer_load_dwordx4 v14, s[8:11], s64 offen lds\n"
      "v_mov_b32 v92, 1065369472\n"
      "v_mov_b32 v93, 1065369472\n"
      "v_mov_b32 v94, 1065369472\n"
      "v_mov_b32 v95, 1065369472\n"
      "v_and_b32 v12, 6, v2\n"
      "v_xor_b32 v12, v12, v3\n"
      "v_lshlrev_b32 v12, 4, v12\n"
      "v_lshl_add_u32 v12, v2, 7, v12\n"
      "s_xor_b32 s45, s72, 0\n"
      "v_add_u32 v76, s45, v12\n"
      "v_xor_b32 v80, 64, v76\n"
      "v_add_u32 v84, 0x8000, v76\n"
      "v_add_u32 v88, 0x8000, v80\n"
      "s_xor_b32 s45, s72, 2048\n"
      "buffer_load_dwordx4 v15, s[8:11], s64 offen offset:1024 lds\n"
      "v_add_u32 v77, s45, v12\n"
      "v_xor_b32 v81, 64, v77\n"
      "v_add_u32 v85, 0x8000, v77\n"
      "v_add_u32 v89, 0x8000, v81\n"
      "s_xor_b32 s45, s72, 4096\n"
      "v_add_u32 v78, s45, v12\n"
      "v_xor_b32 v82, 64, v78\n"
      "v_add_u32 v86, 0x8000, v78\n"
      "v_add_u32 v90, 0x8000, v82\n"
      "s_xor_b32 s45, s72, 6144\n"
      "v_add_u32 v79, s45, v12\n"
      "v_xor_b32 v83, 64, v79\n"
      "v_add_u32 v87, 0x8000, v79\n"
      "v_add_u32 v91, 0x8000, v83\n"
      "v_and_b32 v12, 1, v3\n"
      "v_bfe_u32 v13, v4, 2, 2\n"
      "s_mov_b32 m0, s77\n"
      "v_add_u32 v28, s76, v1\n"
      "buffer_load_dwordx4 v11, s[16:19], s74 offen lds\n"
      "v_lshl_add_u32 v12, v12, 2, v13\n"
      "v_mul_u32_u24 v12, 288, v12\n"
      "v_lshrrev_b32 v13, 1, v3\n"
      "v_mul_u32_u24 v13, 5120, v13\n"
      "v_add_u32 v12, v12, v13\n"
      "v_add_u32 v12, 73728, v12\n"
      "v_and_b32 v13, 3, v4\n"
      "s_mul_i32 s45, s40, 36\n"
      "s_and_b32 s46, s40, 1\n"
      "s_lshl_b32 s46, s46, 2\n"
      "s_add_u32 s45, s45, s46\n"
      "v_lshl_add_u32 v29, v13, 3, v12\n"
      "v_add_u32 v29, s45, v29\n"
      "v_and_b32 v13, 1, v13\n"
      "s_add_u32 m0, s77, 1024\n"
      "s_add_u32 s74, s74, s73\n"
      "buffer_load_dwordx4 v11, s[16:19], s75 offen lds\n"
      "v_mul_u32_u24 v16, 72, v13\n"
      "v_add3_u32 v30, v12, v16, 32\n"
      "v_add_u32 v31, 40960, v29\n"
      "v_add_u32 v73, 40960, v30\n"
      "v_and_b32 v12, 1, v2\n"
      "v_cmp_eq_u32 s[54:55], 1, v12\n"
      "v_and_b32 v12, 2, v2\n"
      "v_cmp_eq_u32 s[56:57], 2, v12\n"
      "v_bfe_u32 v25, v4, 2, 2\n"
      "v_lshrrev_b32 v26, 1, v3\n"
      "v_lshl_add_u32 v26, v26, 3, v25\n"
      "v_and_b32 v27, 1, v3\n"
      "v_lshl_add_u32 v26, v26, 1, v27\n"
      "s_add_u32 m0, s77, 2048\n"
      "s_add_u32 s75, s75, s73\n"
      "buffer_load_dwordx4 v11, s[16:19], s74 offen lds\n"
      "v_and_b32 v27, 3, v4\n"
      "v_lshlrev_b32 v27, 3, v27\n"
      "v_lshl_add_u32 v26, v26, 7, v27\n"
      "v_add_u32 v26, s77, v26\n"
      "v_xor_b32 v27, 0, v25\n"
      "v_lshl_add_u32 v32, v27, 5, v26\n"
      "v_xor_b32 v27, 1, v25\n"
      "v_lshl_add_u32 v33, v27, 5, v26\n"
      "v_xor_b32 v27, 2, v25\n"
      "v_lshl_add_u32 v34, v27, 5, v26\n"
      "v_xor_b32 v27, 3, v25\n"
      "v_lshl_add_u32 v35, v27, 5, v26\n"
      "s_mul_i32 s46, s39, 144\n"
      "s_and_b32 s47, s40, 1\n"
      "s_mul_i32 s47, s47, 55296\n"
      "s_add_u32 m0, s77, 3072\n"
      "s_nop 0\n"
      "buffer_load_dwordx4 v11, s[16:19], s75 offen lds\n"
      "s_add_u32 s46, s46, s47\n"
      "v_mov_b32 v22, 110448\n"
      "v_add_u32 v16, 0, v4\n"
      "v_mul_u32_u24 v17, 0xe39, v16\n"
      "v_lshrrev_b32 v17, 16, v17\n"
      "v_mul_u32_u24 v18, 18, v17\n"
      "v_sub_u32 v18, v16, v18\n"
      "v_cmp_lt_u32 vcc, 8, v18\n"
      "v_and_b32 v19, 3, v17\n"
      "v_and_b32 v16, 4, v17\n"
      "v_lshl_or_b32 v19, v16, 1, v19\n"
      "v_bfe_u32 v16, v17, 3, 1\n"
      "v_lshl_or_b32 v19, v16, 2, v19\n"
      "v_mul_u32_u24 v19, 0xd80, v19\n"
      "s_mov_b32 s4, s36\n"
      "s_and_b32 s5, s37, 0xffff\n"
      "s_mov_b32 s6, 42272768\n"
      "s_mov_b32 s7, 0x20000\n"
      "buffer_load_dwordx2 v[6:7], off, s[4:7], 0 sc1\n"
      "v_lshl_add_u32 v19, v18, 4, v19\n"
      "v_cndmask_b32 v16, 0, v22, vcc\n"
      "v_add3_u32 v68, v19, v16, s46\n"
      "v_add_u32 v16, 64, v4\n"
      "v_mul_u32_u24 v17, 0xe39, v16\n"
      "v_lshrrev_b32 v17, 16, v17\n"
      "v_mul_u32_u24 v18, 18, v17\n"
      "v_sub_u32 v18, v16, v18\n"
      "v_cmp_lt_u32 vcc, 8, v18\n"
      "v_and_b32 v19, 3, v17\n"
      "v_and_b32 v16, 4, v17\n"
      "v_lshl_or_b32 v19, v16, 1, v19\n"
      "s_add_u32 m0, s72, 16384\n"
      "s_nop 0\n"
      "buffer_load_dwordx4 v14, s[8:11], s65 offen lds\n"
      "v_bfe_u32 v16, v17, 3, 1\n"
      "v_lshl_or_b32 v19, v16, 2, v19\n"
      "v_mul_u32_u24 v19, 0xd80, v19\n"
      "v_lshl_add_u32 v19, v18, 4, v19\n"
      "v_cndmask_b32 v16, 0, v22, vcc\n"
      "v_add3_u32 v69, v19, v16, s46\n"
      "v_add_u32 v16, 128, v4\n"
      "v_mul_u32_u24 v17, 0xe39, v16\n"
      "v_lshrrev_b32 v17, 16, v17\n"
      "v_mul_u32_u24 v18, 18, v17\n"
      "v_sub_u32 v18, v16, v18\n"
      "v_cmp_lt_u32 vcc, 8, v18\n"
      "v_and_b32 v19, 3, v17\n"
      "buffer_load_dwordx4 v15, s[8:11], s65 offen offset:1024 lds\n"
      "v_and_b32 v16, 4, v17\n"
      "v_lshl_or_b32 v19, v16, 1, v19\n"
      "v_bfe_u32 v16, v17, 3, 1\n"
      "v_lshl_or_b32 v19, v16, 2, v19\n"
      "v_mul_u32_u24 v19, 0xd80, v19\n"
      "v_lshl_add_u32 v19, v18, 4, v19\n"
      "v_cndmask_b32 v16, 0, v22, vcc\n"
      "v_add3_u32 v70, v19, v16, s46\n"
      "v_add_u32 v16, 192, v4\n"
      "v_mul_u32_u24 v17, 0xe39, v16\n"
      "v_lshrrev_b32 v17, 16, v17\n"
      "v_mul_u32_u24 v18, 18, v17\n"
      "v_sub_u32 v18, v16, v18\n"
      "v_cmp_lt_u32 vcc, 8, v18\n"
      "v_and_b32 v19, 3, v17\n"
      "s_add_u32 m0, s72, 24576\n"
      "s_nop 0\n"
      "buffer_load_dwordx4 v14, s[8:11], s66 offen lds\n"
      "v_and_b32 v16, 4, v17\n"
      "v_lshl_or_b32 v19, v16, 1, v19\n"
      "v_bfe_u32 v16, v17, 3, 1\n"
      "v_lshl_or_b32 v19, v16, 2, v19\n"
      "v_mul_u32_u24 v19, 0xd80, v19\n"
      "v_lshl_add_u32 v19, v18, 4, v19\n"
      "v_cndmask_b32 v16, 0, v22, vcc\n"
      "v_add3_u32 v71, v19, v16, s46\n"
      "v_add_u32 v16, 256, v4\n"
      "v_mul_u32_u24 v17, 0xe39, v16\n"
      "v_lshrrev_b32 v17, 16, v17\n"
      "v_mul_u32_u24 v18, 18, v17\n"
      "v_sub_u32 v18, v16, v18\n"
      "buffer_load_dwordx4 v15, s[8:11], s66 offen offset:1024 lds\n"
      "v_cmp_lt_u32 vcc, 8, v18\n"
      "v_and_b32 v19, 3, v17\n"
      "v_and_b32 v16, 4, v17\n"
      "v_lshl_or_b32 v19, v16, 1, v19\n"
      "v_bfe_u32 v16, v17, 3, 1\n"
      "v_lshl_or_b32 v19, v16, 2, v19\n"
      "v_mul_u32_u24 v19, 0xd80, v19\n"
      "v_lshl_add_u32 v19, v18, 4, v19\n"
      "v_cndmask_b32 v16, 0, v22, vcc\n"
      "v_add3_u32 v72, v19, v16, s46\n"
      "v_subrev_u32 v69, 1024, v69\n"
      "v_subrev_u32 v70, 2048, v70\n"
      "v_subrev_u32 v71, 3072, v71\n"
      "v_cmp_gt_u32 vcc, 32, v4\n"
      "v_mov_b32 v16, 1073741824\n"
      "s_add_u32 m0, s72, 32768\n"
      "s_nop 0\n"
      "buffer_load_dwordx4 v14, s[8:11], s67 offen lds\n"
      "v_cndmask_b32 v72, v16, v72, vcc\n"
      "s_cmp_ge_u32 s40, 2\n"
      "s_cselect_b32 s80, s64, s53\n"
      "s_mul_i32 s80, s80, 1728\n"
      "s_add_u32 s80, s80, 1152\n"
      "s_cmp_ge_u32 s40, 2\n"
      "s_cselect_b32 s81, s66, s65\n"
      "s_mul_i32 s81, s81, 1728\n"
      "s_add_u32 s81, s81, 1152\n"
      "s_cmp_ge_u32 s40, 2\n"
      "s_cselect_b32 s82, s68, s67\n"
      "s_mul_i32 s82, s82, 1728\n"
      "s_add_u32 s82, s82, 1152\n"
      "s_cmp_ge_u32 s40, 2\n"
      "s_cselect_b32 s83, s70, s69\n"
      "s_mul_i32 s83, s83, 1728\n"
      "s_add_u32 s83, s83, 1152\n"
      "s_cmp_ge_u32 s40, 2\n"
      "s_cselect_b32 s84, s53, s71\n"
      "s_cselect_b32 s48, 1152, 0\n"
      "s_mul_i32 s84, s84, 1728\n"
      "s_add_u32 s84, s84, 1152\n"
      "s_add_u32 s84, s84, s48\n"
      "s_cmp_ge_u32 s40, 2\n"
      "s_cselect_b32 s85, s65, s64\n"
      "buffer_load_dwordx4 v15, s[8:11], s67 offen offset:1024 lds\n"
      "s_mul_i32 s85, s85, 1728\n"
      "s_add_u32 s85, s85, 2304\n"
      "s_cmp_ge_u32 s40, 2\n"
      "s_cselect_b32 s86, s67, s66\n"
      "s_mul_i32 s86, s86, 1728\n"
      "s_add_u32 s86, s86, 2304\n"
      "s_cmp_ge_u32 s40, 2\n"
      "s_cselect_b32 s87, s69, s68\n"
      "s_mul_i32 s87, s87, 1728\n"
      "s_add_u32 s87, s87, 2304\n"
      "s_cmp_ge_u32 s40, 2\n"
      "s_cselect_b32 s88, s71, s70\n"
      "s_mul_i32 s88, s88, 1728\n"
      "s_add_u32 s88, s88, 2304\n"
      "s_cmp_ge_u32 s40, 2\n"
      "s_cselect_b32 s89, s64, s53\n"
      "s_mul_i32 s89, s89, 1728\n"
      "s_cmp_ge_u32 s40, 2\n"
      "s_cselect_b32 s90, s66, s65\n"
      "s_mul_i32 s90, s90, 1728\n"
      "s_cmp_ge_u32 s40, 2\n"
      "s_cselect_b32 s91, s68, s67\n"
      "s_mul_i32 s91, s91, 1728\n"
      "s_cmp_ge_u32 s40, 2\n"
      "s_cselect_b32 s92, s70, s69\n"
      "s_mul_i32 s92, s92, 1728\n"
      "s_cmp_ge_u32 s40, 2\n"
      "s_cselect_b32 s93, s71, s71\n"
      "s_mul_i32 s93, s93, 1728\n"
      "s_add_u32 m0, s72, 40960\n"
      "s_nop 0\n"
      "buffer_load_dwordx4 v14, s[8:11], s68 offen lds\n"
      "buffer_load_dwordx4 v15, s[8:11], s68 offen offset:1024 lds\n"
      "s_add_u32 m0, s72, 49152\n"
      "s_nop 0\n"
      "buffer_load_dwordx4 v14, s[8:11], s69 offen lds\n"
      "buffer_load_dwordx4 v15, s[8:11], s69 offen offset:1024 lds\n"
      "s_waitcnt vmcnt(11)\n"
      "s_cmp_lt_u32 s79, 384\n"
      "s_cbranch_scc0 .LRS2_%=\n"
      "v_min_u32 v17, 2, v3\n"
      "v_mul_u32_u24 v16, 0x480, v17\n"
      "v_lshl_add_u32 v16, v2, 3, v16\n"
      "v_add_u32 v16, s77, v16\n"
      "ds_read_b64 v[36:37], v16 offset:0\n"
      "ds_read_b64 v[38:39], v16 offset:128\n"
      "ds_read_b64 v[40:41], v16 offset:256\n"
      "ds_read_b64 v[42:43], v16 offset:384\n"
      "ds_read_b64 v[44:45], v16 offset:512\n"
      "ds_read_b64 v[46:47], v16 offset:640\n"
      "ds_read_b64 v[48:49], v16 offset:768\n"
      "ds_read_b64 v[50:51], v16 offset:896\n"
      "ds_read_b64 v[52:53], v16 offset:1024\n"
      "s_sub_u32 s47, s79, 192\n"
      "s_cselect_b32 s47, s79, s47\n"
      "s_cselect_b32 s48, 64, 0\n"
      "s_cselect_b64 s[58:59], -1, 0\n"
      "s_mul_i32 s47, s47, 3\n"
      "s_add_u32 s48, s48, 548864\n"
      "v_add_u32 v17, s47, v17\n"
      "v_mul_u32_u24 v16, 0x71d, v17\n"
      "v_lshrrev_b32 v16, 16, v16\n"
      "v_mul_u32_u24 v18, 36, v16\n"
      "v_sub_u32 v18, v17, v18\n"
      "v_and_b32 v19, 1, v16\n"
      "v_lshlrev_b32 v16, 2, v19\n"
      "v_sub_u32 v16, v17, v16\n"
      "v_cmp_lt_u32 vcc, 19, v18\n"
      "v_lshlrev_b32 v19, 5, v19\n"
      "v_sub_u32 v19, v17, v19\n"
      "v_subrev_u32 v20, 4, v16\n"
      "v_cndmask_b32 v16, v16, v20, vcc\n"
      "v_lshrrev_b32 v18, 2, v18\n"
      "v_cmp_eq_u32 vcc, 4, v18\n"
      "v_add_u32 v19, 48, v19\n"
      "v_mov_b32 v12, 0\n"
      "v_cndmask_b32 v16, v16, v19, vcc\n"
      "v_cndmask_b32 v17, v17, v16, s[58:59]\n"
      "v_mul_u32_u24 v16, 0xe39, v17\n"
      "v_lshrrev_b32 v16, 19, v16\n"
      "v_mul_u32_u24 v18, 144, v16\n"
      "v_sub_u32 v18, v17, v18\n"
      "v_lshrrev_b32 v19, 3, v18\n"
      "v_and_b32 v18, 7, v18\n"
      "v_lshlrev_b32 v19, 9, v19\n"
      "v_lshl_add_u32 v19, v16, 3, v19\n"
      "v_add_lshl_u32 v19, v19, v18, 1\n"
      "v_cmp_eq_u32 vcc, 0, v2\n"
      "v_mov_b32 v13, 0\n"
      "v_mov_b32 v16, 1073741824\n"
      "v_cndmask_b32 v19, v16, v19, vcc\n"
      "s_waitcnt lgkmcnt(8)\n"
      "v_dot2c_f32_bf16 v12, v36, v92\n"
      "v_dot2c_f32_bf16 v13, v37, v92\n"
      "s_waitcnt lgkmcnt(7)\n"
      "v_dot2c_f32_bf16 v12, v38, v92\n"
      "v_dot2c_f32_bf16 v13, v39, v92\n"
      "s_waitcnt lgkmcnt(6)\n"
      "v_dot2c_f32_bf16 v12, v40, v92\n"
      "v_dot2c_f32_bf16 v13, v41, v92\n"
      "s_waitcnt lgkmcnt(5)\n"
      "v_dot2c_f32_bf16 v12, v42, v92\n"
      "v_dot2c_f32_bf16 v13, v43, v92\n"
      "s_waitcnt lgkmcnt(4)\n"
      "v_dot2c_f32_bf16 v12, v44, v92\n"
      "v_dot2c_f32_bf16 v13, v45, v92\n"
      "s_waitcnt lgkmcnt(3)\n"
      "v_dot2c_f32_bf16 v12, v46, v92\n"
      "v_dot2c_f32_bf16 v13, v47, v92\n"
      "s_waitcnt lgkmcnt(2)\n"
      "v_dot2c_f32_bf16 v12, v48, v92\n"
      "v_dot2c_f32_bf16 v13, v49, v92\n"
      "s_waitcnt lgkmcnt(1)\n"
      "v_dot2c_f32_bf16 v12, v50, v92\n"
      "v_dot2c_f32_bf16 v13, v51, v92\n"
      "s_waitcnt lgkmcnt(0)\n"
      "v_dot2c_f32_bf16 v12, v52, v92\n"
      "v_dot2c_f32_bf16 v13, v53, v92\n"
      "s_nop 2\n"
      "v_add_f32 v12, v12, v13\n"
      "s_nop 1\n"
      "v_add_f32_dpp v12, v12, v12 quad_perm:[1,0,3,2] row_mask:0xf bank_mask:0xf bound_ctrl:1\n"
      "s_nop 1\n"
      "v_add_f32_dpp v12, v12, v12 quad_perm:[2,3,0,1] row_mask:0xf bank_mask:0xf bound_ctrl:1\n"
      "s_nop 1\n"
      "v_add_f32_dpp v12, v12, v12 row_half_mirror row_mask:0xf bank_mask:0xf bound_ctrl:1\n"
      "s_nop 1\n"
      "v_add_f32_dpp v12, v12, v12 row_mirror row_mask:0xf bank_mask:0xf bound_ctrl:1\n"
      "v_mul_f32 v12, 0x3ae38e39, v12\n"
      "v_cvt_pk_bf16_f32 v12, v12, v12\n"
      "buffer_store_short v12, v19, s[4:7], s48 offen sc1\n"
      ".LRS2_%=:\n"
      "s_cmp_lt_u32 s40, 2\n"
      "s_cbranch_scc0 .LPT3_%=\n"
      "ds_read_b64_tr_b16 v[128:129], v32\n"
      "ds_read_b64_tr_b16 v[130:131], v32 offset:1024\n"
      "ds_read_b64_tr_b16 v[132:133], v33\n"
      "ds_read_b64_tr_b16 v[134:135], v33 offset:1024\n"
      "ds_read_b64_tr_b16 v[136:137], v34\n"
      "ds_read_b64_tr_b16 v[138:139], v34 offset:1024\n"
      "ds_read_b64_tr_b16 v[140:141], v35\n"
      "ds_read_b64_tr_b16 v[142:143], v35 offset:1024\n"
      "s_mov_b32 s52, 10790912\n"
      "s_waitcnt lgkmcnt(6)\n"
      "buffer_store_dwordx4 v[128:131], v28, s[4:7], s52 offen sc1\n"
      "s_mov_b32 s52, 10809344\n"
      "s_waitcnt lgkmcnt(4)\n"
      "buffer_store_dwordx4 v[132:135], v28, s[4:7], s52 offen sc1\n"
      "s_mov_b32 s52, 10827776\n"
      "s_waitcnt lgkmcnt(2)\n"
      "buffer_store_dwordx4 v[136:139], v28, s[4:7], s52 offen sc1\n"
      "s_mov_b32 s52, 10846208\n"
      "s_waitcnt lgkmcnt(0)\n"
      "buffer_store_dwordx4 v[140:143], v28, s[4:7], s52 offen sc1\n"
      ".LPT3_%=:\n"
      "s_waitcnt vmcnt(6)\n"
      "ds_read_b128 a[0:3], v76 offset:0\n"
      "ds_read_b128 a[16:19], v80 offset:0\n"
      "ds_read_b128 a[32:35], v76 offset:8192\n"
      "ds_read_b128 a[48:51], v80 offset:8192\n"
      "ds_read_b128 a[64:67], v76 offset:16384\n"
      "ds_read_b128 a[80:83], v80 offset:16384\n"
      "ds_read_b128 a[96:99], v76 offset:24576\n"
      "ds_read_b128 a[112:115], v80 offset:24576\n"
      "s_waitcnt lgkmcnt(0)\n"
      "v_mfma_f32_16x16x32_bf16 v[160:163], a[0:3], a[0:3], 0\n"
      "s_add_u32 m0, s72, 57344\n"
      "s_nop 0\n"
      "buffer_load_dwordx4 v14, s[8:11], s70 offen lds\n"
      "v_mfma_f32_16x16x32_bf16 v[164:167], v[92:95], a[0:3], 0\n"
      "v_mfma_f32_16x16x32_bf16 v[160:163], a[16:19], a[16:19], v[160:163]\n"
      "v_mfma_f32_16x16x32_bf16 v[164:167], v[92:95], a[16:19], v[164:167]\n"
      "v_mfma_f32_16x16x32_bf16 v[160:163], a[32:35], a[32:35], v[160:163]\n"
      "buffer_load_dwordx4 v15, s[8:11], s70 offen offset:1024 lds\n"
      "v_mfma_f32_16x16x32_bf16 v[164:167], v[92:95], a[32:35], v[164:167]\n"
      "v_mfma_f32_16x16x32_bf16 v[160:163], a[48:51], a[48:51], v[160:163]\n"
      "v_mfma_f32_16x16x32_bf16 v[164:167], v[92:95], a[48:51], v[164:167]\n"
      "v_mfma_f32_16x16x32_bf16 v[160:163], a[64:67], a[64:67], v[160:163]\n"
      "s_add_u32 m0, s72, 65536\n"
      "s_nop 0\n"
      "buffer_load_dwordx4 v14, s[8:11], s71 offen lds\n"
      "v_mfma_f32_16x16x32_bf16 v[164:167], v[92:95], a[64:67], v[164:167]\n"
      "v_mfma_f32_16x16x32_bf16 v[160:163], a[80:83], a[80:83], v[160:163]\n"
      "v_mfma_f32_16x16x32_bf16 v[164:167], v[92:95], a[80:83], v[164:167]\n"
      "v_mfma_f32_16x16x32_bf16 v[160:163], a[96:99], a[96:99], v[160:163]\n"
      "buffer_load_dwordx4 v15, s[8:11], s71 offen offset:1024 lds\n"
      "v_mfma_f32_16x16x32_bf16 v[164:167], v[92:95], a[96:99], v[164:167]\n"
      "v_mfma_f32_16x16x32_bf16 v[160:163], a[112:115], a[112:115], v[160:163]\n"
      "v_mfma_f32_16x16x32_bf16 v[164:167], v[92:95], a[112:115], v[164:167]\n"
      "s_add_u32 m0, s78, 0\n"
      "s_nop 0\n"
      "buffer_load_dwordx4 v68, s[12:15], s80 offen lds\n"
      "buffer_load_dwordx4 v69, s[12:15], s80 offen offset:1024 lds\n"
      "buffer_load_dwordx4 v70, s[12:15], s80 offen offset:2048 lds\n"
      "s_waitcnt vmcnt(7)\n"
      "ds_read_b128 a[128:131], v84 offset:0\n"
      "ds_read_b128 a[144:147], v88 offset:0\n"
      "ds_read_b128 a[160:163], v84 offset:8192\n"
      "ds_read_b128 a[176:179], v88 offset:8192\n"
      "ds_read_b128 a[192:195], v84 offset:16384\n"
      "ds_read_b128 a[208:211], v88 offset:16384\n"
      "buffer_load_dwordx4 v71, s[12:15], s80 offen offset:3072 lds\n"
      "s_add_u32 m0, s78, 4096\n"
      "s_nop 0\n"
      "buffer_load_dwordx4 v72, s[12:15], s80 offen lds\n"
      "s_waitcnt lgkmcnt(0)\n"
      "v_mfma_f32_16x16x32_bf16 v[160:163], a[128:131], a[128:131], v[160:163]\n"
      "s_add_u32 m0, s78, 20480\n"
      "s_nop 0\n"
      "buffer_load_dwordx4 v68, s[12:15], s81 offen lds\n"
      "v_mfma_f32_16x16x32_bf16 v[164:167], v[92:95], a[128:131], v[164:167]\n"
      "v_mfma_f32_16x16x32_bf16 v[160:163], a[144:147], a[144:147], v[160:163]\n"
      "v_mfma_f32_16x16x32_bf16 v[164:167], v[92:95], a[144:147], v[164:167]\n"
      "v_mfma_f32_16x16x32_bf16 v[160:163], a[160:163], a[160:163], v[160:163]\n"
      "v_mfma_f32_16x16x32_bf16 v[164:167], v[92:95], a[160:163], v[164:167]\n"
      "v_mfma_f32_16x16x32_bf16 v[160:163], a[176:179], a[176:179], v[160:163]\n"
      "buffer_load_dwordx4 v69, s[12:15], s81 offen offset:1024 lds\n"
      "v_mfma_f32_16x16x32_bf16 v[164:167], v[92:95], a[176:179], v[164:167]\n"
      "v_mfma_f32_16x16x32_bf16 v[160:163], a[192:195], a[192:195], v[160:163]\n"
      "v_mfma_f32_16x16x32_bf16 v[164:167], v[92:95], a[192:195], v[164:167]\n"
      "v_mfma_f32_16x16x32_bf16 v[160:163], a[208:211], a[208:211], v[160:163]\n"
      "v_mfma_f32_16x16x32_bf16 v[164:167], v[92:95], a[208:211], v[164:167]\n"
      "s_waitcnt vmcnt(7)\n"
      "ds_read_b128 a[224:227], v84 offset:24576\n"
      "ds_read_b128 a[240:243], v88 offset:24576\n"
      "ds_read_b128 v[96:99], v84 offset:32768\n"
      "ds_read_b128 v[112:115], v88 offset:32768\n"
      "buffer_load_dwordx4 v70, s[12:15], s81 offen offset:2048 lds\n"
      "s_waitcnt lgkmcnt(0)\n"
      "v_mfma_f32_16x16x32_bf16 v[160:163], a[224:227], a[224:227], v[160:163]\n"
      "buffer_load_dwordx4 v71, s[12:15], s81 offen offset:3072 lds\n"
      "v_mfma_f32_16x16x32_bf16 v[164:167], v[92:95], a[224:227], v[164:167]\n"
      "v_mfma_f32_16x16x32_bf16 v[160:163], a[240:243], a[240:243], v[160:163]\n"
      "v_mfma_f32_16x16x32_bf16 v[164:167], v[92:95], a[240:243], v[164:167]\n"
      "v_mfma_f32_16x16x32_bf16 v[160:163], v[96:99], v[96:99], v[160:163]\n"
      "s_add_u32 m0, s78, 24576\n"
      "s_nop 0\n"
      "buffer_load_dwordx4 v72, s[12:15], s81 offen lds\n"
      "v_mfma_f32_16x16x32_bf16 v[164:167], v[92:95], v[96:99], v[164:167]\n"
      "v_mfma_f32_16x16x32_bf16 v[160:163], v[112:115], v[112:115], v[160:163]\n"
      "v_mfma_f32_16x16x32_bf16 v[164:167], v[92:95], v[112:115], v[164:167]\n"
      "v_readfirstlane_b32 s42, v6\n"
      "v_readfirstlane_b32 s43, v7\n"
      "s_add_u32 s42, s42, 0x7F4A7C15\n"
      "s_addc_u32 s43, s43, 0x9E3779B9\n"
      "v_mov_b32 v6, s42\n"
      "v_mov_b32 v7, s43\n"
      "s_lshl_b32 s45, s40, 6\n"
      "s_add_u32 s45, s45, 161024\n"
      "v_lshl_add_u32 v15, v2, 2, s45\n"
      "v_cndmask_b32 v12, v160, v161, s[54:55]\n"
      "v_cndmask_b32 v13, v162, v163, s[54:55]\n"
      "v_cndmask_b32 v12, v12, v13, s[56:57]\n"
      "v_mul_f32 v14, 0x3ae38e39, v164\n"
      "v_mul_f32 v12, 0x3ae38e39, v12\n"
      "v_fma_f32 v12, -v14, v14, v12\n"
      "v_max_f32 v12, 0, v12\n"
      "v_add_f32 v12, 0x3727c5ac, v12\n"
      "v_rsq_f32 v12, v12\n"
      "s_mov_b32 exec_lo, 15728655\n"
      "s_mov_b32 exec_hi, 4026535680\n"
      "ds_write2_b32 v15, v14, v12 offset0:0 offset1:64\n"
      "s_mov_b64 exec, -1\n"
      "s_waitcnt vmcnt(5)\n"
      "s_waitcnt lgkmcnt(0)\n"
      "s_barrier\n"
      "ds_read_b64_tr_b16 v[128:129], v29 offset:0\n"
      "ds_read_b64_tr_b16 v[130:131], v29 offset:2304\n"
      "ds_read_b64_tr_b16 v[132:133], v30 offset:0\n"
      "ds_read_b64_tr_b16 v[134:135], v30 offset:2304\n"
      "ds_read_b128 a[4:7], v77 offset:0\n"
      "ds_read_b128 a[8:11], v78 offset:0\n"
      "ds_read_b128 a[12:15], v79 offset:0\n"
      "ds_read_b64_tr_b16 v[136:137], v29 offset:144\n"
      "ds_read_b64_tr_b16 v[138:139], v29 offset:2448\n"
      "ds_read_b64_tr_b16 v[140:141], v30 offset:144\n"
      "ds_read_b64_tr_b16 v[142:143], v30 offset:2448\n"
      "ds_read_b128 a[20:23], v81 offset:0\n"
      "ds_read_b128 a[24:27], v82 offset:0\n"
      "ds_read_b128 a[28:31], v83 offset:0\n"
      "s_add_u32 m0, s78, 40960\n"
      "s_nop 0\n"
      "buffer_load_dwordx4 v68, s[12:15], s82 offen lds\n"
      "s_waitcnt lgkmcnt(7)\n"
      "v_mfma_f32_16x16x32_bf16 v[200:203], v[92:95], v[128:131], 0\n"
      "ds_read_b64_tr_b16 v[144:145], v29 offset:10240\n"
      "ds_read_b64_tr_b16 v[146:147], v29 offset:12544\n"
      "v_mfma_f32_16x16x32_bf16 v[160:163], a[0:3], v[128:131], 0\n"
      "ds_read_b64_tr_b16 v[148:149], v30 offset:10240\n"
      "v_bfe_u32 v12, v4, 2, 1\n"
      "v_mfma_f32_16x16x32_bf16 v[164:167], a[4:7], v[128:131], 0\n"
      "ds_read_b64_tr_b16 v[150:151], v30 offset:12544\n"
      "v_cmp_eq_u32 vcc, 0, v12\n"
      "v_mfma_f32_16x16x32_bf16 v[168:171], a[8:11], v[128:131], 0\n"
      "ds_read_b128 a[36:39], v77 offset:8192\n"
      "v_mfma_f32_16x16x32_bf16 v[172:175], a[12:15], v[128:131], 0\n"
      "ds_read_b128 a[40:43], v78 offset:8192\n"
      "v_and_b32 v12, 7, v4\n"
      "s_mul_i32 s45, s40, 576\n"
      "v_mfma_f32_16x16x32_bf16 v[204:207], v[92:95], v[132:135], 0\n"
      "ds_read_b128 a[44:47], v79 offset:8192\n"
      "v_cndmask_b32 v14, 0, v92, vcc\n"
      "v_mfma_f32_16x16x32_bf16 v[192:195], a[0:3], v[132:135], 0\n"
      "buffer_load_dwordx4 v69, s[12:15], s82 offen offset:1024 lds\n"
      "s_add_u32 s45, s45, 155648\n"
      "s_waitcnt lgkmcnt(7)\n"
      "v_mfma_f32_16x16x32_bf16 v[200:203], v[92:95], v[136:139], v[200:203]\n"
      "ds_read_b64_tr_b16 v[152:153], v29 offset:10384\n"
      "ds_read_b64_tr_b16 v[154:155], v29 offset:12688\n"
      "v_mfma_f32_16x16x32_bf16 v[160:163], a[16:19], v[136:139], v[160:163]\n"
      "ds_read_b64_tr_b16 v[156:157], v30 offset:10384\n"
      "v_mov_b32 v15, v14\n"
      "v_mfma_f32_16x16x32_bf16 v[164:167], a[20:23], v[136:139], v[164:167]\n"
      "ds_read_b64_tr_b16 v[158:159], v30 offset:12688\n"
      "v_lshl_add_u32 v12, v12, 3, s45\n"
      "v_mfma_f32_16x16x32_bf16 v[168:171], a[24:27], v[136:139], v[168:171]\n"
      "ds_read_b128 a[52:55], v81 offset:8192\n"
      "ds_write_b64 v12, v[14:15] offset:512\n"
      "v_mfma_f32_16x16x32_bf16 v[172:175], a[28:31], v[136:139], v[172:175]\n"
      "ds_read_b128 a[56:59], v82 offset:8192\n"
      "v_lshl_add_u32 v13, v2, 2, v3\n"
      "v_mfma_f32_16x16x32_bf16 v[204:207], v[92:95], v[140:143], v[204:207]\n"
      "ds_read_b128 a[60:63], v83 offset:8192\n"
      "v_lshl_add_u32 v74, v13, 3, s45\n"
      "v_mfma_f32_16x16x32_bf16 v[192:195], a[16:19], v[140:143], v[192:195]\n"
      "buffer_load_dwordx4 v70, s[12:15], s82 offen offset:2048 lds\n"
      "s_lshl_b32 s46, s40, 1\n"
      "s_waitcnt lgkmcnt(8)\n"
      "v_mfma_f32_16x16x32_bf16 v[200:203], v[92:95], v[144:147], v[200:203]\n"
      "buffer_load_dwordx4 v71, s[12:15], s82 offen offset:3072 lds\n"
      "v_and_b32 v18, 1, v2\n"
      "v_mfma_f32_16x16x32_bf16 v[160:163], a[32:35], v[144:147], v[160:163]\n"
      "v_add_u32 v18, s46, v18\n"
      "v_add_u32 v13, 8, v18\n"
      "v_mfma_f32_16x16x32_bf16 v[164:167], a[36:39], v[144:147], v[164:167]\n"
      "v_cmp_eq_u32 vcc, 6, v2\n"
      "v_mov_b32 v16, 17\n"
      "v_mfma_f32_16x16x32_bf16 v[168:171], a[40:43], v[144:147], v[168:171]\n"
      "s_add_u32 m0, s78, 45056\n"
      "s_nop 0\n"
      "buffer_load_dwordx4 v72, s[12:15], s82 offen lds\n"
      "v_and_b32 v19, 3, v2\n"
      "v_mfma_f32_16x16x32_bf16 v[172:175], a[44:47], v[144:147], v[172:175]\n"
      "v_cndmask_b32 v16, v16, 16, vcc\n"
      "v_cmp_gt_u32 vcc, 6, v2\n"
      "v_mfma_f32_16x16x32_bf16 v[204:207], v[92:95], v[148:151], v[204:207]\n"
      "v_mov_b32 v17, 17\n"
      "s_lshl_b32 s79, s39, 2\n"
      "v_mfma_f32_16x16x32_bf16 v[192:195], a[32:35], v[148:151], v[192:195]\n"
      "v_cndmask_b32 v16, v16, v13, vcc\n"
      "v_cmp_gt_u32 vcc, 4, v2\n"
      "s_waitcnt vmcnt(5)\n"
      "s_waitcnt lgkmcnt(0)\n"
      "s_barrier\n"
      "ds_read_b64_tr_b16 v[128:129], v29 offset:20480\n"
      "ds_read_b64_tr_b16 v[130:131], v29 offset:22784\n"
      "ds_read_b64_tr_b16 v[132:133], v30 offset:20480\n"
      "ds_read_b64_tr_b16 v[134:135], v30 offset:22784\n"
      "ds_read_b128 a[68:71], v77 offset:16384\n"
      "ds_read_b128 a[72:75], v78 offset:16384\n"
      "ds_read_b128 a[76:79], v79 offset:16384\n"
      "v_mfma_f32_16x16x32_bf16 v[200:203], v[92:95], v[152:155], v[200:203]\n"
      "ds_read_b64_tr_b16 v[136:137], v29 offset:20624\n"
      "ds_read_b64_tr_b16 v[138:139], v29 offset:22928\n"
      "v_mfma_f32_16x16x32_bf16 v[160:163], a[48:51], v[152:155], v[160:163]\n"
      "ds_read_b64_tr_b16 v[140:141], v30 offset:20624\n"
      "s_add_u32 s79, s79, s40\n"
      "v_mfma_f32_16x16x32_bf16 v[164:167], a[52:55], v[152:155], v[164:167]\n"
      "ds_read_b64_tr_b16 v[142:143], v30 offset:22928\n"
      "s_mul_i32 s79, s79, 24576\n"
      "v_cndmask_b32 v16, v16, v18, vcc\n"
      "v_mfma_f32_16x16x32_bf16 v[168:171], a[56:59], v[152:155], v[168:171]\n"
      "ds_read_b128 a[84:87], v81 offset:16384\n"
      "v_mfma_f32_16x16x32_bf16 v[172:175], a[60:63], v[152:155], v[172:175]\n"
      "ds_read_b128 a[88:91], v82 offset:16384\n"
      "v_cmp_eq_u32 vcc, 3, v19\n"
      "s_add_u32 s79, s79, 567296\n"
      "v_mfma_f32_16x16x32_bf16 v[204:207], v[92:95], v[156:159], v[204:207]\n"
      "ds_read_b128 a[92:95], v83 offset:16384\n"
      "v_mov_b32 v75, 1073741824\n"
      "v_mfma_f32_16x16x32_bf16 v[192:195], a[48:51], v[156:159], v[192:195]\n"
      "s_add_u32 m0, s78, 61440\n"
      "s_nop 0\n"
      "buffer_load_dwordx4 v68, s[12:15], s83 offen lds\n"
      "s_waitcnt lgkmcnt(7)\n"
      "v_mfma_f32_16x16x32_bf16 v[200:203], v[92:95], v[128:131], v[200:203]\n"
      "ds_read_b64_tr_b16 v[144:145], v29 offset:30720\n"
      "ds_read_b64_tr_b16 v[146:147], v29 offset:33024\n"
      "v_mfma_f32_16x16x32_bf16 v[160:163], a[64:67], v[128:131], v[160:163]\n"
      "ds_read_b64_tr_b16 v[148:149], v30 offset:30720\n"
      "v_cndmask_b32 v17, v17, 16, vcc\n"
      "v_mfma_f32_16x16x32_bf16 v[164:167], a[68:71], v[128:131], v[164:167]\n"
      "ds_read_b64_tr_b16 v[150:151], v30 offset:33024\n"
      "v_cmp_gt_u32 vcc, 2, v2\n"
      "v_mfma_f32_16x16x32_bf16 v[168:171], a[72:75], v[128:131], v[168:171]\n"
      "ds_read_b128 a[100:103], v77 offset:24576\n"
      "v_lshl_add_u32 v16, v16, 2, v3\n"
      "v_mfma_f32_16x16x32_bf16 v[172:175], a[76:79], v[128:131], v[172:175]\n"
      "ds_read_b128 a[104:107], v78 offset:24576\n"
      "v_lshlrev_b32 v20, 4, v3\n"
      "v_mfma_f32_16x16x32_bf16 v[204:207], v[92:95], v[132:135], v[204:207]\n"
      "ds_read_b128 a[108:111], v79 offset:24576\n"
      "v_cndmask_b32 v17, v17, v13, vcc\n"
      "v_mfma_f32_16x16x32_bf16 v[192:195], a[64:67], v[132:135], v[192:195]\n"
      "buffer_load_dwordx4 v69, s[12:15], s83 offen offset:1024 lds\n"
      "v_cmp_gt_u32 vcc, 48, v4\n"
      "s_waitcnt lgkmcnt(7)\n"
      "v_mfma_f32_16x16x32_bf16 v[200:203], v[92:95], v[136:139], v[200:203]\n"
      "ds_read_b64_tr_b16 v[152:153], v29 offset:30864\n"
      "ds_read_b64_tr_b16 v[154:155], v29 offset:33168\n"
      "v_mfma_f32_16x16x32_bf16 v[160:163], a[80:83], v[136:139], v[160:163]\n"
      "ds_read_b64_tr_b16 v[156:157], v30 offset:30864\n"
      "v_lshl_add_u32 v17, v17, 2, v3\n"
      "v_mfma_f32_16x16x32_bf16 v[164:167], a[84:87], v[136:139], v[164:167]\n"
      "ds_read_b64_tr_b16 v[158:159], v30 offset:33168\n"
      "s_xor_b32 s45, s40, 0\n"
      "v_mfma_f32_16x16x32_bf16 v[168:171], a[88:91], v[136:139], v[168:171]\n"
      "ds_read_b128 a[116:119], v81 offset:24576\n"
      "s_mul_i32 s46, s45, 576\n"
      "v_mfma_f32_16x16x32_bf16 v[172:175], a[92:95], v[136:139], v[172:175]\n"
      "ds_read_b128 a[120:123], v82 offset:24576\n"
      "s_add_u32 s46, s46, 155648\n"
      "v_mfma_f32_16x16x32_bf16 v[204:207], v[92:95], v[140:143], v[204:207]\n"
      "ds_read_b128 a[124:127], v83 offset:24576\n"
      "v_lshl_add_u32 v44, v16, 3, s46\n"
      "v_mfma_f32_16x16x32_bf16 v[192:195], a[80:83], v[140:143], v[192:195]\n"
      "buffer_load_dwordx4 v70, s[12:15], s83 offen offset:2048 lds\n"
      "v_lshl_add_u32 v48, v17, 3, s46\n"
      "s_waitcnt lgkmcnt(7)\n"
      "v_mfma_f32_16x16x32_bf16 v[200:203], v[92:95], v[144:147], v[200:203]\n"
      "buffer_load_dwordx4 v71, s[12:15], s83 offen offset:3072 lds\n"
      "v_mfma_f32_16x16x32_bf16 v[160:163], a[96:99], v[144:147], v[160:163]\n"
      "s_lshl_b32 s45, s45, 6\n"
      "s_add_u32 s45, s45, 161024\n"
      "v_mfma_f32_16x16x32_bf16 v[164:167], a[100:103], v[144:147], v[164:167]\n"
      "v_add_u32 v21, s45, v20\n"
      "v_mfma_f32_16x16x32_bf16 v[168:171], a[104:107], v[144:147], v[168:171]\n"
      "s_add_u32 m0, s78, 65536\n"
      "s_nop 0\n"
      "buffer_load_dwordx4 v72, s[12:15], s83 offen lds\n"
      "v_mfma_f32_16x16x32_bf16 v[172:175], a[108:111], v[144:147], v[172:175]\n"
      "ds_read_b128 v[216:219], v21\n"
      "v_mfma_f32_16x16x32_bf16 v[204:207], v[92:95], v[148:151], v[204:207]\n"
      "ds_read_b128 v[232:235], v21 offset:256\n"
      "v_mfma_f32_16x16x32_bf16 v[192:195], a[96:99], v[148:151], v[192:195]\n"
      "s_xor_b32 s45, s40, 1\n"
      "s_waitcnt vmcnt(5)\n"
      "s_waitcnt lgkmcnt(0)\n"
      "s_barrier\n"
      "ds_read_b64_tr_b16 v[128:129], v31 offset:0\n"
      "ds_read_b64_tr_b16 v[130:131], v31 offset:2304\n"
      "ds_read_b64_tr_b16 v[132:133], v73 offset:0\n"
      "ds_read_b64_tr_b16 v[134:135], v73 offset:2304\n"
      "ds_read_b128 a[132:135], v85 offset:0\n"
      "ds_read_b128 a[136:139], v86 offset:0\n"
      "ds_read_b128 a[140:143], v87 offset:0\n"
      "v_mfma_f32_16x16x32_bf16 v[200:203], v[92:95], v[152:155], v[200:203]\n"
      "ds_read_b64_tr_b16 v[136:137], v31 offset:144\n"
      "ds_read_b64_tr_b16 v[138:139], v31 offset:2448\n"
      "v_mfma_f32_16x16x32_bf16 v[160:163], a[112:115], v[152:155], v[160:163]\n"
      "ds_read_b64_tr_b16 v[140:141], v73 offset:144\n"
      "s_mul_i32 s46, s45, 576\n"
      "v_mfma_f32_16x16x32_bf16 v[164:167], a[116:119], v[152:155], v[164:167]\n"
      "ds_read_b64_tr_b16 v[142:143], v73 offset:2448\n"
      "s_add_u32 s46, s46, 155648\n"
      "v_mfma_f32_16x16x32_bf16 v[168:171], a[120:123], v[152:155], v[168:171]\n"
      "ds_read_b128 a[148:151], v89 offset:0\n"
      "v_lshl_add_u32 v45, v16, 3, s46\n"
      "v_mfma_f32_16x16x32_bf16 v[172:175], a[124:127], v[152:155], v[172:175]\n"
      "ds_read_b128 a[152:155], v90 offset:0\n"
      "v_lshl_add_u32 v49, v17, 3, s46\n"
      "v_mfma_f32_16x16x32_bf16 v[204:207], v[92:95], v[156:159], v[204:207]\n"
      "ds_read_b128 a[156:159], v91 offset:0\n"
      "s_lshl_b32 s45, s45, 6\n"
      "v_mfma_f32_16x16x32_bf16 v[192:195], a[112:115], v[156:159], v[192:195]\n"
      "s_add_u32 m0, s78, 0\n"
      "s_nop 0\n"
      "buffer_load_dwordx4 v68, s[12:15], s84 offen lds\n"
      "s_add_u32 s45, s45, 161024\n"
      "s_waitcnt lgkmcnt(7)\n"
      "v_mfma_f32_16x16x32_bf16 v[200:203], v[92:95], v[128:131], v[200:203]\n"
      "ds_read_b64_tr_b16 v[144:145], v31 offset:10240\n"
      "ds_read_b64_tr_b16 v[146:147], v31 offset:12544\n"
      "v_mfma_f32_16x16x32_bf16 v[160:163], a[128:131], v[128:131], v[160:163]\n"
      "ds_read_b64_tr_b16 v[148:149], v73 offset:10240\n"
      "v_add_u32 v21, s45, v20\n"
      "v_mfma_f32_16x16x32_bf16 v[164:167], a[132:135], v[128:131], v[164:167]\n"
      "ds_read_b64_tr_b16 v[150:151], v73 offset:12544\n"
      "v_mfma_f32_16x16x32_bf16 v[168:171], a[136:139], v[128:131], v[168:171]\n"
      "ds_read_b128 a[164:167], v85 offset:8192\n"
      "ds_read_b128 v[220:223], v21\n"
      "v_mfma_f32_16x16x32_bf16 v[172:175], a[140:143], v[128:131], v[172:175]\n"
      "ds_read_b128 a[168:171], v86 offset:8192\n"
      "ds_read_b128 v[236:239], v21 offset:256\n"
      "v_mfma_f32_16x16x32_bf16 v[204:207], v[92:95], v[132:135], v[204:207]\n"
      "ds_read_b128 a[172:175], v87 offset:8192\n"
      "s_xor_b32 s45, s40, 2\n"
      "v_mfma_f32_16x16x32_bf16 v[192:195], a[128:131], v[132:135], v[192:195]\n"
      "buffer_load_dwordx4 v69, s[12:15], s84 offen offset:1024 lds\n"
      "s_mul_i32 s46, s45, 576\n"
      "s_add_u32 s46, s46, 155648\n"
      "s_waitcnt lgkmcnt(9)\n"
      "v_mfma_f32_16x16x32_bf16 v[200:203], v[92:95], v[136:139], v[200:203]\n"
      "ds_read_b64_tr_b16 v[152:153], v31 offset:10384\n"
      "ds_read_b64_tr_b16 v[154:155], v31 offset:12688\n"
      "v_mfma_f32_16x16x32_bf16 v[160:163], a[144:147], v[136:139], v[160:163]\n"
      "ds_read_b64_tr_b16 v[156:157], v73 offset:10384\n"
      "v_lshl_add_u32 v46, v16, 3, s46\n"
      "v_mfma_f32_16x16x32_bf16 v[164:167], a[148:151], v[136:139], v[164:167]\n"
      "ds_read_b64_tr_b16 v[158:159], v73 offset:12688\n"
      "v_lshl_add_u32 v50, v17, 3, s46\n"
      "v_mfma_f32_16x16x32_bf16 v[168:171], a[152:155], v[136:139], v[168:171]\n"
      "ds_read_b128 a[180:183], v89 offset:8192\n"
      "s_lshl_b32 s45, s45, 6\n"
      "v_mfma_f32_16x16x32_bf16 v[172:175], a[156:159], v[136:139], v[172:175]\n"
      "ds_read_b128 a[184:187], v90 offset:8192\n"
      "s_add_u32 s45, s45, 161024\n"
      "v_mfma_f32_16x16x32_bf16 v[204:207], v[92:95], v[140:143], v[204:207]\n"
      "ds_read_b128 a[188:191], v91 offset:8192\n"
      "v_add_u32 v21, s45, v20\n"
      "v_mfma_f32_16x16x32_bf16 v[192:195], a[144:147], v[140:143], v[192:195]\n"
      "buffer_load_dwordx4 v70, s[12:15], s84 offen offset:2048 lds\n"
      "ds_read_b128 v[224:227], v21\n"
      "s_waitcnt lgkmcnt(8)\n"
      "v_mfma_f32_16x16x32_bf16 v[200:203], v[92:95], v[144:147], v[200:203]\n"
      "buffer_load_dwordx4 v71, s[12:15], s84 offen offset:3072 lds\n"
      "v_mfma_f32_16x16x32_bf16 v[160:163], a[160:163], v[144:147], v[160:163]\n"
      "ds_read_b128 v[240:243], v21 offset:256\n"
      "v_mfma_f32_16x16x32_bf16 v[164:167], a[164:167], v[144:147], v[164:167]\n"
      "s_xor_b32 s45, s40, 3\n"
      "s_mul_i32 s46, s45, 576\n"
      "v_mfma_f32_16x16x32_bf16 v[168:171], a[168:171], v[144:147], v[168:171]\n"
      "s_add_u32 m0, s78, 4096\n"
      "s_nop 0\n"
      "buffer_load_dwordx4 v72, s[12:15], s84 offen lds\n"
      "v_mfma_f32_16x16x32_bf16 v[172:175], a[172:175], v[144:147], v[172:175]\n"
      "s_add_u32 s46, s46, 155648\n"
      "v_mfma_f32_16x16x32_bf16 v[204:207], v[92:95], v[148:151], v[204:207]\n"
      "v_lshl_add_u32 v47, v16, 3, s46\n"
      "v_mfma_f32_16x16x32_bf16 v[192:195], a[160:163], v[148:151], v[192:195]\n"
      "v_lshl_add_u32 v51, v17, 3, s46\n"
      "s_waitcnt vmcnt(5)\n"
      "s_waitcnt lgkmcnt(0)\n"
      "s_barrier\n"
      "ds_read_b64_tr_b16 v[128:129], v31 offset:20480\n"
      "ds_read_b64_tr_b16 v[130:131], v31 offset:22784\n"
      "ds_read_b64_tr_b16 v[132:133], v73 offset:20480\n"
      "ds_read_b64_tr_b16 v[134:135], v73 offset:22784\n"
      "ds_read_b128 a[196:199], v85 offset:16384\n"
      "ds_read_b128 a[200:203], v86 offset:16384\n"
      "ds_read_b128 a[204:207], v87 offset:16384\n"
      "v_mfma_f32_16x16x32_bf16 v[200:203], v[92:95], v[152:155], v[200:203]\n"
      "ds_read_b64_tr_b16 v[136:137], v31 offset:20624\n"
      "ds_read_b64_tr_b16 v[138:139], v31 offset:22928\n"
      "v_mfma_f32_16x16x32_bf16 v[160:163], a[176:179], v[152:155], v[160:163]\n"
      "ds_read_b64_tr_b16 v[140:141], v73 offset:20624\n"
      "s_lshl_b32 s45, s45, 6\n"
      "v_mfma_f32_16x16x32_bf16 v[164:167], a[180:183], v[152:155], v[164:167]\n"
      "ds_read_b64_tr_b16 v[142:143], v73 offset:22928\n"
      "s_add_u32 s45, s45, 161024\n"
      "v_mfma_f32_16x16x32_bf16 v[168:171], a[184:187], v[152:155], v[168:171]\n"
      "ds_read_b128 a[212:215], v89 offset:16384\n"
      "v_add_u32 v21, s45, v20\n"
      "v_mfma_f32_16x16x32_bf16 v[172:175], a[188:191], v[152:155], v[172:175]\n"
      "ds_read_b128 a[216:219], v90 offset:16384\n"
      "ds_read_b128 v[228:231], v21\n"
      "v_mfma_f32_16x16x32_bf16 v[204:207], v[92:95], v[156:159], v[204:207]\n"
      "ds_read_b128 a[220:223], v91 offset:16384\n"
      "ds_read_b128 v[244:247], v21 offset:256\n"
      "v_mfma_f32_16x16x32_bf16 v[192:195], a[176:179], v[156:159], v[192:195]\n"
      "s_add_u32 m0, s78, 20480\n"
      "s_nop 0\n"
      "buffer_load_dwordx4 v68, s[12:15], s85 offen lds\n"
      "s_waitcnt lgkmcnt(9)\n"
      "v_mfma_f32_16x16x32_bf16 v[200:203], v[92:95], v[128:131], v[200:203]\n"
      "ds_read_b64_tr_b16 v[144:145], v31 offset:30720\n"
      "ds_read_b64_tr_b16 v[146:147], v31 offset:33024\n"
      "v_mfma_f32_16x16x32_bf16 v[160:163], a[192:195], v[128:131], v[160:163]\n"
      "ds_read_b64_tr_b16 v[148:149], v73 offset:30720\n"
      "v_cndmask_b32 v75, v75, v1, vcc\n"
      "v_mfma_f32_16x16x32_bf16 v[164:167], a[196:199], v[128:131], v[164:167]\n"
      "ds_read_b64_tr_b16 v[150:151], v73 offset:33024\n"
      "v_mul_f32 v52, 0x3fb8aa3b, v232\n"
      "v_mfma_f32_16x16x32_bf16 v[168:171], a[200:203], v[128:131], v[168:171]\n"
      "ds_read_b128 a[228:231], v85 offset:24576\n"
      "v_mfma_f32_16x16x32_bf16 v[172:175], a[204:207], v[128:131], v[172:175]\n"
      "ds_read_b128 a[232:235], v86 offset:24576\n"
      "v_mul_f32 v53, 0x3fb8aa3b, v233\n"
      "v_mfma_f32_16x16x32_bf16 v[204:207], v[92:95], v[132:135], v[204:207]\n"
      "ds_read_b128 a[236:239], v87 offset:24576\n"
      "v_mul_f32 v54, 0x3fb8aa3b, v234\n"
      "v_mfma_f32_16x16x32_bf16 v[192:195], a[192:195], v[132:135], v[192:195]\n"
      "buffer_load_dwordx4 v69, s[12:15], s85 offen offset:1024 lds\n"
      "v_mul_f32 v55, 0x3fb8aa3b, v235\n"
      "s_waitcnt lgkmcnt(8)\n"
      "v_mfma_f32_16x16x32_bf16 v[200:203], v[92:95], v[136:139], v[200:203]\n"
      "ds_read_b64_tr_b16 v[152:153], v31 offset:30864\n"
      "ds_read_b64_tr_b16 v[154:155], v31 offset:33168\n"
      "v_mfma_f32_16x16x32_bf16 v[160:163], a[208:211], v[136:139], v[160:163]\n"
      "ds_read_b64_tr_b16 v[156:157], v73 offset:30864\n"
      "v_mul_f32 v56, 0x3fb8aa3b, v236\n"
      "v_mfma_f32_16x16x32_bf16 v[164:167], a[212:215], v[136:139], v[164:167]\n"
      "ds_read_b64_tr_b16 v[158:159], v73 offset:33168\n"
      "v_mul_f32 v57, 0x3fb8aa3b, v237\n"
      "v_mfma_f32_16x16x32_bf16 v[168:171], a[216:219], v[136:139], v[168:171]\n"
      "ds_read_b128 a[244:247], v89 offset:24576\n"
      "v_mfma_f32_16x16x32_bf16 v[172:175], a[220:223], v[136:139], v[172:175]\n"
      "ds_read_b128 a[248:251], v90 offset:24576\n"
      "v_mul_f32 v58, 0x3fb8aa3b, v238\n"
      "v_mfma_f32_16x16x32_bf16 v[204:207], v[92:95], v[140:143], v[204:207]\n"
      "ds_read_b128 a[252:255], v91 offset:24576\n"
      "v_mul_f32 v59, 0x3fb8aa3b, v239\n"
      "v_mfma_f32_16x16x32_bf16 v[192:195], a[208:211], v[140:143], v[192:195]\n"
      "buffer_load_dwordx4 v70, s[12:15], s85 offen offset:2048 lds\n"
      "v_mul_f32 v60, 0x3fb8aa3b, v240\n"
      "s_waitcnt lgkmcnt(7)\n"
      "v_mfma_f32_16x16x32_bf16 v[200:203], v[92:95], v[144:147], v[200:203]\n"
      "buffer_load_dwordx4 v71, s[12:15], s85 offen offset:3072 lds\n"
      "v_mfma_f32_16x16x32_bf16 v[160:163], a[224:227], v[144:147], v[160:163]\n"
      "v_mul_f32 v61, 0x3fb8aa3b, v241\n"
      "v_mul_f32 v62, 0x3fb8aa3b, v242\n"
      "v_mfma_f32_16x16x32_bf16 v[164:167], a[228:231], v[144:147], v[164:167]\n"
      "v_mul_f32 v63, 0x3fb8aa3b, v243\n"
      "v_mfma_f32_16x16x32_bf16 v[168:171], a[232:235], v[144:147], v[168:171]\n"
      "s_add_u32 m0, s78, 24576\n"
      "s_nop 0\n"
      "buffer_load_dwordx4 v72, s[12:15], s85 offen lds\n"
      "v_mfma_f32_16x16x32_bf16 v[172:175], a[236:239], v[144:147], v[172:175]\n"
      "v_mul_f32 v64, 0x3fb8aa3b, v244\n"
      "v_mfma_f32_16x16x32_bf16 v[204:207], v[92:95], v[148:151], v[204:207]\n"
      "v_mul_f32 v65, 0x3fb8aa3b, v245\n"
      "v_mul_f32 v66, 0x3fb8aa3b, v246\n"
      "v_mfma_f32_16x16x32_bf16 v[192:195], a[224:227], v[148:151], v[192:195]\n"
      "v_mul_f32 v67, 0x3fb8aa3b, v247\n"
      "s_waitcnt vmcnt(5)\n"
      "s_waitcnt lgkmcnt(0)\n"
      "s_barrier\n"
      "ds_read_b64_tr_b16 v[128:129], v29 offset:0\n"
      "ds_read_b64_tr_b16 v[130:131], v29 offset:2304\n"
      "ds_read_b64_tr_b16 v[132:133], v30 offset:0\n"
      "ds_read_b64_tr_b16 v[134:135], v30 offset:2304\n"
      "ds_read_b128 v[100:103], v85 offset:32768\n"
      "ds_read_b128 v[104:107], v86 offset:32768\n"
      "ds_read_b128 v[108:111], v87 offset:32768\n"
      "v_mfma_f32_16x16x32_bf16 v[200:203], v[92:95], v[152:155], v[200:203]\n"
      "ds_read_b64_tr_b16 v[136:137], v29 offset:144\n"
      "ds_read_b64_tr_b16 v[138:139], v29 offset:2448\n"
      "v_mfma_f32_16x16x32_bf16 v[160:163], a[240:243], v[152:155], v[160:163]\n"
      "ds_read_b64_tr_b16 v[140:141], v30 offset:144\n"
      "v_mfma_f32_16x16x32_bf16 v[164:167], a[244:247], v[152:155], v[164:167]\n"
      "ds_read_b64_tr_b16 v[142:143], v30 offset:2448\n"
      "v_mfma_f32_16x16x32_bf16 v[168:171], a[248:251], v[152:155], v[168:171]\n"
      "ds_read_b128 v[116:119], v89 offset:32768\n"
      "v_mfma_f32_16x16x32_bf16 v[172:175], a[252:255], v[152:155], v[172:175]\n"
      "ds_read_b128 v[120:123], v90 offset:32768\n"
      "v_mfma_f32_16x16x32_bf16 v[204:207], v[92:95], v[156:159], v[204:207]\n"
      "ds_read_b128 v[124:127], v91 offset:32768\n"
      "v_mfma_f32_16x16x32_bf16 v[192:195], a[240:243], v[156:159], v[192:195]\n"
      "s_add_u32 m0, s78, 40960\n"
      "s_nop 0\n"
      "buffer_load_dwordx4 v68, s[12:15], s86 offen lds\n"
      "s_waitcnt lgkmcnt(7)\n"
      "v_mfma_f32_16x16x32_bf16 v[200:203], v[92:95], v[128:131], v[200:203]\n"
      "ds_read_b64_tr_b16 v[144:145], v29 offset:10240\n"
      "v_mfma_f32_16x16x32_bf16 v[160:163], v[96:99], v[128:131], v[160:163]\n"
      "ds_read_b64_tr_b16 v[146:147], v29 offset:12544\n"
      "v_mfma_f32_16x16x32_bf16 v[164:167], v[100:103], v[128:131], v[164:167]\n"
      "ds_read_b64_tr_b16 v[148:149], v30 offset:10240\n"
      "v_mfma_f32_16x16x32_bf16 v[168:171], v[104:107], v[128:131], v[168:171]\n"
      "v_mfma_f32_16x16x32_bf16 v[172:175], v[108:111], v[128:131], v[172:175]\n"
      "ds_read_b64_tr_b16 v[150:151], v30 offset:12544\n"
      "v_mfma_f32_16x16x32_bf16 v[204:207], v[92:95], v[132:135], v[204:207]\n"
      "buffer_load_dwordx4 v69, s[12:15], s86 offen offset:1024 lds\n"
      "v_mfma_f32_16x16x32_bf16 v[192:195], v[96:99], v[132:135], v[192:195]\n"
      "s_waitcnt lgkmcnt(4)\n"
      "v_mfma_f32_16x16x32_bf16 v[200:203], v[92:95], v[136:139], v[200:203]\n"
      "ds_read_b64_tr_b16 v[152:153], v29 offset:10384\n"
      "v_mfma_f32_16x16x32_bf16 v[160:163], v[112:115], v[136:139], v[160:163]\n"
      "ds_read_b64_tr_b16 v[154:155], v29 offset:12688\n"
      "v_mfma_f32_16x16x32_bf16 v[164:167], v[116:119], v[136:139], v[164:167]\n"
      "ds_read_b64_tr_b16 v[156:157], v30 offset:10384\n"
      "v_mfma_f32_16x16x32_bf16 v[168:171], v[120:123], v[136:139], v[168:171]\n"
      "v_mfma_f32_16x16x32_bf16 v[172:175], v[124:127], v[136:139], v[172:175]\n"
      "ds_read_b64_tr_b16 v[158:159], v30 offset:12688\n"
      "v_mfma_f32_16x16x32_bf16 v[204:207], v[92:95], v[140:143], v[204:207]\n"
      "buffer_load_dwordx4 v70, s[12:15], s86 offen offset:2048 lds\n"
      "v_mfma_f32_16x16x32_bf16 v[192:195], v[112:115], v[140:143], v[192:195]\n"
      "s_waitcnt lgkmcnt(4)\n"
      "v_mfma_f32_16x16x32_bf16 v[208:211], v[92:95], v[144:147], 0\n"
      "buffer_load_dwordx4 v71, s[12:15], s86 offen offset:3072 lds\n"
      "v_mfma_f32_16x16x32_bf16 v[176:179], a[0:3], v[144:147], 0\n"
      "v_mfma_f32_16x16x32_bf16 v[180:183], a[4:7], v[144:147], 0\n"
      "v_mfma_f32_16x16x32_bf16 v[184:187], a[8:11], v[144:147], 0\n"
      "s_add_u32 m0, s78, 45056\n"
      "s_nop 0\n"
      "buffer_load_dwordx4 v72, s[12:15], s86 offen lds\n"
      "v_mfma_f32_16x16x32_bf16 v[188:191], a[12:15], v[144:147], 0\n"
      "v_mfma_f32_16x16x32_bf16 v[212:215], v[92:95], v[148:151], 0\n"
      "v_mfma_f32_16x16x32_bf16 v[196:199], a[0:3], v[148:151], 0\n"
      "s_waitcnt vmcnt(5)\n"
      "s_waitcnt lgkmcnt(0)\n"
      "s_barrier\n"
      "ds_read_b64_tr_b16 v[128:129], v29 offset:20480\n"
      "ds_read_b64_tr_b16 v[130:131], v29 offset:22784\n"
      "ds_read_b64_tr_b16 v[132:133], v30 offset:20480\n"
      "ds_read_b64_tr_b16 v[134:135], v30 offset:22784\n"
      "v_mfma_f32_16x16x32_bf16 v[208:211], v[92:95], v[152:155], v[208:211]\n"
      "ds_read_b64_tr_b16 v[136:137], v29 offset:20624\n"
      "v_fma_f32 v192, -v216, v204, v192\n"
      "v_fma_f32 v193, -v217, v204, v193\n"
      "v_mfma_f32_16x16x32_bf16 v[176:179], a[16:19], v[152:155], v[176:179]\n"
      "ds_read_b64_tr_b16 v[138:139], v29 offset:22928\n"
      "v_fma_f32 v194, -v218, v204, v194\n"
      "v_fma_f32 v195, -v219, v204, v195\n"
      "v_mfma_f32_16x16x32_bf16 v[180:183], a[20:23], v[152:155], v[180:183]\n"
      "ds_read_b64_tr_b16 v[140:141], v30 offset:20624\n"
      "v_mul_f32 v12, v192, v52\n"
      "v_mul_f32 v13, v193, v53\n"
      "v_mfma_f32_16x16x32_bf16 v[184:187], a[24:27], v[152:155], v[184:187]\n"
      "v_mul_f32 v14, v194, v54\n"
      "v_mul_f32 v15, v195, v55\n"
      "v_exp_f32 v12, v12 clamp\n"
      "v_mfma_f32_16x16x32_bf16 v[188:191], a[28:31], v[152:155], v[188:191]\n"
      "ds_read_b64_tr_b16 v[142:143], v30 offset:22928\n"
      "v_exp_f32 v13, v13 clamp\n"
      "v_mfma_f32_16x16x32_bf16 v[212:215], v[92:95], v[156:159], v[212:215]\n"
      "s_add_u32 m0, s78, 61440\n"
      "s_nop 0\n"
      "buffer_load_dwordx4 v68, s[12:15], s87 offen lds\n"
      "v_mfma_f32_16x16x32_bf16 v[196:199], a[16:19], v[156:159], v[196:199]\n"
      "v_exp_f32 v14, v14 clamp\n"
      "v_exp_f32 v15, v15 clamp\n"
      "s_waitcnt lgkmcnt(4)\n"
      "v_mfma_f32_16x16x32_bf16 v[208:211], v[92:95], v[128:131], v[208:211]\n"
      "ds_read_b64_tr_b16 v[144:145], v29 offset:30720\n"
      "v_max_f32 v192, 0, v192\n"
      "v_mfma_f32_16x16x32_bf16 v[176:179], a[32:35], v[128:131], v[176:179]\n"
      "ds_read_b64_tr_b16 v[146:147], v29 offset:33024\n"
      "v_max_f32 v193, 0, v193\n"
      "v_max_f32 v194, 0, v194\n"
      "v_mfma_f32_16x16x32_bf16 v[180:183], a[36:39], v[128:131], v[180:183]\n"
      "ds_read_b64_tr_b16 v[148:149], v30 offset:30720\n"
      "v_max_f32 v195, 0, v195\n"
      "v_mfma_f32_16x16x32_bf16 v[184:187], a[40:43], v[128:131], v[184:187]\n"
      "v_fma_f32 v192, v192, v232, v12\n"
      "v_fma_f32 v193, v193, v233, v13\n"
      "v_fma_f32 v194, v194, v234, v14\n"
      "v_mfma_f32_16x16x32_bf16 v[188:191], a[44:47], v[128:131], v[188:191]\n"
      "ds_read_b64_tr_b16 v[150:151], v30 offset:33024\n"
      "v_fma_f32 v195, v195, v235, v15\n"
      "v_mfma_f32_16x16x32_bf16 v[212:215], v[92:95], v[132:135], v[212:215]\n"
      "buffer_load_dwordx4 v69, s[12:15], s87 offen offset:1024 lds\n"
      "v_add_f32 v192, 0x358637bd, v192\n"
      "v_add_f32 v193, 0x358637bd, v193\n"
      "v_mfma_f32_16x16x32_bf16 v[196:199], a[32:35], v[132:135], v[196:199]\n"
      "v_add_f32 v194, 0x358637bd, v194\n"
      "v_add_f32 v195, 0x358637bd, v195\n"
      "s_waitcnt lgkmcnt(4)\n"
      "v_mfma_f32_16x16x32_bf16 v[208:211], v[92:95], v[136:139], v[208:211]\n"
      "ds_read_b64_tr_b16 v[152:153], v29 offset:30864\n"
      "v_cvt_pk_bf16_f32 v16, v192, v193\n"
      "v_mfma_f32_16x16x32_bf16 v[176:179], a[48:51], v[136:139], v[176:179]\n"
      "ds_read_b64_tr_b16 v[154:155], v29 offset:33168\n"
      "v_cvt_pk_bf16_f32 v17, v194, v195\n"
      "v_mfma_f32_16x16x32_bf16 v[180:183], a[52:55], v[136:139], v[180:183]\n"
      "ds_read_b64_tr_b16 v[156:157], v30 offset:30864\n"
      "ds_write_b64 v74, v[16:17]\n"
      "v_mov_b32 v18, 0\n"
      "v_mfma_f32_16x16x32_bf16 v[184:187], a[56:59], v[136:139], v[184:187]\n"
      "v_fma_f32 v160, -v216, v200, v160\n"
      "v_fma_f32 v161, -v217, v200, v161\n"
      "v_fma_f32 v162, -v218, v200, v162\n"
      "v_mfma_f32_16x16x32_bf16 v[188:191], a[60:63], v[136:139], v[188:191]\n"
      "ds_read_b64_tr_b16 v[158:159], v30 offset:33168\n"
      "v_fma_f32 v163, -v219, v200, v163\n"
      "v_mfma_f32_16x16x32_bf16 v[212:215], v[92:95], v[140:143], v[212:215]\n"
      "buffer_load_dwordx4 v70, s[12:15], s87 offen offset:2048 lds\n"
      "v_mul_f32 v12, v160, v52\n"
      "v_mul_f32 v13, v161, v53\n"
      "v_mfma_f32_16x16x32_bf16 v[196:199], a[48:51], v[140:143], v[196:199]\n"
      "v_mul_f32 v14, v162, v54\n"
      "v_mul_f32 v15, v163, v55\n"
      "s_waitcnt lgkmcnt(5)\n"
      "v_mfma_f32_16x16x32_bf16 v[208:211], v[92:95], v[144:147], v[208:211]\n"
      "buffer_load_dwordx4 v71, s[12:15], s87 offen offset:3072 lds\n"
      "v_exp_f32 v12, v12 clamp\n"
      "v_mfma_f32_16x16x32_bf16 v[176:179], a[64:67], v[144:147], v[176:179]\n"
      "v_exp_f32 v13, v13 clamp\n"
      "v_mfma_f32_16x16x32_bf16 v[180:183], a[68:71], v[144:147], v[180:183]\n"
      "v_exp_f32 v14, v14 clamp\n"
      "v_mfma_f32_16x16x32_bf16 v[184:187], a[72:75], v[144:147], v[184:187]\n"
      "s_add_u32 m0, s78, 65536\n"
      "s_nop 0\n"
      "buffer_load_dwordx4 v72, s[12:15], s87 offen lds\n"
      "v_exp_f32 v15, v15 clamp\n"
      "v_mfma_f32_16x16x32_bf16 v[188:191], a[76:79], v[144:147], v[188:191]\n"
      "v_max_f32 v160, 0, v160\n"
      "v_max_f32 v161, 0, v161\n"
      "v_max_f32 v162, 0, v162\n"
      "v_mfma_f32_16x16x32_bf16 v[212:215], v[92:95], v[148:151], v[212:215]\n"
      "v_max_f32 v163, 0, v163\n"
      "v_fma_f32 v160, v160, v232, v12\n"
      "v_mfma_f32_16x16x32_bf16 v[196:199], a[64:67], v[148:151], v[196:199]\n"
      "v_fma_f32 v161, v161, v233, v13\n"
      "v_fma_f32 v162, v162, v234, v14\n"
      "v_fma_f32 v163, v163, v235, v15\n"
      "s_waitcnt vmcnt(5)\n"
      "s_waitcnt lgkmcnt(0)\n"
      "s_barrier\n"
      "ds_read_b64_tr_b16 v[128:129], v31 offset:0\n"
      "ds_read_b64_tr_b16 v[130:131], v31 offset:2304\n"
      "ds_read_b64_tr_b16 v[132:133], v73 offset:0\n"
      "ds_read_b64_tr_b16 v[134:135], v73 offset:2304\n"
      "v_mfma_f32_16x16x32_bf16 v[208:211], v[92:95], v[152:155], v[208:211]\n"
      "ds_read_b64_tr_b16 v[136:137], v31 offset:144\n"
      "v_add_f32 v160, 0x358637bd, v160\n"
      "v_add_f32 v161, 0x358637bd, v161\n"
      "v_mfma_f32_16x16x32_bf16 v[176:179], a[80:83], v[152:155], v[176:179]\n"
      "ds_read_b64_tr_b16 v[138:139], v31 offset:2448\n"
      "v_add_f32 v162, 0x358637bd, v162\n"
      "v_mfma_f32_16x16x32_bf16 v[180:183], a[84:87], v[152:155], v[180:183]\n"
      "ds_read_b64_tr_b16 v[140:141], v73 offset:144\n"
      "v_add_f32 v163, 0x358637bd, v163\n"
      "v_cvt_pk_bf16_f32 v36, v160, v161\n"
      "v_mfma_f32_16x16x32_bf16 v[184:187], a[88:91], v[152:155], v[184:187]\n"
      "v_cvt_pk_bf16_f32 v37, v162, v163\n"
      "v_fma_f32 v164, -v220, v200, v164\n"
      "v_mfma_f32_16x16x32_bf16 v[188:191], a[92:95], v[152:155], v[188:191]\n"
      "ds_read_b64_tr_b16 v[142:143], v73 offset:2448\n"
      "v_fma_f32 v165, -v221, v200, v165\n"
      "v_fma_f32 v166, -v222, v200, v166\n"
      "v_mfma_f32_16x16x32_bf16 v[212:215], v[92:95], v[156:159], v[212:215]\n"
      "s_add_u32 m0, s78, 0\n"
      "s_nop 0\n"
      "buffer_load_dwordx4 v68, s[12:15], s88 offen lds\n"
      "v_fma_f32 v167, -v223, v200, v167\n"
      "v_mfma_f32_16x16x32_bf16 v[196:199], a[80:83], v[156:159], v[196:199]\n"
      "v_mul_f32 v12, v164, v56\n"
      "v_mul_f32 v13, v165, v57\n"
      "s_waitcnt lgkmcnt(4)\n"
      "v_mfma_f32_16x16x32_bf16 v[208:211], v[92:95], v[128:131], v[208:211]\n"
      "ds_read_b64_tr_b16 v[144:145], v31 offset:10240\n"
      "v_mul_f32 v14, v166, v58\n"
      "v_mul_f32 v15, v167, v59\n"
      "v_mfma_f32_16x16x32_bf16 v[176:179], a[96:99], v[128:131], v[176:179]\n"
      "ds_read_b64_tr_b16 v[146:147], v31 offset:12544\n"
      "v_exp_f32 v12, v12 clamp\n"
      "v_mfma_f32_16x16x32_bf16 v[180:183], a[100:103], v[128:131], v[180:183]\n"
      "ds_read_b64_tr_b16 v[148:149], v73 offset:10240\n"
      "v_exp_f32 v13, v13 clamp\n"
      "v_mfma_f32_16x16x32_bf16 v[184:187], a[104:107], v[128:131], v[184:187]\n"
      "v_exp_f32 v14, v14 clamp\n"
      "v_exp_f32 v15, v15 clamp\n"
      "v_mfma_f32_16x16x32_bf16 v[188:191], a[108:111], v[128:131], v[188:191]\n"
      "ds_read_b64_tr_b16 v[150:151], v73 offset:12544\n"
      "v_max_f32 v164, 0, v164\n"
      "v_mfma_f32_16x16x32_bf16 v[212:215], v[92:95], v[132:135], v[212:215]\n"
      "buffer_load_dwordx4 v69, s[12:15], s88 offen offset:1024 lds\n"
      "v_max_f32 v165, 0, v165\n"
      "v_max_f32 v166, 0, v166\n"
      "v_mfma_f32_16x16x32_bf16 v[196:199], a[96:99], v[132:135], v[196:199]\n"
      "v_max_f32 v167, 0, v167\n"
      "v_fma_f32 v164, v164, v236, v12\n"
      "v_fma_f32 v165, v165, v237, v13\n"
      "s_waitcnt lgkmcnt(4)\n"
      "v_mfma_f32_16x16x32_bf16 v[208:211], v[92:95], v[136:139], v[208:211]\n"
      "ds_read_b64_tr_b16 v[152:153], v31 offset:10384\n"
      "v_fma_f32 v166, v166, v238, v14\n"
      "v_mfma_f32_16x16x32_bf16 v[176:179], a[112:115], v[136:139], v[176:179]\n"
      "ds_read_b64_tr_b16 v[154:155], v31 offset:12688\n"
      "v_fma_f32 v167, v167, v239, v15\n"
      "v_add_f32 v164, 0x358637bd, v164\n"
      "v_mfma_f32_16x16x32_bf16 v[180:183], a[116:119], v[136:139], v[180:183]\n"
      "ds_read_b64_tr_b16 v[156:157], v73 offset:10384\n"
      "v_add_f32 v165, 0x358637bd, v165\n"
      "v_mfma_f32_16x16x32_bf16 v[184:187], a[120:123], v[136:139], v[184:187]\n"
      "v_add_f32 v166, 0x358637bd, v166\n"
      "v_add_f32 v167, 0x358637bd, v167\n"
      "v_cvt_pk_bf16_f32 v38, v164, v165\n"
      "v_mfma_f32_16x16x32_bf16 v[188:191], a[124:127], v[136:139], v[188:191]\n"
      "ds_read_b64_tr_b16 v[158:159], v73 offset:12688\n"
      "v_cvt_pk_bf16_f32 v39, v166, v167\n"
      "v_mfma_f32_16x16x32_bf16 v[212:215], v[92:95], v[140:143], v[212:215]\n"
      "buffer_load_dwordx4 v70, s[12:15], s88 offen offset:2048 lds\n"
      "v_fma_f32 v168, -v224, v200, v168\n"
      "v_fma_f32 v169, -v225, v200, v169\n"
      "v_mfma_f32_16x16x32_bf16 v[196:199], a[112:115], v[140:143], v[196:199]\n"
      "v_fma_f32 v170, -v226, v200, v170\n"
      "v_fma_f32 v171, -v227, v200, v171\n"
      "s_waitcnt lgkmcnt(4)\n"
      "v_mfma_f32_16x16x32_bf16 v[208:211], v[92:95], v[144:147], v[208:211]\n"
      "buffer_load_dwordx4 v71, s[12:15], s88 offen offset:3072 lds\n"
      "v_mul_f32 v12, v168, v60\n"
      "v_mul_f32 v13, v169, v61\n"
      "v_mfma_f32_16x16x32_bf16 v[176:179], a[128:131], v[144:147], v[176:179]\n"
      "v_mul_f32 v14, v170, v62\n"
      "v_mul_f32 v15, v171, v63\n"
      "v_mfma_f32_16x16x32_bf16 v[180:183], a[132:135], v[144:147], v[180:183]\n"
      "v_exp_f32 v12, v12 clamp\n"
      "v_mfma_f32_16x16x32_bf16 v[184:187], a[136:139], v[144:147], v[184:187]\n"
      "s_add_u32 m0, s78, 4096\n"
      "s_nop 0\n"
      "buffer_load_dwordx4 v72, s[12:15], s88 offen lds\n"
      "v_exp_f32 v13, v13 clamp\n"
      "v_mfma_f32_16x16x32_bf16 v[188:191], a[140:143], v[144:147], v[188:191]\n"
      "v_exp_f32 v14, v14 clamp\n"
      "v_mfma_f32_16x16x32_bf16 v[212:215], v[92:95], v[148:151], v[212:215]\n"
      "v_exp_f32 v15, v15 clamp\n"
      "v_max_f32 v168, 0, v168\n"
      "v_mfma_f32_16x16x32_bf16 v[196:199], a[128:131], v[148:151], v[196:199]\n"
      "v_max_f32 v169, 0, v169\n"
      "v_max_f32 v170, 0, v170\n"
      "v_max_f32 v171, 0, v171\n"
      "s_waitcnt vmcnt(5)\n"
      "s_waitcnt lgkmcnt(0)\n"
      "s_barrier\n"
      "ds_read_b64_tr_b16 v[128:129], v31 offset:20480\n"
      "ds_read_b64_tr_b16 v[130:131], v31 offset:22784\n"
      "ds_read_b64_tr_b16 v[132:133], v73 offset:20480\n"
      "ds_read_b64_tr_b16 v[134:135], v73 offset:22784\n"
      "v_mfma_f32_16x16x32_bf16 v[208:211], v[92:95], v[152:155], v[208:211]\n"
      "ds_read_b64_tr_b16 v[136:137], v31 offset:20624\n"
      "v_fma_f32 v168, v168, v240, v12\n"
      "v_mfma_f32_16x16x32_bf16 v[176:179], a[144:147], v[152:155], v[176:179]\n"
      "ds_read_b64_tr_b16 v[138:139], v31 offset:22928\n"
      "v_fma_f32 v169, v169, v241, v13\n"
      "v_mfma_f32_16x16x32_bf16 v[180:183], a[148:151], v[152:155], v[180:183]\n"
      "ds_read_b64_tr_b16 v[140:141], v73 offset:20624\n"
      "v_fma_f32 v170, v170, v242, v14\n"
      "v_fma_f32 v171, v171, v243, v15\n"
      "v_mfma_f32_16x16x32_bf16 v[184:187], a[152:155], v[152:155], v[184:187]\n"
      "v_add_f32 v168, 0x358637bd, v168\n"
      "v_add_f32 v169, 0x358637bd, v169\n"
      "v_mfma_f32_16x16x32_bf16 v[188:191], a[156:159], v[152:155], v[188:191]\n"
      "ds_read_b64_tr_b16 v[142:143], v73 offset:22928\n"
      "v_add_f32 v170, 0x358637bd, v170\n"
      "v_mfma_f32_16x16x32_bf16 v[212:215], v[92:95], v[156:159], v[212:215]\n"
      "s_add_u32 m0, s78, 20480\n"
      "s_nop 0\n"
      "buffer_load_dwordx4 v68, s[12:15], s89 offen lds\n"
      "v_add_f32 v171, 0x358637bd, v171\n"
      "v_mfma_f32_16x16x32_bf16 v[196:199], a[144:147], v[156:159], v[196:199]\n"
      "v_cvt_pk_bf16_f32 v40, v168, v169\n"
      "v_cvt_pk_bf16_f32 v41, v170, v171\n"
      "s_waitcnt lgkmcnt(4)\n"
      "v_mfma_f32_16x16x32_bf16 v[208:211], v[92:95], v[128:131], v[208:211]\n"
      "ds_read_b64_tr_b16 v[144:145], v31 offset:30720\n"
      "v_fma_f32 v172, -v228, v200, v172\n"
      "v_mfma_f32_16x16x32_bf16 v[176:179], a[160:163], v[128:131], v[176:179]\n"
      "ds_read_b64_tr_b16 v[146:147], v31 offset:33024\n"
      "v_fma_f32 v173, -v229, v200, v173\n"
      "v_fma_f32 v174, -v230, v200, v174\n"
      "v_mfma_f32_16x16x32_bf16 v[180:183], a[164:167], v[128:131], v[180:183]\n"
      "ds_read_b64_tr_b16 v[148:149], v73 offset:30720\n"
      "v_fma_f32 v175, -v231, v200, v175\n"
      "v_mfma_f32_16x16x32_bf16 v[184:187], a[168:171], v[128:131], v[184:187]\n"
      "v_mul_f32 v12, v172, v64\n"
      "v_mul_f32 v13, v173, v65\n"
      "v_mul_f32 v14, v174, v66\n"
      "v_mfma_f32_16x16x32_bf16 v[188:191], a[172:175], v[128:131], v[188:191]\n"
      "ds_read_b64_tr_b16 v[150:151], v73 offset:33024\n"
      "v_mul_f32 v15, v175, v67\n"
      "v_mfma_f32_16x16x32_bf16 v[212:215], v[92:95], v[132:135], v[212:215]\n"
      "buffer_load_dwordx4 v69, s[12:15], s89 offen offset:1024 lds\n"
      "v_exp_f32 v12, v12 clamp\n"
      "v_mfma_f32_16x16x32_bf16 v[196:199], a[160:163], v[132:135], v[196:199]\n"
      "v_exp_f32 v13, v13 clamp\n"
      "s_waitcnt lgkmcnt(4)\n"
      "v_mfma_f32_16x16x32_bf16 v[208:211], v[92:95], v[136:139], v[208:211]\n"
      "ds_read_b64_tr_b16 v[152:153], v31 offset:30864\n"
      "v_exp_f32 v14, v14 clamp\n"
      "v_mfma_f32_16x16x32_bf16 v[176:179], a[176:179], v[136:139], v[176:179]\n"
      "ds_read_b64_tr_b16 v[154:155], v31 offset:33168\n"
      "v_mfma_f32_16x16x32_bf16 v[180:183], a[180:183], v[136:139], v[180:183]\n"
      "ds_read_b64_tr_b16 v[156:157], v73 offset:30864\n"
      "v_exp_f32 v15, v15 clamp\n"
      "v_mfma_f32_16x16x32_bf16 v[184:187], a[184:187], v[136:139], v[184:187]\n"
      "v_max_f32 v172, 0, v172\n"
      "v_max_f32 v173, 0, v173\n"
      "v_max_f32 v174, 0, v174\n"
      "v_mfma_f32_16x16x32_bf16 v[188:191], a[188:191], v[136:139], v[188:191]\n"
      "ds_read_b64_tr_b16 v[158:159], v73 offset:33168\n"
      "v_max_f32 v175, 0, v175\n"
      "v_mfma_f32_16x16x32_bf16 v[212:215], v[92:95], v[140:143], v[212:215]\n"
      "buffer_load_dwordx4 v70, s[12:15], s89 offen offset:2048 lds\n"
      "v_fma_f32 v172, v172, v244, v12\n"
      "v_fma_f32 v173, v173, v245, v13\n"
      "v_mfma_f32_16x16x32_bf16 v[196:199], a[176:179], v[140:143], v[196:199]\n"
      "v_fma_f32 v174, v174, v246, v14\n"
      "v_fma_f32 v175, v175, v247, v15\n"
      "s_waitcnt lgkmcnt(4)\n"
      "v_mfma_f32_16x16x32_bf16 v[208:211], v[92:95], v[144:147], v[208:211]\n"
      "buffer_load_dwordx4 v71, s[12:15], s89 offen offset:3072 lds\n"
      "v_add_f32 v172, 0x358637bd, v172\n"
      "v_mfma_f32_16x16x32_bf16 v[176:179], a[192:195], v[144:147], v[176:179]\n"
      "v_add_f32 v173, 0x358637bd, v173\n"
      "v_add_f32 v174, 0x358637bd, v174\n"
      "v_mfma_f32_16x16x32_bf16 v[180:183], a[196:199], v[144:147], v[180:183]\n"
      "v_add_f32 v175, 0x358637bd, v175\n"
      "v_cvt_pk_bf16_f32 v42, v172, v173\n"
      "v_mfma_f32_16x16x32_bf16 v[184:187], a[200:203], v[144:147], v[184:187]\n"
      "s_add_u32 m0, s78, 24576\n"
      "s_nop 0\n"
      "buffer_load_dwordx4 v72, s[12:15], s89 offen lds\n"
      "v_cvt_pk_bf16_f32 v43, v174, v175\n"
      "v_mfma_f32_16x16x32_bf16 v[188:191], a[204:207], v[144:147], v[188:191]\n"
      "v_cmp_gt_u32 vcc, 8, v2\n"
      "v_mov_b32 v19, 0x100\n"
      "v_mfma_f32_16x16x32_bf16 v[212:215], v[92:95], v[148:151], v[212:215]\n"
      "s_mul_i32 s51, s38, 768\n"
      "v_cndmask_b32 v18, 0, v19, vcc\n"
      "s_add_u32 s51, s51, s79\n"
      "v_mfma_f32_16x16x32_bf16 v[196:199], a[192:195], v[148:151], v[196:199]\n"
      "v_add_u32 v74, v74, v18\n"
      "v_cmp_eq_u32 s[58:59], 0, v3\n"
      "s_waitcnt vmcnt(5)\n"
      "s_waitcnt lgkmcnt(0)\n"
      "s_barrier\n"
      "ds_read_b64_tr_b16 v[128:129], v29 offset:0\n"
      "ds_read_b64_tr_b16 v[130:131], v29 offset:2304\n"
      "ds_read_b64_tr_b16 v[132:133], v30 offset:0\n"
      "ds_read_b64_tr_b16 v[134:135], v30 offset:2304\n"
      "v_mfma_f32_16x16x32_bf16 v[208:211], v[92:95], v[152:155], v[208:211]\n"
      "ds_read_b64_tr_b16 v[136:137], v29 offset:144\n"
      "v_mfma_f32_16x16x32_bf16 v[176:179], a[208:211], v[152:155], v[176:179]\n"
      "ds_read_b64_tr_b16 v[138:139], v29 offset:2448\n"
      "v_mfma_f32_16x16x32_bf16 v[180:183], a[212:215], v[152:155], v[180:183]\n"
      "ds_read_b64_tr_b16 v[140:141], v30 offset:144\n"
      "v_mfma_f32_16x16x32_bf16 v[184:187], a[216:219], v[152:155], v[184:187]\n"
      "v_mfma_f32_16x16x32_bf16 v[188:191], a[220:223], v[152:155], v[188:191]\n"
      "ds_read_b64_tr_b16 v[142:143], v30 offset:2448\n"
      "v_mfma_f32_16x16x32_bf16 v[212:215], v[92:95], v[156:159], v[212:215]\n"
      "s_add_u32 m0, s78, 40960\n"
      "s_nop 0\n"
      "buffer_load_dwordx4 v68, s[12:15], s90 offen lds\n"
      "v_mfma_f32_16x16x32_bf16 v[196:199], a[208:211], v[156:159], v[196:199]\n"
      "s_waitcnt lgkmcnt(4)\n"
      "v_mfma_f32_16x16x32_bf16 v[208:211], v[92:95], v[128:131], v[208:211]\n"
      "ds_read_b64_tr_b16 v[144:145], v29 offset:10240\n"
      "v_mfma_f32_16x16x32_bf16 v[176:179], a[224:227], v[128:131], v[176:179]\n"
      "ds_read_b64_tr_b16 v[146:147], v29 offset:12544\n"
      "v_mfma_f32_16x16x32_bf16 v[180:183], a[228:231], v[128:131], v[180:183]\n"
      "ds_read_b64_tr_b16 v[148:149], v30 offset:10240\n"
      "v_mfma_f32_16x16x32_bf16 v[184:187], a[232:235], v[128:131], v[184:187]\n"
      "v_mfma_f32_16x16x32_bf16 v[188:191], a[236:239], v[128:131], v[188:191]\n"
      "ds_read_b64_tr_b16 v[150:151], v30 offset:12544\n"
      "v_mfma_f32_16x16x32_bf16 v[212:215], v[92:95], v[132:135], v[212:215]\n"
      "buffer_load_dwordx4 v69, s[12:15], s90 offen offset:1024 lds\n"
      "v_mfma_f32_16x16x32_bf16 v[196:199], a[224:227], v[132:135], v[196:199]\n"
      "s_waitcnt lgkmcnt(4)\n"
      "v_mfma_f32_16x16x32_bf16 v[208:211], v[92:95], v[136:139], v[208:211]\n"
      "ds_read_b64_tr_b16 v[152:153], v29 offset:10384\n"
      "v_mfma_f32_16x16x32_bf16 v[176:179], a[240:243], v[136:139], v[176:179]\n"
      "ds_read_b64_tr_b16 v[154:155], v29 offset:12688\n"
      "v_mfma_f32_16x16x32_bf16 v[180:183], a[244:247], v[136:139], v[180:183]\n"
      "ds_read_b64_tr_b16 v[156:157], v30 offset:10384\n"
      "v_mfma_f32_16x16x32_bf16 v[184:187], a[248:251], v[136:139], v[184:187]\n"
      "v_mfma_f32_16x16x32_bf16 v[188:191], a[252:255], v[136:139], v[188:191]\n"
      "ds_read_b64_tr_b16 v[158:159], v30 offset:12688\n"
      "v_mfma_f32_16x16x32_bf16 v[212:215], v[92:95], v[140:143], v[212:215]\n"
      "buffer_load_dwordx4 v70, s[12:15], s90 offen offset:2048 lds\n"
      "v_mfma_f32_16x16x32_bf16 v[196:199], a[240:243], v[140:143], v[196:199]\n"
      "s_waitcnt lgkmcnt(4)\n"
      "v_mfma_f32_16x16x32_bf16 v[212:215], v[92:95], v[148:151], v[212:215]\n"
      "v_mfma_f32_16x16x32_bf16 v[196:199], v[96:99], v[148:151], v[196:199]\n"
      "buffer_load_dwordx4 v71, s[12:15], s90 offen offset:3072 lds\n"
      "v_mfma_f32_16x16x32_bf16 v[208:211], v[92:95], v[144:147], v[208:211]\n"
      "s_add_u32 m0, s78, 45056\n"
      "s_nop 0\n"
      "buffer_load_dwordx4 v72, s[12:15], s90 offen lds\n"
      "s_waitcnt lgkmcnt(0)\n"
      "v_mfma_f32_16x16x32_bf16 v[212:215], v[92:95], v[156:159], v[212:215]\n"
      "v_mfma_f32_16x16x32_bf16 v[196:199], v[112:115], v[156:159], v[196:199]\n"
      "v_mfma_f32_16x16x32_bf16 v[208:211], v[92:95], v[152:155], v[208:211]\n"
      "v_mfma_f32_16x16x32_bf16 v[176:179], v[96:99], v[144:147], v[176:179]\n"
      "v_mfma_f32_16x16x32_bf16 v[176:179], v[112:115], v[152:155], v[176:179]\n"
      "v_mfma_f32_16x16x32_bf16 v[180:183], v[100:103], v[144:147], v[180:183]\n"
      "s_nop 3\n"
      "v_fma_f32 v196, -v216, v212, v196\n"
      "v_fma_f32 v197, -v217, v212, v197\n"
      "v_mfma_f32_16x16x32_bf16 v[180:183], v[116:119], v[152:155], v[180:183]\n"
      "v_fma_f32 v198, -v218, v212, v198\n"
      "v_fma_f32 v199, -v219, v212, v199\n"
      "v_mfma_f32_16x16x32_bf16 v[184:187], v[104:107], v[144:147], v[184:187]\n"
      "v_mul_f32 v196, v196, v232\n"
      "v_mul_f32 v197, v197, v233\n"
      "v_mfma_f32_16x16x32_bf16 v[184:187], v[120:123], v[152:155], v[184:187]\n"
      "v_mul_f32 v198, v198, v234\n"
      "v_mfma_f32_16x16x32_bf16 v[188:191], v[108:111], v[144:147], v[188:191]\n"
      "v_mul_f32 v199, v199, v235\n"
      "v_cvt_pk_bf16_f32 v16, v196, v197\n"
      "v_mfma_f32_16x16x32_bf16 v[188:191], v[124:127], v[152:155], v[188:191]\n"
      "v_cvt_pk_bf16_f32 v17, v198, v199\n"
      "ds_write_b64 v74, v[16:17]\n"
      "v_fma_f32 v176, -v216, v208, v176\n"
      "v_fma_f32 v177, -v217, v208, v177\n"
      "v_fma_f32 v178, -v218, v208, v178\n"
      "v_fma_f32 v179, -v219, v208, v179\n"
      "v_mul_f32 v176, v176, v232\n"
      "v_mul_f32 v177, v177, v233\n"
      "v_mul_f32 v178, v178, v234\n"
      "v_mul_f32 v179, v179, v235\n"
      "s_waitcnt lgkmcnt(0)\n"
      "s_barrier\n"
      "ds_read_b64 v[136:137], v44\n"
      "ds_read_b64 v[144:145], v48\n"
      "ds_read_b64 v[138:139], v45\n"
      "ds_read_b64 v[146:147], v49\n"
      "ds_read_b64 v[140:141], v46\n"
      "ds_read_b64 v[148:149], v50\n"
      "ds_read_b64 v[142:143], v47\n"
      "ds_read_b64 v[150:151], v51\n"
      "v_fma_f32 v180, -v220, v208, v180\n"
      "v_fma_f32 v181, -v221, v208, v181\n"
      "v_fma_f32 v182, -v222, v208, v182\n"
      "v_fma_f32 v183, -v223, v208, v183\n"
      "v_mul_f32 v180, v180, v236\n"
      "v_mul_f32 v181, v181, v237\n"
      "v_mul_f32 v182, v182, v238\n"
      "v_mul_f32 v183, v183, v239\n"
      "v_cvt_pk_bf16_f32 v128, v176, v177\n"
      "v_cvt_pk_bf16_f32 v129, v178, v179\n"
      "v_cvt_pk_bf16_f32 v130, v180, v181\n"
      "v_cvt_pk_bf16_f32 v131, v182, v183\n"
      "s_nop 1\n"
      "v_mfma_f32_16x16x32_bf16 v[160:163], v[36:39], v[128:131], 0\n"
      "v_fma_f32 v184, -v224, v208, v184\n"
      "v_fma_f32 v185, -v225, v208, v185\n"
      "v_fma_f32 v186, -v226, v208, v186\n"
      "s_waitcnt lgkmcnt(5)\n"
      "v_mfma_f32_16x16x32_bf16 v[172:175], v[136:139], v[36:39], 0\n"
      "v_fma_f32 v187, -v227, v208, v187\n"
      "v_fma_f32 v188, -v228, v208, v188\n"
      "v_fma_f32 v189, -v229, v208, v189\n"
      "v_fma_f32 v190, -v230, v208, v190\n"
      "s_waitcnt lgkmcnt(4)\n"
      "v_mfma_f32_16x16x32_bf16 v[168:171], v[136:139], v[144:147], 0\n"
      "v_fma_f32 v191, -v231, v208, v191\n"
      "v_mul_f32 v184, v184, v240\n"
      "v_mul_f32 v185, v185, v241\n"
      "v_mfma_f32_16x16x32_bf16 v[164:167], v[136:139], v[128:131], 0\n"
      "v_mul_f32 v186, v186, v242\n"
      "v_mul_f32 v187, v187, v243\n"
      "v_mul_f32 v188, v188, v244\n"
      "s_waitcnt lgkmcnt(1)\n"
      "v_mfma_f32_16x16x32_bf16 v[172:175], v[140:143], v[40:43], v[172:175]\n"
      "v_mul_f32 v189, v189, v245\n"
      "v_mul_f32 v190, v190, v246\n"
      "v_mul_f32 v191, v191, v247\n"
      "v_cvt_pk_bf16_f32 v132, v184, v185\n"
      "s_waitcnt lgkmcnt(0)\n"
      "v_mfma_f32_16x16x32_bf16 v[168:171], v[140:143], v[148:151], v[168:171]\n"
      "v_cvt_pk_bf16_f32 v133, v186, v187\n"
      "v_cvt_pk_bf16_f32 v134, v188, v189\n"
      "v_cvt_pk_bf16_f32 v135, v190, v191\n"
      "s_nop 1\n"
      "v_mfma_f32_16x16x32_bf16 v[160:163], v[40:43], v[132:135], v[160:163]\n"
      "v_mfma_f32_16x16x32_bf16 v[164:167], v[140:143], v[132:135], v[164:167]\n"
      "v_cmp_eq_u32 vcc, 2, v3\n"
      "v_cvt_pk_bf16_f32 v14, v172, v173\n"
      "v_cvt_pk_bf16_f32 v15, v174, v175\n"
      "v_cvt_pk_bf16_f32 v19, v170, v171\n"
      "v_cndmask_b32 v15, v15, v19, s[58:59]\n"
      "s_nop 1\n"
      "v_cvt_pk_bf16_f32 v20, v160, v161\n"
      "v_cvt_pk_bf16_f32 v21, v162, v163\n"
      "v_cvt_pk_bf16_f32 v22, v160, v161\n"
      "v_cvt_pk_bf16_f32 v23, v162, v163\n"
      "v_cvt_pk_bf16_f32 v18, v164, v165\n"
      "v_cvt_pk_bf16_f32 v12, v160, v161\n"
      "v_permlane16_swap_b32 v20, v22\n"
      "v_permlane16_swap_b32 v21, v23\n"
      "v_cvt_pk_bf16_f32 v13, v162, v163\n"
      "v_cndmask_b32 v14, v14, v18, s[58:59]\n"
      "v_cndmask_b32 v15, v15, v23, vcc\n"
      "v_cndmask_b32 v14, v14, v22, vcc\n"
      "buffer_store_dwordx4 v[12:15], v75, s[4:7], s51 offen sc1\n"
      "s_waitcnt vmcnt(6)\n"
      "s_barrier\n"
      "ds_read_b64_tr_b16 v[128:129], v29 offset:20480\n"
      "ds_read_b64_tr_b16 v[130:131], v29 offset:22784\n"
      "ds_read_b64_tr_b16 v[132:133], v30 offset:20480\n"
      "ds_read_b64_tr_b16 v[134:135], v30 offset:22784\n"
      "ds_read_b64_tr_b16 v[136:137], v29 offset:20624\n"
      "ds_read_b64_tr_b16 v[138:139], v29 offset:22928\n"
      "ds_read_b64_tr_b16 v[140:141], v30 offset:20624\n"
      "ds_read_b64_tr_b16 v[142:143], v30 offset:22928\n"
      "s_add_u32 m0, s78, 61440\n"
      "s_nop 0\n"
      "buffer_load_dwordx4 v68, s[12:15], s91 offen lds\n"
      "s_waitcnt lgkmcnt(4)\n"
      "v_mfma_f32_16x16x32_bf16 v[200:203], v[128:131], v[92:95], 0\n"
      "ds_read_b64_tr_b16 v[144:145], v29 offset:30720\n"
      "s_xor_b32 s45, s40, 0\n"
      "s_lshl_b32 s46, s45, 6\n"
      "v_mfma_f32_16x16x32_bf16 v[160:163], v[128:131], a[0:3], 0\n"
      "ds_read_b64_tr_b16 v[146:147], v29 offset:33024\n"
      "s_add_u32 s46, s46, 161024\n"
      "v_mfma_f32_16x16x32_bf16 v[164:167], v[128:131], a[4:7], 0\n"
      "ds_read_b64_tr_b16 v[148:149], v30 offset:30720\n"
      "v_lshl_add_u32 v16, v2, 2, s46\n"
      "v_mfma_f32_16x16x32_bf16 v[168:171], v[128:131], a[8:11], 0\n"
      "ds_read2_b32 v[48:49], v16 offset0:0 offset1:64\n"
      "s_lshl_b32 s46, s45, 2\n"
      "s_add_u32 s46, s46, s40\n"
      "v_mfma_f32_16x16x32_bf16 v[172:175], v[128:131], a[12:15], 0\n"
      "ds_read_b64_tr_b16 v[150:151], v30 offset:33024\n"
      "s_lshl_b32 s46, s46, 6\n"
      "s_add_u32 s46, s46, 157952\n"
      "v_mfma_f32_16x16x32_bf16 v[204:207], v[132:135], v[92:95], 0\n"
      "buffer_load_dwordx4 v69, s[12:15], s91 offen offset:1024 lds\n"
      "v_lshl_add_u32 v180, v2, 2, s46\n"
      "v_mfma_f32_16x16x32_bf16 v[192:195], v[132:135], a[0:3], 0\n"
      "s_xor_b32 s45, s40, 1\n"
      "s_lshl_b32 s46, s45, 6\n"
      "s_add_u32 s46, s46, 161024\n"
      "s_waitcnt lgkmcnt(5)\n"
      "v_mfma_f32_16x16x32_bf16 v[200:203], v[136:139], v[92:95], v[200:203]\n"
      "ds_read_b64_tr_b16 v[152:153], v29 offset:30864\n"
      "v_lshl_add_u32 v16, v2, 2, s46\n"
      "v_mfma_f32_16x16x32_bf16 v[160:163], v[136:139], a[16:19], v[160:163]\n"
      "ds_read_b64_tr_b16 v[154:155], v29 offset:33168\n"
      "ds_read2_b32 v[50:51], v16 offset0:0 offset1:64\n"
      "v_mfma_f32_16x16x32_bf16 v[164:167], v[136:139], a[20:23], v[164:167]\n"
      "ds_read_b64_tr_b16 v[156:157], v30 offset:30864\n"
      "s_lshl_b32 s46, s45, 2\n"
      "s_add_u32 s46, s46, s40\n"
      "v_mfma_f32_16x16x32_bf16 v[168:171], v[136:139], a[24:27], v[168:171]\n"
      "s_lshl_b32 s46, s46, 6\n"
      "s_add_u32 s46, s46, 157952\n"
      "v_lshl_add_u32 v181, v2, 2, s46\n"
      "v_mfma_f32_16x16x32_bf16 v[172:175], v[136:139], a[28:31], v[172:175]\n"
      "ds_read_b64_tr_b16 v[158:159], v30 offset:33168\n"
      "s_xor_b32 s45, s40, 2\n"
      "s_lshl_b32 s46, s45, 6\n"
      "v_mfma_f32_16x16x32_bf16 v[204:207], v[140:143], v[92:95], v[204:207]\n"
      "buffer_load_dwordx4 v70, s[12:15], s91 offen offset:2048 lds\n"
      "s_add_u32 s46, s46, 161024\n"
      "v_lshl_add_u32 v16, v2, 2, s46\n"
      "v_mfma_f32_16x16x32_bf16 v[192:195], v[140:143], a[16:19], v[192:195]\n"
      "ds_read2_b32 v[52:53], v16 offset0:0 offset1:64\n"
      "s_lshl_b32 s46, s45, 2\n"
      "s_waitcnt lgkmcnt(6)\n"
      "v_mfma_f32_16x16x32_bf16 v[200:203], v[144:147], v[92:95], v[200:203]\n"
      "buffer_load_dwordx4 v71, s[12:15], s91 offen offset:3072 lds\n"
      "s_add_u32 s46, s46, s40\n"
      "s_lshl_b32 s46, s46, 6\n"
      "s_add_u32 s46, s46, 157952\n"
      "v_lshl_add_u32 v182, v2, 2, s46\n"
      "v_mfma_f32_16x16x32_bf16 v[160:163], v[144:147], a[32:35], v[160:163]\n"
      "s_xor_b32 s45, s40, 3\n"
      "s_lshl_b32 s46, s45, 6\n"
      "s_add_u32 s46, s46, 161024\n"
      "v_lshl_add_u32 v16, v2, 2, s46\n"
      "v_mfma_f32_16x16x32_bf16 v[164:167], v[144:147], a[36:39], v[164:167]\n"
      "ds_read2_b32 v[54:55], v16 offset0:0 offset1:64\n"
      "s_lshl_b32 s46, s45, 2\n"
      "s_add_u32 s46, s46, s40\n"
      "s_lshl_b32 s46, s46, 6\n"
      "s_add_u32 s46, s46, 157952\n"
      "v_mfma_f32_16x16x32_bf16 v[168:171], v[144:147], a[40:43], v[168:171]\n"
      "s_add_u32 m0, s78, 65536\n"
      "s_nop 0\n"
      "buffer_load_dwordx4 v72, s[12:15], s91 offen lds\n"
      "v_lshl_add_u32 v183, v2, 2, s46\n"
      "s_lshl_b32 s45, s40, 8\n"
      "s_add_u32 s45, s45, 157952\n"
      "v_mfma_f32_16x16x32_bf16 v[172:175], v[144:147], a[44:47], v[172:175]\n"
      "v_and_b32 v16, 1, v3\n"
      "v_lshlrev_b32 v16, 7, v16\n"
      "v_lshl_add_u32 v16, v2, 2, v16\n"
      "v_mfma_f32_16x16x32_bf16 v[204:207], v[148:151], v[92:95], v[204:207]\n"
      "v_add_u32 v177, s45, v16\n"
      "v_add3_u32 v178, v4, s38, 1\n"
      "v_and_b32 v178, 31, v178\n"
      "v_mfma_f32_16x16x32_bf16 v[192:195], v[148:151], a[32:35], v[192:195]\n"
      "v_lshlrev_b32 v178, 7, v178\n"
      "s_mul_i32 s69, s39, 6144\n"
      "s_add_u32 s69, s69, 206848\n"
      "v_mul_f32 v56, 0x3fb8aa3b, v49\n"
      "s_waitcnt vmcnt(6)\n"
      "s_waitcnt lgkmcnt(0)\n"
      "s_barrier\n"
      "ds_read_b64_tr_b16 v[128:129], v31 offset:0\n"
      "ds_read_b64_tr_b16 v[130:131], v31 offset:2304\n"
      "ds_read_b64_tr_b16 v[132:133], v73 offset:0\n"
      "ds_read_b64_tr_b16 v[134:135], v73 offset:2304\n"
      "v_mfma_f32_16x16x32_bf16 v[200:203], v[152:155], v[92:95], v[200:203]\n"
      "ds_read_b64_tr_b16 v[136:137], v31 offset:144\n"
      "v_mul_f32 v57, 0x3fb8aa3b, v51\n"
      "v_mfma_f32_16x16x32_bf16 v[160:163], v[152:155], a[48:51], v[160:163]\n"
      "ds_read_b64_tr_b16 v[138:139], v31 offset:2448\n"
      "v_mul_f32 v58, 0x3fb8aa3b, v53\n"
      "v_mul_f32 v59, 0x3fb8aa3b, v55\n"
      "v_mfma_f32_16x16x32_bf16 v[164:167], v[152:155], a[52:55], v[164:167]\n"
      "ds_read_b64_tr_b16 v[140:141], v73 offset:144\n"
      "v_lshrrev_b32 v16, 2, v2\n"
      "v_mfma_f32_16x16x32_bf16 v[168:171], v[152:155], a[56:59], v[168:171]\n"
      "v_and_b32 v17, 3, v2\n"
      "v_cmp_gt_u32 vcc, 12, v2\n"
      "v_mfma_f32_16x16x32_bf16 v[172:175], v[152:155], a[60:63], v[172:175]\n"
      "ds_read_b64_tr_b16 v[142:143], v73 offset:2448\n"
      "v_lshl_add_u32 v16, v16, 3, v17\n"
      "v_add_u32 v18, 20, v17\n"
      "v_mfma_f32_16x16x32_bf16 v[204:207], v[156:159], v[92:95], v[204:207]\n"
      "s_add_u32 m0, s78, 0\n"
      "s_nop 0\n"
      "buffer_load_dwordx4 v68, s[12:15], s92 offen lds\n"
      "v_cndmask_b32 v16, v18, v16, vcc\n"
      "v_mfma_f32_16x16x32_bf16 v[192:195], v[156:159], a[48:51], v[192:195]\n"
      "v_lshlrev_b32 v24, 3, v3\n"
      "v_sub_u32 v25, v16, v24\n"
      "s_waitcnt lgkmcnt(4)\n"
      "v_mfma_f32_16x16x32_bf16 v[200:203], v[128:131], v[92:95], v[200:203]\n"
      "ds_read_b64_tr_b16 v[144:145], v31 offset:10240\n"
      "v_and_b32 v26, 1, v25\n"
      "v_mfma_f32_16x16x32_bf16 v[160:163], v[128:131], a[64:67], v[160:163]\n"
      "ds_read_b64_tr_b16 v[146:147], v31 offset:12544\n"
      "v_lshlrev_b32 v26, 4, v26\n"
      "v_mov_b32 v27, 0x3f80\n"
      "v_mfma_f32_16x16x32_bf16 v[164:167], v[128:131], a[68:71], v[164:167]\n"
      "ds_read_b64_tr_b16 v[148:149], v73 offset:10240\n"
      "v_lshlrev_b32 v26, v26, v27\n"
      "v_mfma_f32_16x16x32_bf16 v[168:171], v[128:131], a[72:75], v[168:171]\n"
      "v_lshrrev_b32 v25, 1, v25\n"
      "v_cmp_eq_u32 vcc, 0, v25\n"
      "v_mfma_f32_16x16x32_bf16 v[172:175], v[128:131], a[76:79], v[172:175]\n"
      "ds_read_b64_tr_b16 v[150:151], v73 offset:12544\n"
      "v_cndmask_b32 v60, 0, v26, vcc\n"
      "v_mfma_f32_16x16x32_bf16 v[204:207], v[132:135], v[92:95], v[204:207]\n"
      "buffer_load_dwordx4 v69, s[12:15], s92 offen offset:1024 lds\n"
      "v_cmp_eq_u32 vcc, 1, v25\n"
      "s_nop 1\n"
      "v_cndmask_b32 v61, 0, v26, vcc\n"
      "v_mfma_f32_16x16x32_bf16 v[192:195], v[132:135], a[64:67], v[192:195]\n"
      "v_cmp_eq_u32 vcc, 2, v25\n"
      "s_nop 1\n"
      "v_cndmask_b32 v62, 0, v26, vcc\n"
      "s_waitcnt lgkmcnt(4)\n"
      "v_mfma_f32_16x16x32_bf16 v[200:203], v[136:139], v[92:95], v[200:203]\n"
      "ds_read_b64_tr_b16 v[152:153], v31 offset:10384\n"
      "v_cmp_eq_u32 vcc, 3, v25\n"
      "v_mfma_f32_16x16x32_bf16 v[160:163], v[136:139], a[80:83], v[160:163]\n"
      "ds_read_b64_tr_b16 v[154:155], v31 offset:12688\n"
      "v_cndmask_b32 v63, 0, v26, vcc\n"
      "v_cmp_gt_u32 vcc, 4, v2\n"
      "v_mfma_f32_16x16x32_bf16 v[164:167], v[136:139], a[84:87], v[164:167]\n"
      "ds_read_b64_tr_b16 v[156:157], v73 offset:10384\n"
      "v_add_u32 v16, 4, v2\n"
      "v_mfma_f32_16x16x32_bf16 v[168:171], v[136:139], a[88:91], v[168:171]\n"
      "v_cndmask_b32 v16, -1, v16, vcc\n"
      "v_lshlrev_b32 v24, 3, v3\n"
      "v_mfma_f32_16x16x32_bf16 v[172:175], v[136:139], a[92:95], v[172:175]\n"
      "ds_read_b64_tr_b16 v[158:159], v73 offset:12688\n"
      "v_sub_u32 v25, v16, v24\n"
      "v_mfma_f32_16x16x32_bf16 v[204:207], v[140:143], v[92:95], v[204:207]\n"
      "buffer_load_dwordx4 v70, s[12:15], s92 offen offset:2048 lds\n"
      "v_and_b32 v26, 1, v25\n"
      "v_lshlrev_b32 v26, 4, v26\n"
      "v_mfma_f32_16x16x32_bf16 v[192:195], v[140:143], a[80:83], v[192:195]\n"
      "v_mov_b32 v27, 0x3f80\n"
      "v_lshlrev_b32 v26, v26, v27\n"
      "s_waitcnt lgkmcnt(4)\n"
      "v_mfma_f32_16x16x32_bf16 v[200:203], v[144:147], v[92:95], v[200:203]\n"
      "buffer_load_dwordx4 v71, s[12:15], s92 offen offset:3072 lds\n"
      "v_lshrrev_b32 v25, 1, v25\n"
      "v_mfma_f32_16x16x32_bf16 v[160:163], v[144:147], a[96:99], v[160:163]\n"
      "v_cmp_eq_u32 vcc, 0, v25\n"
      "s_nop 1\n"
      "v_cndmask_b32 v64, 0, v26, vcc\n"
      "v_mfma_f32_16x16x32_bf16 v[164:167], v[144:147], a[100:103], v[164:167]\n"
      "v_cmp_eq_u32 vcc, 1, v25\n"
      "s_nop 1\n"
      "v_cndmask_b32 v65, 0, v26, vcc\n"
      "v_mfma_f32_16x16x32_bf16 v[168:171], v[144:147], a[104:107], v[168:171]\n"
      "s_add_u32 m0, s78, 4096\n"
      "s_nop 0\n"
      "buffer_load_dwordx4 v72, s[12:15], s92 offen lds\n"
      "v_mfma_f32_16x16x32_bf16 v[172:175], v[144:147], a[108:111], v[172:175]\n"
      "v_cmp_eq_u32 vcc, 2, v25\n"
      "s_nop 1\n"
      "v_cndmask_b32 v66, 0, v26, vcc\n"
      "v_mfma_f32_16x16x32_bf16 v[204:207], v[148:151], v[92:95], v[204:207]\n"
      "v_cmp_eq_u32 vcc, 3, v25\n"
      "s_nop 1\n"
      "v_cndmask_b32 v67, 0, v26, vcc\n"
      "v_mfma_f32_16x16x32_bf16 v[192:195], v[148:151], a[96:99], v[192:195]\n"
      "v_cmp_eq_u32 vcc, 3, v17\n"
      "v_mov_b32 v16, 14\n"
      "s_waitcnt vmcnt(10)\n"
      "s_waitcnt vmcnt(5)\n"
      "s_waitcnt lgkmcnt(0)\n"
      "s_barrier\n"
      "s_lshl_b32 s52, s38, 7\n"
      "s_add_u32 s52, s52, s69\n"
      "buffer_store_dwordx2 v[6:7], v10, s[4:7], s52 offen sc1\n"
      "s_lshl_b32 s52, s2, 3\n"
      "s_add_u32 s52, s52, 1024\n"
      "buffer_store_dwordx2 v[6:7], v5, s[4:7], s52 offen sc1\n"
      "ds_read_b64_tr_b16 v[128:129], v31 offset:20480\n"
      "ds_read_b64_tr_b16 v[130:131], v31 offset:22784\n"
      "ds_read_b64_tr_b16 v[132:133], v73 offset:20480\n"
      "ds_read_b64_tr_b16 v[134:135], v73 offset:22784\n"
      "v_mfma_f32_16x16x32_bf16 v[200:203], v[152:155], v[92:95], v[200:203]\n"
      "ds_read_b64_tr_b16 v[136:137], v31 offset:20624\n"
      "v_add_u32 v18, 12, v2\n"
      "v_mfma_f32_16x16x32_bf16 v[160:163], v[152:155], a[112:115], v[160:163]\n"
      "ds_read_b64_tr_b16 v[138:139], v31 offset:22928\n"
      "v_cndmask_b32 v16, -1, v16, vcc\n"
      "v_cmp_gt_u32 vcc, 2, v2\n"
      "v_mfma_f32_16x16x32_bf16 v[164:167], v[152:155], a[116:119], v[164:167]\n"
      "ds_read_b64_tr_b16 v[140:141], v73 offset:20624\n"
      "v_cndmask_b32 v16, v16, v18, vcc\n"
      "v_mfma_f32_16x16x32_bf16 v[168:171], v[152:155], a[120:123], v[168:171]\n"
      "v_lshlrev_b32 v24, 3, v3\n"
      "v_sub_u32 v25, v16, v24\n"
      "v_mfma_f32_16x16x32_bf16 v[172:175], v[152:155], a[124:127], v[172:175]\n"
      "ds_read_b64_tr_b16 v[142:143], v73 offset:22928\n"
      "v_and_b32 v26, 1, v25\n"
      "v_lshlrev_b32 v26, 4, v26\n"
      "v_mfma_f32_16x16x32_bf16 v[204:207], v[156:159], v[92:95], v[204:207]\n"
      "s_add_u32 m0, s78, 20480\n"
      "s_nop 0\n"
      "buffer_load_dwordx4 v68, s[12:15], s93 offen lds\n"
      "v_mov_b32 v27, 0x3f80\n"
      "v_mfma_f32_16x16x32_bf16 v[192:195], v[156:159], a[112:115], v[192:195]\n"
      "v_lshlrev_b32 v26, v26, v27\n"
      "v_lshrrev_b32 v25, 1, v25\n"
      "s_waitcnt lgkmcnt(4)\n"
      "v_mfma_f32_16x16x32_bf16 v[200:203], v[128:131], v[92:95], v[200:203]\n"
      "ds_read_b64_tr_b16 v[144:145], v31 offset:30720\n"
      "v_cmp_eq_u32 vcc, 0, v25\n"
      "v_mfma_f32_16x16x32_bf16 v[160:163], v[128:131], a[128:131], v[160:163]\n"
      "ds_read_b64_tr_b16 v[146:147], v31 offset:33024\n"
      "v_cndmask_b32 v76, 0, v26, vcc\n"
      "v_mfma_f32_16x16x32_bf16 v[164:167], v[128:131], a[132:135], v[164:167]\n"
      "ds_read_b64_tr_b16 v[148:149], v73 offset:30720\n"
      "v_cmp_eq_u32 vcc, 1, v25\n"
      "v_mfma_f32_16x16x32_bf16 v[168:171], v[128:131], a[136:139], v[168:171]\n"
      "s_nop 0\n"
      "v_cndmask_b32 v77, 0, v26, vcc\n"
      "v_cmp_eq_u32 vcc, 2, v25\n"
      "s_nop 1\n"
      "v_cndmask_b32 v78, 0, v26, vcc\n"
      "v_mfma_f32_16x16x32_bf16 v[172:175], v[128:131], a[140:143], v[172:175]\n"
      "ds_read_b64_tr_b16 v[150:151], v73 offset:33024\n"
      "v_cmp_eq_u32 vcc, 3, v25\n"
      "v_mfma_f32_16x16x32_bf16 v[204:207], v[132:135], v[92:95], v[204:207]\n"
      "buffer_load_dwordx4 v69, s[12:15], s93 offen offset:1024 lds\n"
      "v_cndmask_b32 v79, 0, v26, vcc\n"
      "v_mfma_f32_16x16x32_bf16 v[192:195], v[132:135], a[128:131], v[192:195]\n"
      "s_lshl_b32 s45, s39, 7\n"
      "s_sub_u32 s45, s45, s53\n"
      "s_cselect_b32 s46, 1152, 0\n"
      "s_add_u32 s45, s45, s46\n"
      "s_waitcnt lgkmcnt(4)\n"
      "v_mfma_f32_16x16x32_bf16 v[200:203], v[136:139], v[92:95], v[200:203]\n"
      "ds_read_b64_tr_b16 v[152:153], v31 offset:30864\n"
      "s_lshl_b32 s45, s45, 6\n"
      "s_add_u32 s45, s45, s72\n"
      "v_mfma_f32_16x16x32_bf16 v[160:163], v[136:139], a[144:147], v[160:163]\n"
      "ds_read_b64_tr_b16 v[154:155], v31 offset:33168\n"
      "s_sub_u32 s46, 1024, s53\n"
      "s_lshl_b32 s46, s46, 6\n"
      "v_mfma_f32_16x16x32_bf16 v[164:167], v[136:139], a[148:151], v[164:167]\n"
      "ds_read_b64_tr_b16 v[156:157], v73 offset:30864\n"
      "s_add_u32 s46, s46, s72\n"
      "v_mfma_f32_16x16x32_bf16 v[168:171], v[136:139], a[152:155], v[168:171]\n"
      "v_lshrrev_b32 v12, 1, v3\n"
      "v_and_b32 v13, 6, v2\n"
      "v_mfma_f32_16x16x32_bf16 v[172:175], v[136:139], a[156:159], v[172:175]\n"
      "ds_read_b64_tr_b16 v[158:159], v73 offset:33168\n"
      "v_or_b32 v12, v12, v13\n"
      "v_mfma_f32_16x16x32_bf16 v[204:207], v[140:143], v[92:95], v[204:207]\n"
      "buffer_load_dwordx4 v70, s[12:15], s93 offen offset:2048 lds\n"
      "v_xor_b32 v13, s39, v13\n"
      "v_mfma_f32_16x16x32_bf16 v[192:195], v[140:143], a[144:147], v[192:195]\n"
      "v_and_b32 v14, 1, v3\n"
      "v_lshlrev_b32 v14, 3, v14\n"
      "s_waitcnt lgkmcnt(4)\n"
      "v_mfma_f32_16x16x32_bf16 v[200:203], v[144:147], v[92:95], v[200:203]\n"
      "buffer_load_dwordx4 v71, s[12:15], s93 offen offset:3072 lds\n"
      "v_lshl_add_u32 v14, v2, 7, v14\n"
      "v_mfma_f32_16x16x32_bf16 v[160:163], v[144:147], a[160:163], v[160:163]\n"
      "v_lshl_add_u32 v12, v12, 4, v14\n"
      "v_lshl_add_u32 v13, v13, 4, v14\n"
      "v_mfma_f32_16x16x32_bf16 v[164:167], v[144:147], a[164:167], v[164:167]\n"
      "v_add_u32 v13, s46, v13\n"
      "v_add_u32 v12, s45, v12\n"
      "v_mfma_f32_16x16x32_bf16 v[168:171], v[144:147], a[168:171], v[168:171]\n"
      "s_add_u32 m0, s78, 24576\n"
      "s_nop 0\n"
      "buffer_load_dwordx4 v72, s[12:15], s93 offen lds\n"
      "v_mfma_f32_16x16x32_bf16 v[172:175], v[144:147], a[172:175], v[172:175]\n"
      "ds_read_b64 v[44:45], v13\n"
      "v_xor_b32 v16, 0, v12\n"
      "v_mfma_f32_16x16x32_bf16 v[204:207], v[148:151], v[92:95], v[204:207]\n"
      "ds_read_b64 v[36:37], v16\n"
      "v_xor_b32 v17, 32, v12\n"
      "v_mfma_f32_16x16x32_bf16 v[192:195], v[148:151], a[160:163], v[192:195]\n"
      "ds_read_b64 v[38:39], v17\n"
      "v_xor_b32 v18, 64, v12\n"
      "s_waitcnt vmcnt(7)\n"
      "s_waitcnt lgkmcnt(0)\n"
      "s_barrier\n"
      "ds_read_b64_tr_b16 v[128:129], v29 offset:0\n"
      "ds_read_b64_tr_b16 v[130:131], v29 offset:2304\n"
      "ds_read_b64_tr_b16 v[132:133], v30 offset:0\n"
      "ds_read_b64_tr_b16 v[134:135], v30 offset:2304\n"
      "v_mfma_f32_16x16x32_bf16 v[200:203], v[152:155], v[92:95], v[200:203]\n"
      "ds_read_b64_tr_b16 v[136:137], v29 offset:144\n"
      "v_mfma_f32_16x16x32_bf16 v[160:163], v[152:155], a[176:179], v[160:163]\n"
      "ds_read_b64_tr_b16 v[138:139], v29 offset:2448\n"
      "v_mfma_f32_16x16x32_bf16 v[164:167], v[152:155], a[180:183], v[164:167]\n"
      "v_mfma_f32_16x16x32_bf16 v[168:171], v[152:155], a[184:187], v[168:171]\n"
      "ds_read_b64_tr_b16 v[140:141], v30 offset:144\n"
      "v_mfma_f32_16x16x32_bf16 v[172:175], v[152:155], a[188:191], v[172:175]\n"
      "ds_read_b64 v[40:41], v18\n"
      "v_mfma_f32_16x16x32_bf16 v[204:207], v[156:159], v[92:95], v[204:207]\n"
      "ds_read_b64_tr_b16 v[142:143], v30 offset:2448\n"
      "v_mfma_f32_16x16x32_bf16 v[192:195], v[156:159], a[176:179], v[192:195]\n"
      "s_waitcnt lgkmcnt(5)\n"
      "v_mfma_f32_16x16x32_bf16 v[200:203], v[128:131], v[92:95], v[200:203]\n"
      "ds_read_b64_tr_b16 v[144:145], v29 offset:10240\n"
      "v_mfma_f32_16x16x32_bf16 v[160:163], v[128:131], a[192:195], v[160:163]\n"
      "ds_read_b64_tr_b16 v[146:147], v29 offset:12544\n"
      "v_mfma_f32_16x16x32_bf16 v[164:167], v[128:131], a[196:199], v[164:167]\n"
      "v_mfma_f32_16x16x32_bf16 v[168:171], v[128:131], a[200:203], v[168:171]\n"
      "ds_read_b64_tr_b16 v[148:149], v30 offset:10240\n"
      "v_mfma_f32_16x16x32_bf16 v[172:175], v[128:131], a[204:207], v[172:175]\n"
      "v_xor_b32 v19, 96, v12\n"
      "v_mfma_f32_16x16x32_bf16 v[204:207], v[132:135], v[92:95], v[204:207]\n"
      "ds_read_b64_tr_b16 v[150:151], v30 offset:12544\n"
      "v_mfma_f32_16x16x32_bf16 v[192:195], v[132:135], a[192:195], v[192:195]\n"
      "s_waitcnt lgkmcnt(4)\n"
      "v_mfma_f32_16x16x32_bf16 v[200:203], v[136:139], v[92:95], v[200:203]\n"
      "ds_read_b64_tr_b16 v[152:153], v29 offset:10384\n"
      "v_mfma_f32_16x16x32_bf16 v[160:163], v[136:139], a[208:211], v[160:163]\n"
      "ds_read_b64_tr_b16 v[154:155], v29 offset:12688\n"
      "v_mfma_f32_16x16x32_bf16 v[164:167], v[136:139], a[212:215], v[164:167]\n"
      "v_mfma_f32_16x16x32_bf16 v[168:171], v[136:139], a[216:219], v[168:171]\n"
      "ds_read_b64_tr_b16 v[156:157], v30 offset:10384\n"
      "v_mfma_f32_16x16x32_bf16 v[172:175], v[136:139], a[220:223], v[172:175]\n"
      "ds_read_b64 v[42:43], v19\n"
      "v_mfma_f32_16x16x32_bf16 v[204:207], v[140:143], v[92:95], v[204:207]\n"
      "ds_read_b64_tr_b16 v[158:159], v30 offset:12688\n"
      "v_mfma_f32_16x16x32_bf16 v[192:195], v[140:143], a[208:211], v[192:195]\n"
      "s_waitcnt lgkmcnt(5)\n"
      "v_mfma_f32_16x16x32_bf16 v[200:203], v[144:147], v[92:95], v[200:203]\n"
      "v_mfma_f32_16x16x32_bf16 v[160:163], v[144:147], a[224:227], v[160:163]\n"
      "v_mfma_f32_16x16x32_bf16 v[164:167], v[144:147], a[228:231], v[164:167]\n"
      "v_mfma_f32_16x16x32_bf16 v[168:171], v[144:147], a[232:235], v[168:171]\n"
      "v_mov_b32 v46, v48\n"
      "v_mfma_f32_16x16x32_bf16 v[172:175], v[144:147], a[236:239], v[172:175]\n"
      "v_mfma_f32_16x16x32_bf16 v[204:207], v[148:151], v[92:95], v[204:207]\n"
      "v_mfma_f32_16x16x32_bf16 v[192:195], v[148:151], a[224:227], v[192:195]\n"
      "s_waitcnt vmcnt(0)\n"
      "s_waitcnt lgkmcnt(0)\n"
      "s_barrier\n"
      "ds_read_b64_tr_b16 v[128:129], v29 offset:20480\n"
      "ds_read_b64_tr_b16 v[130:131], v29 offset:22784\n"
      "ds_read_b64_tr_b16 v[132:133], v30 offset:20480\n"
      "ds_read_b64_tr_b16 v[134:135], v30 offset:22784\n"
      "v_mfma_f32_16x16x32_bf16 v[200:203], v[152:155], v[92:95], v[200:203]\n"
      "ds_read_b64_tr_b16 v[136:137], v29 offset:20624\n"
      "v_mfma_f32_16x16x32_bf16 v[160:163], v[152:155], a[240:243], v[160:163]\n"
      "ds_read_b64_tr_b16 v[138:139], v29 offset:22928\n"
      "v_mfma_f32_16x16x32_bf16 v[164:167], v[152:155], a[244:247], v[164:167]\n"
      "v_mfma_f32_16x16x32_bf16 v[168:171], v[152:155], a[248:251], v[168:171]\n"
      "ds_read_b64_tr_b16 v[140:141], v30 offset:20624\n"
      "v_mfma_f32_16x16x32_bf16 v[172:175], v[152:155], a[252:255], v[172:175]\n"
      "v_mfma_f32_16x16x32_bf16 v[204:207], v[156:159], v[92:95], v[204:207]\n"
      "ds_read_b64_tr_b16 v[142:143], v30 offset:22928\n"
      "v_mfma_f32_16x16x32_bf16 v[192:195], v[156:159], a[240:243], v[192:195]\n"
      "s_waitcnt lgkmcnt(4)\n"
      "v_mfma_f32_16x16x32_bf16 v[204:207], v[132:135], v[92:95], v[204:207]\n"
      "v_mfma_f32_16x16x32_bf16 v[192:195], v[132:135], v[96:99], v[192:195]\n"
      "v_mfma_f32_16x16x32_bf16 v[200:203], v[128:131], v[92:95], v[200:203]\n"
      "s_waitcnt lgkmcnt(0)\n"
      "v_mfma_f32_16x16x32_bf16 v[204:207], v[140:143], v[92:95], v[204:207]\n"
      "v_mfma_f32_16x16x32_bf16 v[192:195], v[140:143], v[112:115], v[192:195]\n"
      "v_mfma_f32_16x16x32_bf16 v[200:203], v[136:139], v[92:95], v[200:203]\n"
      "s_mul_i32 s44, s40, 8\n"
      "s_mov_b32 exec_hi, 0\n"
      "s_lshl_b32 s44, 255, s44\n"
      "s_and_b32 exec_lo, s44, 2147483647\n"
      "v_mov_b32 v26, 0\n"
      "v_mov_b32 v27, 0\n"
      "buffer_atomic_or_x2 v[26:27], v178, s[4:7], s69 offen sc0 sc1\n"
      "s_mov_b64 exec, -1\n"
      "v_mfma_f32_16x16x32_bf16 v[160:163], v[128:131], v[96:99], v[160:163]\n"
      "v_mfma_f32_16x16x32_bf16 v[160:163], v[136:139], v[112:115], v[160:163]\n"
      "v_mfma_f32_16x16x32_bf16 v[164:167], v[128:131], v[100:103], v[164:167]\n"
      "v_fma_f32 v192, -v48, v204, v192\n"
      "v_fma_f32 v193, -v48, v205, v193\n"
      "v_fma_f32 v194, -v48, v206, v194\n"
      "v_fma_f32 v195, -v48, v207, v195\n"
      "v_mul_f32 v12, v192, v56\n"
      "v_mfma_f32_16x16x32_bf16 v[164:167], v[136:139], v[116:119], v[164:167]\n"
      "v_mul_f32 v13, v193, v56\n"
      "v_mul_f32 v14, v194, v56\n"
      "v_mul_f32 v15, v195, v56\n"
      "v_exp_f32 v12, v12 clamp\n"
      "v_mfma_f32_16x16x32_bf16 v[168:171], v[128:131], v[104:107], v[168:171]\n"
      "v_exp_f32 v13, v13 clamp\n"
      "v_exp_f32 v14, v14 clamp\n"
      "v_exp_f32 v15, v15 clamp\n"
      "v_mfma_f32_16x16x32_bf16 v[168:171], v[136:139], v[120:123], v[168:171]\n"
      "v_max_f32 v192, 0, v192\n"
      "v_max_f32 v193, 0, v193\n"
      "v_max_f32 v194, 0, v194\n"
      "v_max_f32 v195, 0, v195\n"
      "v_fma_f32 v192, v192, v49, v12\n"
      "v_mfma_f32_16x16x32_bf16 v[172:175], v[128:131], v[108:111], v[172:175]\n"
      "v_fma_f32 v193, v193, v49, v13\n"
      "v_fma_f32 v194, v194, v49, v14\n"
      "v_fma_f32 v195, v195, v49, v15\n"
      "v_add_f32 v192, 0x358637bd, v192\n"
      "v_add_f32 v193, 0x358637bd, v193\n"
      "v_mfma_f32_16x16x32_bf16 v[172:175], v[136:139], v[124:127], v[172:175]\n"
      "v_add_f32 v194, 0x358637bd, v194\n"
      "v_add_f32 v195, 0x358637bd, v195\n"
      "v_cvt_pk_bf16_f32 v16, v192, v193\n"
      "v_cvt_pk_bf16_f32 v17, v194, v195\n"
      "ds_write2_b32 v177, v16, v17 offset0:0 offset1:16\n"
      "s_mul_i32 s44, s40, 8\n"
      "s_mov_b32 exec_hi, 0\n"
      "s_lshl_b32 s44, 255, s44\n"
      "s_and_b32 exec_lo, s44, 2147483647\n"
      "s_waitcnt vmcnt(0)\n"
      "v_cmp_ne_u64 vcc, s[42:43], v[26:27]\n"
      "s_cbranch_vccz .LD5_%=\n"
      "s_mov_b32 s44, 0\n"
      ".LP4_%=:\n"
      "s_mov_b64 exec, vcc\n"
      "v_mov_b32 v26, 0\n"
      "v_mov_b32 v27, 0\n"
      "buffer_atomic_or_x2 v[26:27], v178, s[4:7], s69 offen sc0 sc1\n"
      "s_waitcnt vmcnt(0)\n"
      "v_cmp_ne_u64 vcc, s[42:43], v[26:27]\n"
      "s_cbranch_vccz .LD5_%=\n"
      "s_add_u32 s44, s44, 1\n"
      "s_cmp_lt_u32 s44, s41\n"
      "s_cbranch_scc1 .LP4_%=\n"
      "s_mov_b32 s41, 0\n"
      ".LD5_%=:\n"
      "s_mov_b64 exec, -1\n"
      "s_waitcnt lgkmcnt(0)\n"
      "s_barrier\n"
      "buffer_load_dwordx4 a[128:131], v75, s[4:7], s79 offen offset:0 sc1\n"
      "buffer_load_dwordx4 a[132:135], v75, s[4:7], s79 offen offset:768 sc1\n"
      "buffer_load_dwordx4 a[136:139], v75, s[4:7], s79 offen offset:1536 sc1\n"
      "v_fma_f32 v160, -v48, v200, v160\n"
      "buffer_load_dwordx4 a[140:143], v75, s[4:7], s79 offen offset:2304 sc1\n"
      "v_fma_f32 v161, -v48, v201, v161\n"
      "v_fma_f32 v162, -v48, v202, v162\n"
      "v_fma_f32 v163, -v48, v203, v163\n"
      "v_mul_f32 v12, v160, v56\n"
      "v_mul_f32 v13, v161, v56\n"
      "v_mul_f32 v14, v162, v56\n"
      "v_mul_f32 v15, v163, v56\n"
      "v_exp_f32 v12, v12 clamp\n"
      "v_exp_f32 v13, v13 clamp\n"
      "v_exp_f32 v14, v14 clamp\n"
      "v_exp_f32 v15, v15 clamp\n"
      "v_max_f32 v160, 0, v160\n"
      "buffer_load_dwordx4 a[144:147], v75, s[4:7], s79 offen offset:3072 sc1\n"
      "v_max_f32 v161, 0, v161\n"
      "v_max_f32 v162, 0, v162\n"
      "v_max_f32 v163, 0, v163\n"
      "v_fma_f32 v160, v160, v49, v12\n"
      "v_fma_f32 v161, v161, v49, v13\n"
      "v_fma_f32 v162, v162, v49, v14\n"
      "v_fma_f32 v163, v163, v49, v15\n"
      "v_add_f32 v160, 0x358637bd, v160\n"
      "v_add_f32 v161, 0x358637bd, v161\n"
      "v_add_f32 v162, 0x358637bd, v162\n"
      "v_add_f32 v163, 0x358637bd, v163\n"
      "v_cvt_pk_bf16_f32 v128, v160, v161\n"
      "s_add_u32 s79, s79, 3840\n"
      "buffer_load_dwordx4 a[148:151], v75, s[4:7], s79 offen offset:0 sc1\n"
      "v_cvt_pk_bf16_f32 v129, v162, v163\n"
      "v_mov_b32 v131, 0\n"
      "v_fma_f32 v164, -v50, v200, v164\n"
      "v_fma_f32 v165, -v50, v201, v165\n"
      "v_fma_f32 v166, -v50, v202, v166\n"
      "v_fma_f32 v167, -v50, v203, v167\n"
      "v_mul_f32 v12, v164, v57\n"
      "v_mul_f32 v13, v165, v57\n"
      "v_mul_f32 v14, v166, v57\n"
      "v_mul_f32 v15, v167, v57\n"
      "v_exp_f32 v12, v12 clamp\n"
      "v_exp_f32 v13, v13 clamp\n"
      "buffer_load_dwordx4 a[152:155], v75, s[4:7], s79 offen offset:768 sc1\n"
      "v_exp_f32 v14, v14 clamp\n"
      "v_exp_f32 v15, v15 clamp\n"
      "v_max_f32 v164, 0, v164\n"
      "v_max_f32 v165, 0, v165\n"
      "v_max_f32 v166, 0, v166\n"
      "v_max_f32 v167, 0, v167\n"
      "v_fma_f32 v164, v164, v51, v12\n"
      "v_fma_f32 v165, v165, v51, v13\n"
      "v_fma_f32 v166, v166, v51, v14\n"
      "v_fma_f32 v167, v167, v51, v15\n"
      "v_add_f32 v164, 0x358637bd, v164\n"
      "v_add_f32 v165, 0x358637bd, v165\n"
      "buffer_load_dwordx4 a[156:159], v75, s[4:7], s79 offen offset:1536 sc1\n"
      "v_add_f32 v166, 0x358637bd, v166\n"
      "v_add_f32 v167, 0x358637bd, v167\n"
      "v_cvt_pk_bf16_f32 v132, v164, v165\n"
      "v_cvt_pk_bf16_f32 v133, v166, v167\n"
      "v_mov_b32 v135, 0\n"
      "v_fma_f32 v168, -v52, v200, v168\n"
      "v_fma_f32 v169, -v52, v201, v169\n"
      "v_fma_f32 v170, -v52, v202, v170\n"
      "v_fma_f32 v171, -v52, v203, v171\n"
      "v_mul_f32 v12, v168, v58\n"
      "v_mul_f32 v13, v169, v58\n"
      "v_mul_f32 v14, v170, v58\n"
      "buffer_load_dwordx4 a[160:163], v75, s[4:7], s79 offen offset:2304 sc1\n"
      "v_mul_f32 v15, v171, v58\n"
      "v_exp_f32 v12, v12 clamp\n"
      "v_exp_f32 v13, v13 clamp\n"
      "v_exp_f32 v14, v14 clamp\n"
      "v_exp_f32 v15, v15 clamp\n"
      "v_max_f32 v168, 0, v168\n"
      "v_max_f32 v169, 0, v169\n"
      "v_max_f32 v170, 0, v170\n"
      "v_max_f32 v171, 0, v171\n"
      "v_fma_f32 v168, v168, v53, v12\n"
      "v_fma_f32 v169, v169, v53, v13\n"
      "v_fma_f32 v170, v170, v53, v14\n"
      "buffer_load_dwordx4 a[164:167], v75, s[4:7], s79 offen offset:3072 sc1\n"
      "v_fma_f32 v171, v171, v53, v15\n"
      "v_add_f32 v168, 0x358637bd, v168\n"
      "v_add_f32 v169, 0x358637bd, v169\n"
      "v_add_f32 v170, 0x358637bd, v170\n"
      "v_add_f32 v171, 0x358637bd, v171\n"
      "v_cvt_pk_bf16_f32 v136, v168, v169\n"
      "v_cvt_pk_bf16_f32 v137, v170, v171\n"
      "v_mov_b32 v139, 0\n"
      "v_fma_f32 v172, -v54, v200, v172\n"
      "v_fma_f32 v173, -v54, v201, v173\n"
      "v_fma_f32 v174, -v54, v202, v174\n"
      "v_fma_f32 v175, -v54, v203, v175\n"
      "s_add_u32 s79, s79, 3840\n"
      "buffer_load_dwordx4 a[168:171], v75, s[4:7], s79 offen offset:0 sc1\n"
      "v_mul_f32 v12, v172, v59\n"
      "v_mul_f32 v13, v173, v59\n"
      "v_mul_f32 v14, v174, v59\n"
      "v_mul_f32 v15, v175, v59\n"
      "v_exp_f32 v12, v12 clamp\n"
      "v_exp_f32 v13, v13 clamp\n"
      "v_exp_f32 v14, v14 clamp\n"
      "v_exp_f32 v15, v15 clamp\n"
      "v_max_f32 v172, 0, v172\n"
      "v_max_f32 v173, 0, v173\n"
      "v_max_f32 v174, 0, v174\n"
      "v_max_f32 v175, 0, v175\n"
      "buffer_load_dwordx4 a[172:175], v75, s[4:7], s79 offen offset:768 sc1\n"
      "v_fma_f32 v172, v172, v55, v12\n"
      "v_fma_f32 v173, v173, v55, v13\n"
      "v_fma_f32 v174, v174, v55, v14\n"
      "v_fma_f32 v175, v175, v55, v15\n"
      "v_add_f32 v172, 0x358637bd, v172\n"
      "v_add_f32 v173, 0x358637bd, v173\n"
      "v_add_f32 v174, 0x358637bd, v174\n"
      "v_add_f32 v175, 0x358637bd, v175\n"
      "v_cvt_pk_bf16_f32 v140, v172, v173\n"
      "v_cvt_pk_bf16_f32 v141, v174, v175\n"
      "v_mov_b32 v143, 0\n"
      "ds_read_b32 v130, v180\n"
      "ds_read_b32 v134, v181\n"
      "ds_read_b32 v138, v182\n"
      "ds_read_b32 v142, v183\n"
      "s_waitcnt vmcnt(11)\n"
      "v_mfma_f32_16x16x32_bf16 v[160:163], v[60:63], a[128:131], 0\n"
      "buffer_load_dwordx4 a[176:179], v75, s[4:7], s79 offen offset:1536 sc1\n"
      "v_mfma_f32_16x16x32_bf16 v[164:167], v[64:67], a[128:131], 0\n"
      "s_mul_i32 s64, s38, 73728\n"
      "v_mul_u32_u24 v12, 18432, v3\n"
      "v_mfma_f32_16x16x32_bf16 v[168:171], a[128:131], v[76:79], 0\n"
      "s_waitcnt vmcnt(11)\n"
      "v_lshl_add_u32 v12, v2, 4, v12\n"
      "v_mfma_f32_16x16x32_bf16 v[160:163], v[60:63], a[132:135], v[160:163]\n"
      "buffer_load_dwordx4 a[180:183], v75, s[4:7], s79 offen offset:2304 sc1\n"
      "v_mfma_f32_16x16x32_bf16 v[164:167], v[64:67], a[132:135], v[164:167]\n"
      "s_xor_b32 s45, s40, 0\n"
      "s_lshl_b32 s45, s45, 8\n"
      "v_mfma_f32_16x16x32_bf16 v[168:171], a[132:135], v[76:79], v[168:171]\n"
      "s_waitcnt vmcnt(11)\n"
      "v_add_u32 v80, s45, v12\n"
      "v_mfma_f32_16x16x32_bf16 v[160:163], v[60:63], a[136:139], v[160:163]\n"
      "buffer_load_dwordx4 a[184:187], v75, s[4:7], s79 offen offset:3072 sc1\n"
      "v_mfma_f32_16x16x32_bf16 v[164:167], v[64:67], a[136:139], v[164:167]\n"
      "s_add_u32 s45, s45, 160000\n"
      "v_mfma_f32_16x16x32_bf16 v[168:171], a[136:139], v[76:79], v[168:171]\n"
      "s_waitcnt vmcnt(11)\n"
      "v_lshl_add_u32 v88, v2, 4, s45\n"
      "s_xor_b32 s45, s40, 1\n"
      "v_mfma_f32_16x16x32_bf16 v[160:163], v[60:63], a[140:143], v[160:163]\n"
      "s_add_u32 s79, s79, 3840\n"
      "buffer_load_dwordx4 a[188:191], v75, s[4:7], s79 offen offset:0 sc1\n"
      "v_mfma_f32_16x16x32_bf16 v[164:167], v[64:67], a[140:143], v[164:167]\n"
      "s_lshl_b32 s45, s45, 8\n"
      "v_mfma_f32_16x16x32_bf16 v[168:171], a[140:143], v[76:79], v[168:171]\n"
      "s_waitcnt vmcnt(11)\n"
      "v_add_u32 v81, s45, v12\n"
      "v_mfma_f32_16x16x32_bf16 v[160:163], v[60:63], a[144:147], v[160:163]\n"
      "buffer_load_dwordx4 a[192:195], v75, s[4:7], s79 offen offset:768 sc1\n"
      "v_mfma_f32_16x16x32_bf16 v[164:167], v[64:67], a[144:147], v[164:167]\n"
      "s_add_u32 s45, s45, 160000\n"
      "v_lshl_add_u32 v89, v2, 4, s45\n"
      "v_mfma_f32_16x16x32_bf16 v[168:171], a[144:147], v[76:79], v[168:171]\n"
      "s_waitcnt vmcnt(11)\n"
      "s_xor_b32 s45, s40, 2\n"
      "s_lshl_b32 s45, s45, 8\n"
      "v_mfma_f32_16x16x32_bf16 v[160:163], v[60:63], a[148:151], v[160:163]\n"
      "buffer_load_dwordx4 a[196:199], v75, s[4:7], s79 offen offset:1536 sc1\n"
      "v_mfma_f32_16x16x32_bf16 v[164:167], v[64:67], a[148:151], v[164:167]\n"
      "v_add_u32 v82, s45, v12\n"
      "v_mfma_f32_16x16x32_bf16 v[168:171], a[148:151], v[76:79], v[168:171]\n"
      "s_waitcnt vmcnt(11)\n"
      "s_add_u32 s45, s45, 160000\n"
      "v_mfma_f32_16x16x32_bf16 v[160:163], v[60:63], a[152:155], v[160:163]\n"
      "buffer_load_dwordx4 a[200:203], v75, s[4:7], s79 offen offset:2304 sc1\n"
      "v_mfma_f32_16x16x32_bf16 v[164:167], v[64:67], a[152:155], v[164:167]\n"
      "v_lshl_add_u32 v90, v2, 4, s45\n"
      "s_xor_b32 s45, s40, 3\n"
      "v_mfma_f32_16x16x32_bf16 v[168:171], a[152:155], v[76:79], v[168:171]\n"
      "s_waitcnt vmcnt(11)\n"
      "s_lshl_b32 s45, s45, 8\n"
      "v_add_u32 v83, s45, v12\n"
      "v_mfma_f32_16x16x32_bf16 v[160:163], v[60:63], a[156:159], v[160:163]\n"
      "buffer_load_dwordx4 a[204:207], v75, s[4:7], s79 offen offset:3072 sc1\n"
      "v_mfma_f32_16x16x32_bf16 v[164:167], v[64:67], a[156:159], v[164:167]\n"
      "s_add_u32 s45, s45, 160000\n"
      "v_mfma_f32_16x16x32_bf16 v[168:171], a[156:159], v[76:79], v[168:171]\n"
      "s_waitcnt vmcnt(11)\n"
      "v_lshl_add_u32 v91, v2, 4, s45\n"
      "v_mfma_f32_16x16x32_bf16 v[160:163], v[60:63], a[160:163], v[160:163]\n"
      "s_add_u32 s79, s79, 3840\n"
      "buffer_load_dwordx4 a[208:211], v75, s[4:7], s79 offen offset:0 sc1\n"
      "v_mfma_f32_16x16x32_bf16 v[164:167], v[64:67], a[160:163], v[164:167]\n"
      "v_and_b32 v13, 1, v3\n"
      "v_mfma_f32_16x16x32_bf16 v[168:171], a[160:163], v[76:79], v[168:171]\n"
      "s_waitcnt vmcnt(11)\n"
      "v_xor_b32 v13, s40, v13\n"
      "v_mfma_f32_16x16x32_bf16 v[160:163], v[60:63], a[164:167], v[160:163]\n"
      "buffer_load_dwordx4 a[212:215], v75, s[4:7], s79 offen offset:768 sc1\n"
      "v_mfma_f32_16x16x32_bf16 v[164:167], v[64:67], a[164:167], v[164:167]\n"
      "v_lshl_add_u32 v13, v13, 4, v2\n"
      "v_mfma_f32_16x16x32_bf16 v[168:171], a[164:167], v[76:79], v[168:171]\n"
      "s_waitcnt vmcnt(11)\n"
      "v_lshrrev_b32 v14, 1, v3\n"
      "v_mfma_f32_16x16x32_bf16 v[160:163], v[60:63], a[168:171], v[160:163]\n"
      "buffer_load_dwordx4 a[216:219], v75, s[4:7], s79 offen offset:1536 sc1\n"
      "v_mfma_f32_16x16x32_bf16 v[164:167], v[64:67], a[168:171], v[164:167]\n"
      "v_lshlrev_b32 v14, 10, v14\n"
      "v_mfma_f32_16x16x32_bf16 v[168:171], a[168:171], v[76:79], v[168:171]\n"
      "s_waitcnt vmcnt(11)\n"
      "v_lshl_add_u32 v84, v13, 4, v14\n"
      "v_mfma_f32_16x16x32_bf16 v[160:163], v[60:63], a[172:175], v[160:163]\n"
      "buffer_load_dwordx4 a[220:223], v75, s[4:7], s79 offen offset:2304 sc1\n"
      "v_mfma_f32_16x16x32_bf16 v[164:167], v[64:67], a[172:175], v[164:167]\n"
      "s_lshl_b32 s45, s40, 11\n"
      "s_add_u32 s45, s45, s64\n"
      "v_mfma_f32_16x16x32_bf16 v[168:171], a[172:175], v[76:79], v[168:171]\n"
      "s_waitcnt vmcnt(11)\n"
      "v_add_u32 v84, s45, v84\n"
      "v_mfma_f32_16x16x32_bf16 v[160:163], v[60:63], a[176:179], v[160:163]\n"
      "buffer_load_dwordx4 a[224:227], v75, s[4:7], s79 offen offset:3072 sc1\n"
      "v_mfma_f32_16x16x32_bf16 v[164:167], v[64:67], a[176:179], v[164:167]\n"
      "v_xor_b32 v85, 0x200, v84\n"
      "v_mfma_f32_16x16x32_bf16 v[168:171], a[176:179], v[76:79], v[168:171]\n"
      "s_waitcnt vmcnt(11)\n"
      "v_cmp_gt_u32 vcc, 16, v4\n"
      "s_lshl_b32 s45, s40, 8\n"
      "v_mfma_f32_16x16x32_bf16 v[160:163], v[60:63], a[180:183], v[160:163]\n"
      "s_add_u32 s79, s79, 3840\n"
      "buffer_load_dwordx4 a[228:231], v75, s[4:7], s79 offen offset:0 sc1\n"
      "v_mfma_f32_16x16x32_bf16 v[164:167], v[64:67], a[180:183], v[164:167]\n"
      "s_add_u32 s45, s45, s64\n"
      "v_mfma_f32_16x16x32_bf16 v[168:171], a[180:183], v[76:79], v[168:171]\n"
      "s_waitcnt vmcnt(11)\n"
      "v_add_u32 v13, s45, v1\n"
      "v_mfma_f32_16x16x32_bf16 v[160:163], v[60:63], a[184:187], v[160:163]\n"
      "buffer_load_dwordx4 a[232:235], v75, s[4:7], s79 offen offset:768 sc1\n"
      "v_mfma_f32_16x16x32_bf16 v[164:167], v[64:67], a[184:187], v[164:167]\n"
      "v_mov_b32 v74, 1073741824\n"
      "v_mfma_f32_16x16x32_bf16 v[168:171], a[184:187], v[76:79], v[168:171]\n"
      "s_waitcnt vmcnt(11)\n"
      "v_cndmask_b32 v74, v74, v13, vcc\n"
      "s_lshl_b32 s45, s40, 2\n"
      "v_mfma_f32_16x16x32_bf16 v[160:163], v[60:63], a[188:191], v[160:163]\n"
      "buffer_load_dwordx4 a[236:239], v75, s[4:7], s79 offen offset:1536 sc1\n"
      "v_mfma_f32_16x16x32_bf16 v[164:167], v[64:67], a[188:191], v[164:167]\n"
      "s_add_u32 s45, s45, -1024\n"
      "v_mfma_f32_16x16x32_bf16 v[168:171], a[188:191], v[76:79], v[168:171]\n"
      "s_waitcnt vmcnt(11)\n"
      "v_add_u32 v20, s45, v88\n"
      "v_mfma_f32_16x16x32_bf16 v[160:163], v[60:63], a[192:195], v[160:163]\n"
      "buffer_load_dwordx4 a[240:243], v75, s[4:7], s79 offen offset:2304 sc1\n"
      "v_mfma_f32_16x16x32_bf16 v[164:167], v[64:67], a[192:195], v[164:167]\n"
      "v_add_u32 v21, s45, v89\n"
      "v_add_u32 v22, s45, v90\n"
      "v_mfma_f32_16x16x32_bf16 v[168:171], a[192:195], v[76:79], v[168:171]\n"
      "s_waitcnt vmcnt(11)\n"
      "v_add_u32 v23, s45, v91\n"
      "v_mfma_f32_16x16x32_bf16 v[160:163], v[60:63], a[196:199], v[160:163]\n"
      "buffer_load_dwordx4 a[244:247], v75, s[4:7], s79 offen offset:3072 sc1\n"
      "v_mfma_f32_16x16x32_bf16 v[164:167], v[64:67], a[196:199], v[164:167]\n"
      "v_add_u32 v24, -1024, v88\n"
      "v_mfma_f32_16x16x32_bf16 v[168:171], a[196:199], v[76:79], v[168:171]\n"
      "s_waitcnt vmcnt(11)\n"
      "s_mov_b32 s77, s78\n"
      "v_mfma_f32_16x16x32_bf16 v[160:163], v[60:63], a[200:203], v[160:163]\n"
      "s_add_u32 s79, s79, 3840\n"
      "buffer_load_dwordx4 a[248:251], v75, s[4:7], s79 offen offset:0 sc1\n"
      "v_mfma_f32_16x16x32_bf16 v[164:167], v[64:67], a[200:203], v[164:167]\n"
      "v_subrev_u32 v32, 61440, v32\n"
      "v_mfma_f32_16x16x32_bf16 v[168:171], a[200:203], v[76:79], v[168:171]\n"
      "s_waitcnt vmcnt(11)\n"
      "v_subrev_u32 v33, 61440, v33\n"
      "v_mfma_f32_16x16x32_bf16 v[160:163], v[60:63], a[204:207], v[160:163]\n"
      "buffer_load_dwordx4 a[252:255], v75, s[4:7], s79 offen offset:768 sc1\n"
      "v_mfma_f32_16x16x32_bf16 v[164:167], v[64:67], a[204:207], v[164:167]\n"
      "v_subrev_u32 v34, 61440, v34\n"
      "v_mfma_f32_16x16x32_bf16 v[168:171], a[204:207], v[76:79], v[168:171]\n"
      "s_waitcnt vmcnt(11)\n"
      "v_subrev_u32 v35, 61440, v35\n"
      "v_mfma_f32_16x16x32_bf16 v[160:163], v[60:63], a[208:211], v[160:163]\n"
      "s_mov_b32 s52, 1024\n"
      "buffer_load_dwordx4 v[48:51], v1, s[4:7], s52 offen sc1\n"
      "buffer_load_dwordx4 v[52:55], v1, s[4:7], s52 offen offset:1024 sc1\n"
      "v_mfma_f32_16x16x32_bf16 v[164:167], v[64:67], a[208:211], v[164:167]\n"
      "v_mfma_f32_16x16x32_bf16 v[168:171], a[208:211], v[76:79], v[168:171]\n"
      "s_waitcnt vmcnt(12)\n"
      "v_mfma_f32_16x16x32_bf16 v[160:163], v[60:63], a[212:215], v[160:163]\n"
      "v_mfma_f32_16x16x32_bf16 v[164:167], v[64:67], a[212:215], v[164:167]\n"
      "v_mfma_f32_16x16x32_bf16 v[168:171], a[212:215], v[76:79], v[168:171]\n"
      "s_waitcnt vmcnt(11)\n"
      "v_mfma_f32_16x16x32_bf16 v[160:163], v[60:63], a[216:219], v[160:163]\n"
      "v_mfma_f32_16x16x32_bf16 v[164:167], v[64:67], a[216:219], v[164:167]\n"
      "v_mfma_f32_16x16x32_bf16 v[168:171], a[216:219], v[76:79], v[168:171]\n"
      "s_waitcnt vmcnt(10)\n"
      "v_mfma_f32_16x16x32_bf16 v[160:163], v[60:63], a[220:223], v[160:163]\n"
      "v_mfma_f32_16x16x32_bf16 v[164:167], v[64:67], a[220:223], v[164:167]\n"
      "v_mfma_f32_16x16x32_bf16 v[168:171], a[220:223], v[76:79], v[168:171]\n"
      "s_waitcnt vmcnt(9)\n"
      "v_mfma_f32_16x16x32_bf16 v[160:163], v[60:63], a[224:227], v[160:163]\n"
      "v_mfma_f32_16x16x32_bf16 v[164:167], v[64:67], a[224:227], v[164:167]\n"
      "v_mfma_f32_16x16x32_bf16 v[168:171], a[224:227], v[76:79], v[168:171]\n"
      "s_waitcnt vmcnt(8)\n"
      "v_mfma_f32_16x16x32_bf16 v[160:163], v[60:63], a[228:231], v[160:163]\n"
      "v_mfma_f32_16x16x32_bf16 v[164:167], v[64:67], a[228:231], v[164:167]\n"
      "v_mfma_f32_16x16x32_bf16 v[168:171], a[228:231], v[76:79], v[168:171]\n"
      "s_waitcnt vmcnt(7)\n"
      "v_mfma_f32_16x16x32_bf16 v[160:163], v[60:63], a[232:235], v[160:163]\n"
      "v_mfma_f32_16x16x32_bf16 v[164:167], v[64:67], a[232:235], v[164:167]\n"
      "v_mfma_f32_16x16x32_bf16 v[168:171], a[232:235], v[76:79], v[168:171]\n"
      "s_waitcnt vmcnt(6)\n"
      "v_mfma_f32_16x16x32_bf16 v[160:163], v[60:63], a[236:239], v[160:163]\n"
      "v_mfma_f32_16x16x32_bf16 v[164:167], v[64:67], a[236:239], v[164:167]\n"
      "v_mfma_f32_16x16x32_bf16 v[168:171], a[236:239], v[76:79], v[168:171]\n"
      "s_waitcnt vmcnt(5)\n"
      "v_mfma_f32_16x16x32_bf16 v[160:163], v[60:63], a[240:243], v[160:163]\n"
      "v_mfma_f32_16x16x32_bf16 v[164:167], v[64:67], a[240:243], v[164:167]\n"
      "v_mfma_f32_16x16x32_bf16 v[168:171], a[240:243], v[76:79], v[168:171]\n"
      "s_waitcnt vmcnt(4)\n"
      "v_mfma_f32_16x16x32_bf16 v[160:163], v[60:63], a[244:247], v[160:163]\n"
      "v_mfma_f32_16x16x32_bf16 v[164:167], v[64:67], a[244:247], v[164:167]\n"
      "v_mfma_f32_16x16x32_bf16 v[168:171], a[244:247], v[76:79], v[168:171]\n"
      "s_waitcnt vmcnt(3)\n"
      "v_mfma_f32_16x16x32_bf16 v[160:163], v[60:63], a[248:251], v[160:163]\n"
      "v_mfma_f32_16x16x32_bf16 v[164:167], v[64:67], a[248:251], v[164:167]\n"
      "v_mfma_f32_16x16x32_bf16 v[168:171], a[248:251], v[76:79], v[168:171]\n"
      "s_waitcnt vmcnt(2)\n"
      "v_mfma_f32_16x16x32_bf16 v[160:163], v[60:63], a[252:255], v[160:163]\n"
      "v_mfma_f32_16x16x32_bf16 v[164:167], v[64:67], a[252:255], v[164:167]\n"
      "v_mfma_f32_16x16x32_bf16 v[168:171], a[252:255], v[76:79], v[168:171]\n"
      "s_mul_i32 s46, s39, 9216\n"
      "s_add_u32 s46, s46, 1353728\n"
      "s_add_u32 s47, s46, 8192\n"
      "v_mov_b32 v63, 0\n"
      "v_mov_b32 v67, 0\n"
      "s_nop 0\n"
      "v_cvt_pk_bf16_f32 v60, v160, v161\n"
      "v_cvt_pk_bf16_f32 v61, v162, v163\n"
      "v_cvt_pk_bf16_f32 v62, v164, v165\n"
      "v_cvt_pk_bf16_f32 v66, v166, v167\n"
      "v_cvt_pk_bf16_f32 v64, v168, v169\n"
      "v_cvt_pk_bf16_f32 v65, v170, v171\n"
      "s_waitcnt lgkmcnt(3)\n"
      "s_nop 0\n"
      "v_mfma_f32_16x16x32_bf16 v[144:147], v[64:67], v[128:131], 0\n"
      "s_waitcnt lgkmcnt(2)\n"
      "v_mfma_f32_16x16x32_bf16 v[148:151], v[64:67], v[132:135], 0\n"
      "s_waitcnt lgkmcnt(1)\n"
      "v_mfma_f32_16x16x32_bf16 v[152:155], v[64:67], v[136:139], 0\n"
      "s_waitcnt lgkmcnt(0)\n"
      "v_mfma_f32_16x16x32_bf16 v[156:159], v[64:67], v[140:143], 0\n"
      "v_mfma_f32_16x16x32_bf16 v[208:211], v[60:63], v[128:131], 0\n"
      "v_mfma_f32_16x16x32_bf16 v[212:215], v[60:63], v[132:135], 0\n"
      "v_mfma_f32_16x16x32_bf16 v[216:219], v[60:63], v[136:139], 0\n"
      "v_mfma_f32_16x16x32_bf16 v[220:223], v[60:63], v[140:143], 0\n"
      "v_add_f32 v147, 0x358637bd, v147\n"
      "v_add_f32 v151, 0x358637bd, v151\n"
      "v_add_f32 v155, 0x358637bd, v155\n"
      "s_nop 0\n"
      "v_add_f32 v159, 0x358637bd, v159\n"
      "v_rcp_f32 v147, v147\n"
      "v_rcp_f32 v151, v151\n"
      "v_rcp_f32 v155, v155\n"
      "v_rcp_f32 v159, v159\n"
      "v_mul_f32 v144, v144, v147\n"
      "v_mul_f32 v148, v148, v151\n"
      "v_mul_f32 v152, v152, v155\n"
      "v_mul_f32 v156, v156, v159\n"
      "v_mul_f32 v145, v145, v147\n"
      "v_mul_f32 v149, v149, v151\n"
      "v_mul_f32 v153, v153, v155\n"
      "v_mul_f32 v157, v157, v159\n"
      "v_cvt_pk_bf16_f32 v144, v144, v145\n"
      "v_cvt_pk_bf16_f32 v148, v148, v149\n"
      "v_cvt_pk_bf16_f32 v152, v152, v153\n"
      "v_cvt_pk_bf16_f32 v156, v156, v157\n"
      "s_mov_b64 exec, 0xffff\n"
      "ds_write_b32 v20, v144\n"
      "ds_write_b32 v21, v148\n"
      "ds_write_b32 v22, v152\n"
      "ds_write_b32 v23, v156\n"
      "s_mov_b64 exec, -1\n"
      "v_mul_f32 v208, v208, v147\n"
      "v_mul_f32 v209, v209, v147\n"
      "v_mul_f32 v210, v210, v147\n"
      "v_mul_f32 v211, v211, v147\n"
      "v_mul_f32 v212, v212, v151\n"
      "v_mul_f32 v213, v213, v151\n"
      "v_mul_f32 v214, v214, v151\n"
      "v_mul_f32 v215, v215, v151\n"
      "v_cvt_pk_bf16_f32 v12, v208, v209\n"
      "v_cvt_pk_bf16_f32 v13, v210, v211\n"
      "v_cvt_pk_bf16_f32 v14, v212, v213\n"
      "v_cvt_pk_bf16_f32 v15, v214, v215\n"
      "s_waitcnt lgkmcnt(0)\n"
      "s_barrier\n"
      "ds_read_b128 v[20:23], v24\n"
      "v_permlane16_swap_b32 v12, v14\n"
      "v_permlane16_swap_b32 v13, v15\n"
      "buffer_store_dwordx4 v[12:15], v84, s[4:7], s46 offen sc1\n"
      "v_mul_f32 v216, v216, v155\n"
      "v_mul_f32 v217, v217, v155\n"
      "v_mul_f32 v218, v218, v155\n"
      "v_mul_f32 v219, v219, v155\n"
      "v_mul_f32 v220, v220, v159\n"
      "v_mul_f32 v221, v221, v159\n"
      "v_mul_f32 v222, v222, v159\n"
      "v_mul_f32 v223, v223, v159\n"
      "v_cvt_pk_bf16_f32 v16, v216, v217\n"
      "v_cvt_pk_bf16_f32 v17, v218, v219\n"
      "v_cvt_pk_bf16_f32 v18, v220, v221\n"
      "v_cvt_pk_bf16_f32 v19, v222, v223\n"
      "s_nop 0\n"
      "v_permlane16_swap_b32 v16, v18\n"
      "v_permlane16_swap_b32 v17, v19\n"
      "buffer_store_dwordx4 v[16:19], v85, s[4:7], s46 offen sc1\n"
      "s_waitcnt lgkmcnt(0)\n"
      "buffer_store_dwordx4 v[20:23], v74, s[4:7], s47 offen sc1\n"
      "s_waitcnt vmcnt(3)\n"
      "s_mov_b32 s44, 0\n"
      ".LP6_%=:\n"
      "v_cmp_ne_u64 vcc, s[42:43], v[48:49]\n"
      "v_cmp_ne_u64 s[58:59], s[42:43], v[50:51]\n"
      "s_or_b64 vcc, vcc, s[58:59]\n"
      "v_cmp_ne_u64 s[58:59], s[42:43], v[52:53]\n"
      "s_or_b64 vcc, vcc, s[58:59]\n"
      "v_cmp_ne_u64 s[58:59], s[42:43], v[54:55]\n"
      "s_or_b64 vcc, vcc, s[58:59]\n"
      "s_cbranch_scc0 .LD7_%=\n"
      "s_mov_b32 s52, 1024\n"
      "buffer_load_dwordx4 v[48:51], v1, s[4:7], s52 offen sc1\n"
      "buffer_load_dwordx4 v[52:55], v1, s[4:7], s52 offen offset:1024 sc1\n"
      "s_waitcnt vmcnt(0)\n"
      "s_add_u32 s44, s44, 1\n"
      "s_cmp_lt_u32 s44, s41\n"
      "s_cbranch_scc1 .LP6_%=\n"
      "s_mov_b32 s41, 0\n"
      ".LD7_%=:\n"
      "s_lshl_b32 s65, s39, 2\n"
      "s_add_u32 s65, s65, s40\n"
      "s_mul_i32 s65, s65, 18432\n"
      "s_add_u32 s65, s65, 10790912\n"
      "s_mul_i32 s66, s40, 18432\n"
      "s_add_u32 m0, s66, 0\n"
      "s_add_u32 s53, s65, 0\n"
      "buffer_load_dwordx4 v1, s[4:7], s53 offen sc1 lds\n"
      "s_mul_i32 s67, s38, 6144\n"
      "v_add3_u32 v176, v4, s39, 1\n"
      "v_and_b32 v176, 7, v176\n"
      "v_lshlrev_b32 v176, 8, v176\n"
      "s_add_u32 m0, s66, 1024\n"
      "s_add_u32 s53, s65, 1024\n"
      "buffer_load_dwordx4 v1, s[4:7], s53 offen sc1 lds\n"
      "v_add_u32 v176, s67, v176\n"
      "s_lshl_b32 s45, s39, 8\n"
      "s_add_u32 s67, s67, s45\n"
      "s_add_u32 s68, s64, s66\n"
      "s_add_u32 m0, s66, 2048\n"
      "s_add_u32 s53, s65, 2048\n"
      "buffer_load_dwordx4 v1, s[4:7], s53 offen sc1 lds\n"
      "s_add_u32 s45, s39, 64\n"
      "s_lshl_b32 s45, s45, 10\n"
      "v_add_u32 v74, s45, v74\n"
      "v_add_u32 v31, s64, v80\n"
      "s_add_u32 m0, s66, 3072\n"
      "s_add_u32 s53, s65, 3072\n"
      "buffer_load_dwordx4 v1, s[4:7], s53 offen sc1 lds\n"
      "v_and_b32 v25, 1, v3\n"
      "v_lshrrev_b32 v26, 1, v3\n"
      "v_lshl_add_u32 v25, v25, 1, v26\n"
      "s_add_u32 m0, s66, 4096\n"
      "s_add_u32 s53, s65, 4096\n"
      "buffer_load_dwordx4 v1, s[4:7], s53 offen sc1 lds\n"
      "v_lshlrev_b32 v25, 6, v25\n"
      "v_add_u32 v25, v25, v2\n"
      "s_lshl_b32 s45, s39, 13\n"
      "s_add_u32 s45, s45, s64\n"
      "s_lshl_b32 s48, s40, 8\n"
      "s_add_u32 m0, s66, 5120\n"
      "s_add_u32 s53, s65, 5120\n"
      "buffer_load_dwordx4 v1, s[4:7], s53 offen sc1 lds\n"
      "s_add_u32 s45, s45, s48\n"
      "v_lshl_add_u32 v84, v25, 4, s45\n"
      "s_lshl_b32 s72, s2, 3\n"
      "s_mul_i32 s70, s40, 5120\n"
      "s_min_u32 s70, s70, 13312\n"
      "s_add_u32 m0, s66, 6144\n"
      "s_add_u32 s53, s65, 6144\n"
      "buffer_load_dwordx4 v1, s[4:7], s53 offen sc1 lds\n"
      "v_add_u32 v30, 36864, v1\n"
      "v_cmp_gt_u32 vcc, 8, v2\n"
      "v_lshl_add_u32 v25, v3, 3, v2\n"
      "s_add_u32 m0, s66, 7168\n"
      "s_add_u32 s53, s65, 7168\n"
      "buffer_load_dwordx4 v1, s[4:7], s53 offen sc1 lds\n"
      "v_add_u32 v26, 32, v3\n"
      "v_cndmask_b32 v25, v26, v25, vcc\n"
      "v_lshlrev_b32 v25, 4, v25\n"
      "s_add_u32 m0, s66, 8192\n"
      "s_add_u32 s53, s65, 8192\n"
      "buffer_load_dwordx4 v1, s[4:7], s53 offen sc1 lds\n"
      "v_add_u32 v87, 94208, v25\n"
      "s_cmp_eq_u32 s2, 0\n"
      "s_cselect_b32 s45, 0, 1073741824\n"
      "v_or_b32 v24, s45, v5\n"
      "s_lshr_b32 s48, s39, 1\n"
      "s_add_u32 m0, s66, 9216\n"
      "s_add_u32 s53, s65, 9216\n"
      "buffer_load_dwordx4 v1, s[4:7], s53 offen sc1 lds\n"
      "s_mul_i32 s48, s48, 18432\n"
      "s_and_b32 s49, s39, 1\n"
      "s_lshl_b32 s49, s49, 7\n"
      "s_add_u32 s48, s48, s49\n"
      "s_add_u32 s48, s48, 11380736\n"
      "v_cmp_gt_u32 vcc, 36, v4\n"
      "s_add_u32 m0, s66, 10240\n"
      "s_add_u32 s53, s65, 10240\n"
      "buffer_load_dwordx4 v1, s[4:7], s53 offen sc1 lds\n"
      "v_lshrrev_b32 v25, 3, v4\n"
      "v_add_u32 v27, 548416, v1\n"
      "v_mov_b32 v86, 1073741824\n"
      "s_add_u32 m0, s66, 11264\n"
      "s_add_u32 s53, s65, 11264\n"
      "buffer_load_dwordx4 v1, s[4:7], s53 offen sc1 lds\n"
      "v_cndmask_b32 v86, v86, v27, vcc\n"
      "v_cmp_gt_u32 vcc, 32, v4\n"
      "v_and_b32 v26, 7, v4\n"
      "s_add_u32 m0, s66, 12288\n"
      "s_add_u32 s53, s65, 12288\n"
      "buffer_load_dwordx4 v1, s[4:7], s53 offen sc1 lds\n"
      "v_lshl_add_u32 v25, v25, 4, v26\n"
      "v_lshl_add_u32 v25, v25, 4, s48\n"
      "v_cndmask_b32 v86, v86, v25, vcc\n"
      "s_waitcnt vmcnt(13)\n"
      "s_barrier\n"
      "s_add_u32 s45, s67, 8192\n"
      "buffer_store_dwordx2 v[6:7], v10, s[4:7], s45 offen sc1\n"
      "s_add_u32 m0, s66, 13312\n"
      "s_add_u32 s53, s65, 13312\n"
      "buffer_load_dwordx4 v1, s[4:7], s53 offen sc1 lds\n"
      "v_lshlrev_b32 v196, 16, v36\n"
      "v_and_b32 v197, 0xffff0000, v36\n"
      "v_lshlrev_b32 v198, 16, v37\n"
      "v_and_b32 v199, 0xffff0000, v37\n"
      "v_lshlrev_b32 v200, 16, v38\n"
      "v_and_b32 v201, 0xffff0000, v38\n"
      "v_lshlrev_b32 v202, 16, v39\n"
      "v_and_b32 v203, 0xffff0000, v39\n"
      "v_lshlrev_b32 v204, 16, v40\n"
      "v_and_b32 v205, 0xffff0000, v40\n"
      "v_lshlrev_b32 v206, 16, v41\n"
      "v_and_b32 v207, 0xffff0000, v41\n"
      "v_lshlrev_b32 v208, 16, v42\n"
      "s_add_u32 m0, s66, 14336\n"
      "s_add_u32 s53, s65, 14336\n"
      "buffer_load_dwordx4 v1, s[4:7], s53 offen sc1 lds\n"
      "v_and_b32 v209, 0xffff0000, v42\n"
      "v_lshlrev_b32 v210, 16, v43\n"
      "v_and_b32 v211, 0xffff0000, v43\n"
      "v_lshlrev_b32 v212, 16, v44\n"
      "v_and_b32 v213, 0xffff0000, v44\n"
      "v_lshlrev_b32 v214, 16, v45\n"
      "v_and_b32 v215, 0xffff0000, v45\n"
      "v_sub_f32 v196, v196, v46\n"
      "v_sub_f32 v197, v197, v46\n"
      "v_sub_f32 v198, v198, v46\n"
      "v_sub_f32 v199, v199, v46\n"
      "v_sub_f32 v200, v200, v46\n"
      "v_sub_f32 v201, v201, v46\n"
      "s_add_u32 m0, s66, 15360\n"
      "s_add_u32 s53, s65, 15360\n"
      "buffer_load_dwordx4 v1, s[4:7], s53 offen sc1 lds\n"
      "v_sub_f32 v202, v202, v46\n"
      "v_sub_f32 v203, v203, v46\n"
      "v_sub_f32 v204, v204, v46\n"
      "v_sub_f32 v205, v205, v46\n"
      "v_sub_f32 v206, v206, v46\n"
      "v_sub_f32 v207, v207, v46\n"
      "v_sub_f32 v208, v208, v46\n"
      "v_sub_f32 v209, v209, v46\n"
      "v_sub_f32 v210, v210, v46\n"
      "v_sub_f32 v211, v211, v46\n"
      "v_sub_f32 v212, v212, v46\n"
      "v_sub_f32 v213, v213, v46\n"
      "s_add_u32 m0, s66, 16384\n"
      "s_add_u32 s53, s65, 16384\n"
      "buffer_load_dwordx4 v1, s[4:7], s53 offen sc1 lds\n"
      "v_sub_f32 v214, v214, v46\n"
      "v_sub_f32 v215, v215, v46\n"
      "v_cmp_gt_u32 vcc, 32, v4\n"
      "s_nop 0\n"
      "s_nop 0\n"
      "v_cndmask_b32 v212, 0, v212, vcc\n"
      "v_cndmask_b32 v213, 0, v213, vcc\n"
      "v_cndmask_b32 v214, 0, v214, vcc\n"
      "v_cndmask_b32 v215, 0, v215, vcc\n"
      "v_mov_b32 v29, v46\n"
      "s_lshl_b32 s45, s2, 2\n"
      "s_add_u32 s45, s45, s40\n"
      "s_add_u32 s45, s45, 486\n"
      "s_mul_i32 s46, s45, 6473\n"
      "s_lshr_b32 s46, s46, 20\n"
      "s_mul_i32 s47, s46, 162\n"
      "s_sub_u32 s47, s45, s47\n"
      "s_mul_i32 s48, s47, 57\n"
      "s_lshr_b32 s48, s48, 9\n"
      "s_add_u32 m0, s66, 17408\n"
      "s_add_u32 s53, s65, 17408\n"
      "buffer_load_dwordx4 v1, s[4:7], s53 offen sc1 lds\n"
      "s_mul_i32 s49, s48, 9\n"
      "s_sub_u32 s49, s47, s49\n"
      "s_mul_i32 s76, s46, 663552\n"
      "s_mul_i32 s50, s49, 72\n"
      "s_add_u32 s50, s50, s48\n"
      "s_lshl_b32 s50, s50, 10\n"
      "s_add_u32 s76, s76, s50\n"
      "s_lshl_b32 s74, s48, 3\n"
      "s_lshl_b32 s49, s49, 7\n"
      "s_sub_u32 s50, s46, 3\n"
      "s_mul_i32 s50, s50, 1152\n"
      "s_sub_u32 s51, s46, 11\n"
      "s_mul_i32 s51, s51, 576\n"
      "s_mov_b32 s73, 1152\n"
      "s_cmp_lt_u32 s46, 11\n"
      "s_cselect_b32 s50, s50, 0\n"
      "s_cselect_b32 s51, 0, s51\n"
      "s_cselect_b64 s[16:17], s[30:31], s[32:33]\n"
      "s_cselect_b32 s73, 9216, s73\n"
      "s_add_u32 s49, s49, s50\n"
      "s_add_u32 s74, s74, s51\n"
      "s_add_u32 s75, s74, 4\n"
      "s_mov_b32 s18, 5308416\n"
      "s_and_b32 s17, s17, 0xffff\n"
      "s_mov_b32 s19, 0x20000\n"
      "s_mul_i32 s74, s74, s73\n"
      "s_add_u32 m0, s70, 94208\n"
      "s_add_u32 s53, s70, 0\n"
      "buffer_load_dwordx4 v86, s[4:7], s53 offen sc1 lds\n"
      "s_mul_i32 s75, s75, s73\n"
      "s_add_u32 s74, s74, s49\n"
      "s_add_u32 s75, s75, s49\n"
      "s_cmp_lt_u32 s45, 3078\n"
      "s_cselect_b32 s18, s18, 0\n"
      "s_cselect_b32 s76, s76, 1073741824\n"
      "s_cselect_b32 s74, s74, 0\n"
      "s_cselect_b32 s75, s75, 0\n"
      "v_bfe_u32 v26, v4, 3, 1\n"
      "v_mul_u32_u24 v26, 144, v26\n"
      "v_add_u32 v26, v26, v3\n"
      "v_mul_lo_u32 v11, v26, s73\n"
      "v_and_b32 v26, 7, v4\n"
      "v_lshlrev_b32 v27, 1, v3\n"
      "v_xor_b32 v26, v26, v27\n"
      "v_lshl_add_u32 v11, v26, 4, v11\n"
      "s_mul_i32 s73, s73, 288\n"
      "s_mov_b32 s51, 8192\n"
      "s_mul_i32 s44, s40, 2\n"
      "s_mov_b32 exec_hi, 0\n"
      "s_lshl_b32 s44, 3, s44\n"
      "s_and_b32 exec_lo, s44, 127\n"
      "v_mov_b32 v26, 0\n"
      "v_mov_b32 v27, 0\n"
      "buffer_atomic_or_x2 v[26:27], v176, s[4:7], s51 offen sc0 sc1\n"
      "s_mov_b64 exec, -1\n"
      "s_add_u32 m0, s70, 95232\n"
      "s_add_u32 s53, s70, 1024\n"
      "buffer_load_dwordx4 v86, s[4:7], s53 offen sc1 lds\n"
      "s_add_u32 m0, s70, 96256\n"
      "s_add_u32 s53, s70, 2048\n"
      "buffer_load_dwordx4 v86, s[4:7], s53 offen sc1 lds\n"
      "s_add_u32 m0, s70, 97280\n"
      "s_add_u32 s53, s70, 3072\n"
      "buffer_load_dwordx4 v86, s[4:7], s53 offen sc1 lds\n"
      "s_add_u32 m0, s70, 98304\n"
      "s_add_u32 s53, s70, 4096\n"
      "buffer_load_dwordx4 v86, s[4:7], s53 offen sc1 lds\n"
      "buffer_store_dwordx2 v[6:7], v24, s[4:7], 0 offen sc1\n"
      "s_mov_b32 m0, s77\n"
      "v_add_u32 v28, s76, v1\n"
      "buffer_load_dwordx4 v11, s[16:19], s74 offen lds\n"
      "s_add_u32 m0, s77, 1024\n"
      "s_add_u32 s74, s74, s73\n"
      "buffer_load_dwordx4 v11, s[16:19], s75 offen lds\n"
      "s_add_u32 m0, s77, 2048\n"
      "s_add_u32 s75, s75, s73\n"
      "buffer_load_dwordx4 v11, s[16:19], s74 offen lds\n"
      "s_add_u32 m0, s77, 3072\n"
      "s_nop 0\n"
      "buffer_load_dwordx4 v11, s[16:19], s75 offen lds\n"
      "s_mul_i32 s44, s40, 2\n"
      "s_mov_b32 exec_hi, 0\n"
      "s_lshl_b32 s44, 3, s44\n"
      "s_and_b32 exec_lo, s44, 127\n"
      "s_waitcnt vmcnt(9)\n"
      "v_cmp_ne_u64 vcc, s[42:43], v[26:27]\n"
      "s_cbranch_vccz .LD9_%=\n"
      "s_mov_b32 s44, 0\n"
      ".LP8_%=:\n"
      "s_mov_b64 exec, vcc\n"
      "v_mov_b32 v26, 0\n"
      "v_mov_b32 v27, 0\n"
      "buffer_atomic_or_x2 v[26:27], v176, s[4:7], s51 offen sc0 sc1\n"
      "s_waitcnt vmcnt(0)\n"
      "v_cmp_ne_u64 vcc, s[42:43], v[26:27]\n"
      "s_cbranch_vccz .LD9_%=\n"
      "s_add_u32 s44, s44, 1\n"
      "s_cmp_lt_u32 s44, s41\n"
      "s_cbranch_scc1 .LP8_%=\n"
      "s_mov_b32 s41, 0\n"
      ".LD9_%=:\n"
      "s_mov_b64 exec, -1\n"
      "s_waitcnt vmcnt(5)\n"
      "s_barrier\n"
      "s_mov_b32 s69, 1353728\n"
      "buffer_load_dwordx4 v[56:59], v31, s[4:7], s69 offen offset:0 sc1\n"
      "buffer_load_dwordx4 v[60:63], v31, s[4:7], s69 offen offset:1024 sc1\n"
      "buffer_load_dwordx4 v[64:67], v31, s[4:7], s69 offen offset:2048 sc1\n"
      "ds_read_b128 a[0:3], v1 offset:0\n"
      "buffer_load_dwordx4 v[68:71], v31, s[4:7], s69 offen offset:3072 sc1\n"
      "ds_read_b128 a[4:7], v1 offset:18432\n"
      "s_lshr_b32 s48, s39, 1\n"
      "ds_read_b128 a[8:11], v30 offset:0\n"
      "s_mul_i32 s48, s48, 18432\n"
      "ds_read_b128 a[12:15], v30 offset:18432\n"
      "s_and_b32 s49, s39, 1\n"
      "ds_read_b128 v[180:183], v87 offset:0\n"
      "s_mov_b32 s69, 1357824\n"
      "buffer_load_dwordx4 v[76:79], v31, s[4:7], s69 offen offset:0 sc1\n"
      "ds_read_b128 a[16:19], v1 offset:1024\n"
      "ds_read_b128 a[20:23], v1 offset:19456\n"
      "s_lshl_b32 s49, s49, 7\n"
      "ds_read_b128 a[24:27], v30 offset:1024\n"
      "s_add_u32 s48, s48, s49\n"
      "ds_read_b128 a[28:31], v30 offset:19456\n"
      "buffer_load_dwordx4 v[92:95], v31, s[4:7], s69 offen offset:1024 sc1\n"
      "ds_read_b128 v[184:187], v87 offset:1024\n"
      "s_add_u32 s48, s48, 12044288\n"
      "ds_read_b128 a[32:35], v1 offset:2048\n"
      "v_cmp_gt_u32 vcc, 36, v4\n"
      "ds_read_b128 a[36:39], v1 offset:20480\n"
      "ds_read_b128 a[40:43], v30 offset:2048\n"
      "buffer_load_dwordx4 v[128:131], v31, s[4:7], s69 offen offset:2048 sc1\n"
      "ds_read_b128 a[44:47], v30 offset:20480\n"
      "ds_read_b128 v[188:191], v87 offset:2048\n"
      "v_lshrrev_b32 v25, 3, v4\n"
      "ds_read_b128 a[48:51], v1 offset:3072\n"
      "v_add_u32 v27, 548352, v1\n"
      "ds_read_b128 a[52:55], v1 offset:21504\n"
      "buffer_load_dwordx4 v[132:135], v31, s[4:7], s69 offen offset:3072 sc1\n"
      "ds_read_b128 a[56:59], v30 offset:3072\n"
      "ds_read_b128 a[60:63], v30 offset:21504\n"
      "v_mov_b32 v86, 1073741824\n"
      "ds_read_b128 v[192:195], v87 offset:3072\n"
      "ds_read_b128 a[64:67], v1 offset:4096\n"
      "s_mov_b32 s69, 1361920\n"
      "buffer_load_dwordx4 v[136:139], v31, s[4:7], s69 offen offset:0 sc1\n"
      "ds_read_b128 a[68:71], v1 offset:22528\n"
      "ds_read_b128 a[72:75], v30 offset:4096\n"
      "v_cndmask_b32 v86, v86, v27, vcc\n"
      "ds_read_b128 a[76:79], v30 offset:22528\n"
      "ds_read_b128 a[80:83], v1 offset:5120\n"
      "buffer_load_dwordx4 v[140:143], v31, s[4:7], s69 offen offset:1024 sc1\n"
      "ds_read_b128 a[84:87], v1 offset:23552\n"
      "v_cmp_gt_u32 vcc, 32, v4\n"
      "ds_read_b128 a[88:91], v30 offset:5120\n"
      "ds_read_b128 a[92:95], v30 offset:23552\n"
      "v_and_b32 v26, 7, v4\n"
      "ds_read_b128 a[96:99], v1 offset:6144\n"
      "buffer_load_dwordx4 v[144:147], v31, s[4:7], s69 offen offset:2048 sc1\n"
      "ds_read_b128 a[100:103], v1 offset:24576\n"
      "ds_read_b128 a[104:107], v30 offset:6144\n"
      "v_lshl_add_u32 v25, v25, 4, v26\n"
      "ds_read_b128 a[108:111], v30 offset:24576\n"
      "ds_read_b128 a[112:115], v1 offset:7168\n"
      "buffer_load_dwordx4 v[148:151], v31, s[4:7], s69 offen offset:3072 sc1\n"
      "ds_read_b128 a[116:119], v1 offset:25600\n"
      "v_lshl_add_u32 v25, v25, 4, s48\n"
      "ds_read_b128 a[120:123], v30 offset:7168\n"
      "ds_read_b128 a[124:127], v30 offset:25600\n"
      "v_cndmask_b32 v86, v86, v25, vcc\n"
      "s_waitcnt vmcnt(11) lgkmcnt(15)\n"
      "v_mfma_f32_16x16x32_bf16 v[212:215], v[180:183], v[56:59], v[212:215]\n"
      "ds_read_b128 v[180:183], v87 offset:4096\n"
      "ds_read_b128 a[128:131], v1 offset:8192\n"
      "v_mfma_f32_16x16x32_bf16 v[196:199], a[0:3], v[56:59], v[196:199]\n"
      "ds_read_b128 a[132:135], v1 offset:26624\n"
      "v_mfma_f32_16x16x32_bf16 v[200:203], a[4:7], v[56:59], v[200:203]\n"
      "ds_read_b128 a[136:139], v30 offset:8192\n"
      "v_mfma_f32_16x16x32_bf16 v[204:207], a[8:11], v[56:59], v[204:207]\n"
      "ds_read_b128 a[140:143], v30 offset:26624\n"
      "v_mfma_f32_16x16x32_bf16 v[208:211], a[12:15], v[56:59], v[208:211]\n"
      "s_mov_b32 s69, 1366016\n"
      "buffer_load_dwordx4 v[152:155], v31, s[4:7], s69 offen offset:0 sc1\n"
      "s_waitcnt vmcnt(11) lgkmcnt(15)\n"
      "v_mfma_f32_16x16x32_bf16 v[212:215], v[184:187], v[60:63], v[212:215]\n"
      "ds_read_b128 v[184:187], v87 offset:5120\n"
      "ds_read_b128 a[144:147], v1 offset:9216\n"
      "v_mfma_f32_16x16x32_bf16 v[196:199], a[16:19], v[60:63], v[196:199]\n"
      "ds_read_b128 a[148:151], v1 offset:27648\n"
      "v_mfma_f32_16x16x32_bf16 v[200:203], a[20:23], v[60:63], v[200:203]\n"
      "ds_read_b128 a[152:155], v30 offset:9216\n"
      "v_mfma_f32_16x16x32_bf16 v[204:207], a[24:27], v[60:63], v[204:207]\n"
      "ds_read_b128 a[156:159], v30 offset:27648\n"
      "v_mfma_f32_16x16x32_bf16 v[208:211], a[28:31], v[60:63], v[208:211]\n"
      "buffer_load_dwordx4 v[156:159], v31, s[4:7], s69 offen offset:1024 sc1\n"
      "s_waitcnt vmcnt(11) lgkmcnt(15)\n"
      "v_mfma_f32_16x16x32_bf16 v[212:215], v[188:191], v[64:67], v[212:215]\n"
      "ds_read_b128 v[188:191], v87 offset:6144\n"
      "ds_read_b128 a[160:163], v1 offset:10240\n"
      "v_mfma_f32_16x16x32_bf16 v[196:199], a[32:35], v[64:67], v[196:199]\n"
      "ds_read_b128 a[164:167], v1 offset:28672\n"
      "v_mfma_f32_16x16x32_bf16 v[200:203], a[36:39], v[64:67], v[200:203]\n"
      "ds_read_b128 a[168:171], v30 offset:10240\n"
      "v_mfma_f32_16x16x32_bf16 v[204:207], a[40:43], v[64:67], v[204:207]\n"
      "ds_read_b128 a[172:175], v30 offset:28672\n"
      "v_mfma_f32_16x16x32_bf16 v[208:211], a[44:47], v[64:67], v[208:211]\n"
      "buffer_load_dwordx4 v[160:163], v31, s[4:7], s69 offen offset:2048 sc1\n"
      "s_waitcnt vmcnt(11) lgkmcnt(15)\n"
      "v_mfma_f32_16x16x32_bf16 v[212:215], v[192:195], v[68:71], v[212:215]\n"
      "ds_read_b128 v[192:195], v87 offset:7168\n"
      "ds_read_b128 a[176:179], v1 offset:11264\n"
      "v_mfma_f32_16x16x32_bf16 v[196:199], a[48:51], v[68:71], v[196:199]\n"
      "ds_read_b128 a[180:183], v1 offset:29696\n"
      "v_mfma_f32_16x16x32_bf16 v[200:203], a[52:55], v[68:71], v[200:203]\n"
      "ds_read_b128 a[184:187], v30 offset:11264\n"
      "v_mfma_f32_16x16x32_bf16 v[204:207], a[56:59], v[68:71], v[204:207]\n"
      "ds_read_b128 a[188:191], v30 offset:29696\n"
      "v_mfma_f32_16x16x32_bf16 v[208:211], a[60:63], v[68:71], v[208:211]\n"
      "buffer_load_dwordx4 v[164:167], v31, s[4:7], s69 offen offset:3072 sc1\n"
      "s_waitcnt vmcnt(11) lgkmcnt(15)\n"
      "v_mfma_f32_16x16x32_bf16 v[212:215], v[180:183], v[76:79], v[212:215]\n"
      "ds_read_b128 v[180:183], v87 offset:8192\n"
      "ds_read_b128 a[192:195], v1 offset:12288\n"
      "v_mfma_f32_16x16x32_bf16 v[196:199], a[64:67], v[76:79], v[196:199]\n"
      "ds_read_b128 a[196:199], v1 offset:30720\n"
      "v_mfma_f32_16x16x32_bf16 v[200:203], a[68:71], v[76:79], v[200:203]\n"
      "ds_read_b128 a[200:203], v30 offset:12288\n"
      "v_mfma_f32_16x16x32_bf16 v[204:207], a[72:75], v[76:79], v[204:207]\n"
      "ds_read_b128 a[204:207], v30 offset:30720\n"
      "v_mfma_f32_16x16x32_bf16 v[208:211], a[76:79], v[76:79], v[208:211]\n"
      "s_mov_b32 s69, 1370112\n"
      "buffer_load_dwordx4 v[168:171], v31, s[4:7], s69 offen offset:0 sc1\n"
      "s_waitcnt vmcnt(11) lgkmcnt(15)\n"
      "v_mfma_f32_16x16x32_bf16 v[212:215], v[184:187], v[92:95], v[212:215]\n"
      "ds_read_b128 v[184:187], v87 offset:9216\n"
      "ds_read_b128 a[208:211], v1 offset:13312\n"
      "v_mfma_f32_16x16x32_bf16 v[196:199], a[80:83], v[92:95], v[196:199]\n"
      "ds_read_b128 a[212:215], v1 offset:31744\n"
      "v_mfma_f32_16x16x32_bf16 v[200:203], a[84:87], v[92:95], v[200:203]\n"
      "ds_read_b128 a[216:219], v30 offset:13312\n"
      "v_mfma_f32_16x16x32_bf16 v[204:207], a[88:91], v[92:95], v[204:207]\n"
      "ds_read_b128 a[220:223], v30 offset:31744\n"
      "v_mfma_f32_16x16x32_bf16 v[208:211], a[92:95], v[92:95], v[208:211]\n"
      "buffer_load_dwordx4 v[172:175], v31, s[4:7], s69 offen offset:1024 sc1\n"
      "s_waitcnt vmcnt(11) lgkmcnt(15)\n"
      "v_mfma_f32_16x16x32_bf16 v[212:215], v[188:191], v[128:131], v[212:215]\n"
      "ds_read_b128 v[188:191], v87 offset:10240\n"
      "ds_read_b128 a[224:227], v1 offset:14336\n"
      "v_mfma_f32_16x16x32_bf16 v[196:199], a[96:99], v[128:131], v[196:199]\n"
      "ds_read_b128 a[228:231], v1 offset:32768\n"
      "v_mfma_f32_16x16x32_bf16 v[200:203], a[100:103], v[128:131], v[200:203]\n"
      "ds_read_b128 a[232:235], v30 offset:14336\n"
      "v_mfma_f32_16x16x32_bf16 v[204:207], a[104:107], v[128:131], v[204:207]\n"
      "ds_read_b128 a[236:239], v30 offset:32768\n"
      "v_mfma_f32_16x16x32_bf16 v[208:211], a[108:111], v[128:131], v[208:211]\n"
      "ds_read_b64_tr_b16 v[240:241], v32\n"
      "ds_read_b64_tr_b16 v[242:243], v32 offset:1024\n"
      "ds_read_b64_tr_b16 v[244:245], v33\n"
      "ds_read_b64_tr_b16 v[246:247], v33 offset:1024\n"
      "ds_read_b64_tr_b16 v[248:249], v34\n"
      "ds_read_b64_tr_b16 v[250:251], v34 offset:1024\n"
      "ds_read_b64_tr_b16 v[252:253], v35\n"
      "ds_read_b64_tr_b16 v[254:255], v35 offset:1024\n"
      "s_waitcnt vmcnt(10) lgkmcnt(15)\n"
      "v_mfma_f32_16x16x32_bf16 v[212:215], v[192:195], v[132:135], v[212:215]\n"
      "ds_read_b128 v[192:195], v87 offset:11264\n"
      "ds_read_b128 a[240:243], v1 offset:15360\n"
      "v_mfma_f32_16x16x32_bf16 v[196:199], a[112:115], v[132:135], v[196:199]\n"
      "ds_read_b128 a[244:247], v1 offset:33792\n"
      "v_mfma_f32_16x16x32_bf16 v[200:203], a[116:119], v[132:135], v[200:203]\n"
      "ds_read_b128 a[248:251], v30 offset:15360\n"
      "v_mfma_f32_16x16x32_bf16 v[204:207], a[120:123], v[132:135], v[204:207]\n"
      "ds_read_b128 a[252:255], v30 offset:33792\n"
      "v_mfma_f32_16x16x32_bf16 v[208:211], a[124:127], v[132:135], v[208:211]\n"
      "s_mov_b32 s52, 10790912\n"
      "s_waitcnt lgkmcnt(11)\n"
      "buffer_store_dwordx4 v[240:243], v28, s[4:7], s52 offen sc1\n"
      "s_waitcnt vmcnt(10)\n"
      "v_mfma_f32_16x16x32_bf16 v[212:215], v[180:183], v[136:139], v[212:215]\n"
      "ds_read_b128 v[180:183], v87 offset:12288\n"
      "ds_read_b128 v[96:99], v1 offset:16384\n"
      "v_mfma_f32_16x16x32_bf16 v[196:199], a[128:131], v[136:139], v[196:199]\n"
      "ds_read_b128 v[100:103], v1 offset:34816\n"
      "v_mfma_f32_16x16x32_bf16 v[200:203], a[132:135], v[136:139], v[200:203]\n"
      "ds_read_b128 v[104:107], v30 offset:16384\n"
      "v_mfma_f32_16x16x32_bf16 v[204:207], a[136:139], v[136:139], v[204:207]\n"
      "ds_read_b128 v[108:111], v30 offset:34816\n"
      "v_mfma_f32_16x16x32_bf16 v[208:211], a[140:143], v[136:139], v[208:211]\n"
      "s_mov_b32 s52, 10809344\n"
      "s_waitcnt lgkmcnt(14)\n"
      "buffer_store_dwordx4 v[244:247], v28, s[4:7], s52 offen sc1\n"
      "s_waitcnt vmcnt(10)\n"
      "v_mfma_f32_16x16x32_bf16 v[212:215], v[184:187], v[140:143], v[212:215]\n"
      "ds_read_b128 v[184:187], v87 offset:13312\n"
      "ds_read_b128 v[112:115], v1 offset:17408\n"
      "v_mfma_f32_16x16x32_bf16 v[196:199], a[144:147], v[140:143], v[196:199]\n"
      "ds_read_b128 v[116:119], v1 offset:35840\n"
      "v_mfma_f32_16x16x32_bf16 v[200:203], a[148:151], v[140:143], v[200:203]\n"
      "ds_read_b128 v[120:123], v30 offset:17408\n"
      "v_mfma_f32_16x16x32_bf16 v[204:207], a[152:155], v[140:143], v[204:207]\n"
      "ds_read_b128 v[124:127], v30 offset:35840\n"
      "v_mfma_f32_16x16x32_bf16 v[208:211], a[156:159], v[140:143], v[208:211]\n"
      "s_mov_b32 s52, 10827776\n"
      "s_waitcnt lgkmcnt(15)\n"
      "buffer_store_dwordx4 v[248:251], v28, s[4:7], s52 offen sc1\n"
      "s_waitcnt vmcnt(10)\n"
      "v_mfma_f32_16x16x32_bf16 v[212:215], v[188:191], v[144:147], v[212:215]\n"
      "ds_read_b128 v[188:191], v87 offset:14336\n"
      "v_mfma_f32_16x16x32_bf16 v[196:199], a[160:163], v[144:147], v[196:199]\n"
      "v_mfma_f32_16x16x32_bf16 v[200:203], a[164:167], v[144:147], v[200:203]\n"
      "s_mov_b32 s52, 10846208\n"
      "s_waitcnt lgkmcnt(15)\n"
      "buffer_store_dwordx4 v[252:255], v28, s[4:7], s52 offen sc1\n"
      "v_mfma_f32_16x16x32_bf16 v[204:207], a[168:171], v[144:147], v[204:207]\n"
      "v_mfma_f32_16x16x32_bf16 v[208:211], a[172:175], v[144:147], v[208:211]\n"
      "s_waitcnt vmcnt(10) lgkmcnt(15)\n"
      "v_mfma_f32_16x16x32_bf16 v[212:215], v[192:195], v[148:151], v[212:215]\n"
      "ds_read_b128 v[192:195], v87 offset:15360\n"
      "v_mfma_f32_16x16x32_bf16 v[196:199], a[176:179], v[148:151], v[196:199]\n"
      "v_mfma_f32_16x16x32_bf16 v[200:203], a[180:183], v[148:151], v[200:203]\n"
      "v_mfma_f32_16x16x32_bf16 v[204:207], a[184:187], v[148:151], v[204:207]\n"
      "v_mfma_f32_16x16x32_bf16 v[208:211], a[188:191], v[148:151], v[208:211]\n"
      "s_waitcnt vmcnt(9) lgkmcnt(11)\n"
      "v_mfma_f32_16x16x32_bf16 v[212:215], v[180:183], v[152:155], v[212:215]\n"
      "ds_read_b128 v[180:183], v87 offset:16384\n"
      "v_mfma_f32_16x16x32_bf16 v[196:199], a[192:195], v[152:155], v[196:199]\n"
      "v_mfma_f32_16x16x32_bf16 v[200:203], a[196:199], v[152:155], v[200:203]\n"
      "v_mfma_f32_16x16x32_bf16 v[204:207], a[200:203], v[152:155], v[204:207]\n"
      "v_mfma_f32_16x16x32_bf16 v[208:211], a[204:207], v[152:155], v[208:211]\n"
      "s_waitcnt vmcnt(8) lgkmcnt(7)\n"
      "v_mfma_f32_16x16x32_bf16 v[212:215], v[184:187], v[156:159], v[212:215]\n"
      "ds_read_b128 v[184:187], v87 offset:17408\n"
      "v_mfma_f32_16x16x32_bf16 v[196:199], a[208:211], v[156:159], v[196:199]\n"
      "v_mfma_f32_16x16x32_bf16 v[200:203], a[212:215], v[156:159], v[200:203]\n"
      "v_mfma_f32_16x16x32_bf16 v[204:207], a[216:219], v[156:159], v[204:207]\n"
      "v_mfma_f32_16x16x32_bf16 v[208:211], a[220:223], v[156:159], v[208:211]\n"
      "s_waitcnt vmcnt(7) lgkmcnt(3)\n"
      "v_mfma_f32_16x16x32_bf16 v[212:215], v[188:191], v[160:163], v[212:215]\n"
      "v_mfma_f32_16x16x32_bf16 v[196:199], a[224:227], v[160:163], v[196:199]\n"
      "v_mfma_f32_16x16x32_bf16 v[200:203], a[228:231], v[160:163], v[200:203]\n"
      "v_mfma_f32_16x16x32_bf16 v[204:207], a[232:235], v[160:163], v[204:207]\n"
      "v_mfma_f32_16x16x32_bf16 v[208:211], a[236:239], v[160:163], v[208:211]\n"
      "s_waitcnt vmcnt(6) lgkmcnt(2)\n"
      "s_waitcnt lgkmcnt(0)\n"
      "s_barrier\n"
      "v_mfma_f32_16x16x32_bf16 v[212:215], v[192:195], v[164:167], v[212:215]\n"
      "s_add_u32 m0, s66, 0\n"
      "s_add_u32 s53, s65, 663552\n"
      "buffer_load_dwordx4 v1, s[4:7], s53 offen sc1 lds\n"
      "v_mfma_f32_16x16x32_bf16 v[196:199], a[240:243], v[164:167], v[196:199]\n"
      "v_mfma_f32_16x16x32_bf16 v[200:203], a[244:247], v[164:167], v[200:203]\n"
      "s_add_u32 m0, s66, 1024\n"
      "s_add_u32 s53, s65, 664576\n"
      "buffer_load_dwordx4 v1, s[4:7], s53 offen sc1 lds\n"
      "v_mfma_f32_16x16x32_bf16 v[204:207], a[248:251], v[164:167], v[204:207]\n"
      "v_mfma_f32_16x16x32_bf16 v[208:211], a[252:255], v[164:167], v[208:211]\n"
      "s_waitcnt vmcnt(7)\n"
      "v_mfma_f32_16x16x32_bf16 v[212:215], v[180:183], v[168:171], v[212:215]\n"
      "s_add_u32 m0, s66, 2048\n"
      "s_add_u32 s53, s65, 665600\n"
      "buffer_load_dwordx4 v1, s[4:7], s53 offen sc1 lds\n"
      "s_add_u32 m0, s66, 3072\n"
      "s_add_u32 s53, s65, 666624\n"
      "buffer_load_dwordx4 v1, s[4:7], s53 offen sc1 lds\n"
      "s_waitcnt vmcnt(8)\n"
      "v_mfma_f32_16x16x32_bf16 v[212:215], v[184:187], v[172:175], v[212:215]\n"
      "v_mfma_f32_16x16x32_bf16 v[196:199], v[96:99], v[168:171], v[196:199]\n"
      "s_add_u32 m0, s66, 4096\n"
      "s_add_u32 s53, s65, 667648\n"
      "buffer_load_dwordx4 v1, s[4:7], s53 offen sc1 lds\n"
      "v_mfma_f32_16x16x32_bf16 v[196:199], v[112:115], v[172:175], v[196:199]\n"
      "v_mfma_f32_16x16x32_bf16 v[200:203], v[100:103], v[168:171], v[200:203]\n"
      "s_nop 1\n"
      "v_mov_b32 v24, v212\n"
      "v_mfma_f32_16x16x32_bf16 v[200:203], v[116:119], v[172:175], v[200:203]\n"
      "v_mfma_f32_16x16x32_bf16 v[204:207], v[104:107], v[168:171], v[204:207]\n"
      "v_mov_b32 v25, v212\n"
      "v_mfma_f32_16x16x32_bf16 v[204:207], v[120:123], v[172:175], v[204:207]\n"
      "v_mfma_f32_16x16x32_bf16 v[208:211], v[108:111], v[168:171], v[208:211]\n"
      "v_permlane32_swap_b32 v24, v25\n"
      "v_mfma_f32_16x16x32_bf16 v[208:211], v[124:127], v[172:175], v[208:211]\n"
      "v_sub_f32 v236, v212, v25\n"
      "v_sub_f32 v237, v213, v25\n"
      "v_sub_f32 v238, v214, v25\n"
      "v_sub_f32 v239, v215, v25\n"
      "v_cvt_pk_bf16_f32 v20, v236, v237\n"
      "v_cvt_pk_bf16_f32 v21, v238, v239\n"
      "v_cvt_pk_bf16_f32 v22, v236, v237\n"
      "v_cvt_pk_bf16_f32 v23, v238, v239\n"
      "s_mov_b32 s46, 3713024\n"
      "s_mov_b32 s47, 3717120\n"
      "v_permlane16_swap_b32 v20, v22\n"
      "v_permlane16_swap_b32 v21, v23\n"
      "buffer_store_dwordx4 v[20:23], v74, s[4:7], s46 offen sc1\n"
      "v_sub_f32 v220, v196, v25\n"
      "v_sub_f32 v221, v197, v25\n"
      "v_sub_f32 v222, v198, v25\n"
      "v_sub_f32 v223, v199, v25\n"
      "v_cvt_pk_bf16_f32 v12, v220, v221\n"
      "v_cvt_pk_bf16_f32 v13, v222, v223\n"
      "v_sub_f32 v224, v200, v25\n"
      "v_sub_f32 v225, v201, v25\n"
      "v_sub_f32 v226, v202, v25\n"
      "v_sub_f32 v227, v203, v25\n"
      "v_cvt_pk_bf16_f32 v14, v224, v225\n"
      "v_cvt_pk_bf16_f32 v15, v226, v227\n"
      "s_nop 0\n"
      "v_permlane16_swap_b32 v12, v14\n"
      "v_permlane16_swap_b32 v13, v15\n"
      "buffer_store_dwordx4 v[12:15], v84, s[4:7], s46 offen sc1\n"
      "v_sub_f32 v228, v204, v25\n"
      "v_sub_f32 v229, v205, v25\n"
      "v_sub_f32 v230, v206, v25\n"
      "v_sub_f32 v231, v207, v25\n"
      "v_cvt_pk_bf16_f32 v16, v228, v229\n"
      "v_cvt_pk_bf16_f32 v17, v230, v231\n"
      "v_sub_f32 v232, v208, v25\n"
      "v_sub_f32 v233, v209, v25\n"
      "v_sub_f32 v234, v210, v25\n"
      "v_sub_f32 v235, v211, v25\n"
      "v_cvt_pk_bf16_f32 v18, v232, v233\n"
      "v_cvt_pk_bf16_f32 v19, v234, v235\n"
      "s_nop 0\n"
      "v_permlane16_swap_b32 v16, v18\n"
      "v_permlane16_swap_b32 v17, v19\n"
      "buffer_store_dwordx4 v[16:19], v84, s[4:7], s47 offen sc1\n"
      "s_add_u32 m0, s66, 5120\n"
      "s_add_u32 s53, s65, 668672\n"
      "buffer_load_dwordx4 v1, s[4:7], s53 offen sc1 lds\n"
      "s_add_u32 m0, s66, 6144\n"
      "s_add_u32 s53, s65, 669696\n"
      "buffer_load_dwordx4 v1, s[4:7], s53 offen sc1 lds\n"
      "v_add_f32 v29, v29, v25\n"
      "s_add_u32 m0, s66, 7168\n"
      "s_add_u32 s53, s65, 670720\n"
      "buffer_load_dwordx4 v1, s[4:7], s53 offen sc1 lds\n"
      "s_add_u32 m0, s66, 8192\n"
      "s_add_u32 s53, s65, 671744\n"
      "buffer_load_dwordx4 v1, s[4:7], s53 offen sc1 lds\n"
      "s_add_u32 m0, s66, 9216\n"
      "s_add_u32 s53, s65, 672768\n"
      "buffer_load_dwordx4 v1, s[4:7], s53 offen sc1 lds\n"
      "v_lshrrev_b32 v36, 2, v2\n"
      "s_add_u32 m0, s66, 10240\n"
      "s_add_u32 s53, s65, 673792\n"
      "buffer_load_dwordx4 v1, s[4:7], s53 offen sc1 lds\n"
      "s_add_u32 m0, s66, 11264\n"
      "s_add_u32 s53, s65, 674816\n"
      "buffer_load_dwordx4 v1, s[4:7], s53 offen sc1 lds\n"
      "s_add_u32 m0, s66, 12288\n"
      "s_add_u32 s53, s65, 675840\n"
      "buffer_load_dwordx4 v1, s[4:7], s53 offen sc1 lds\n"
      "s_add_u32 m0, s66, 13312\n"
      "s_add_u32 s53, s65, 676864\n"
      "buffer_load_dwordx4 v1, s[4:7], s53 offen sc1 lds\n"
      "v_lshl_add_u32 v36, v36, 4, v2\n"
      "s_add_u32 m0, s66, 14336\n"
      "s_add_u32 s53, s65, 677888\n"
      "buffer_load_dwordx4 v1, s[4:7], s53 offen sc1 lds\n"
      "s_add_u32 m0, s66, 15360\n"
      "s_add_u32 s53, s65, 678912\n"
      "buffer_load_dwordx4 v1, s[4:7], s53 offen sc1 lds\n"
      "s_add_u32 m0, s66, 16384\n"
      "s_add_u32 s53, s65, 679936\n"
      "buffer_load_dwordx4 v1, s[4:7], s53 offen sc1 lds\n"
      "v_lshlrev_b32 v36, 2, v36\n"
      "s_add_u32 m0, s66, 17408\n"
      "s_add_u32 s53, s65, 680960\n"
      "buffer_load_dwordx4 v1, s[4:7], s53 offen sc1 lds\n"
      "s_waitcnt vmcnt(13)\n"
      "s_barrier\n"
      "s_add_u32 s45, s67, 10240\n"
      "buffer_store_dwordx2 v[6:7], v10, s[4:7], s45 offen sc1\n"
      "s_add_u32 m0, s70, 94208\n"
      "s_add_u32 s53, s70, 0\n"
      "buffer_load_dwordx4 v86, s[4:7], s53 offen sc1 lds\n"
      "s_lshl_b32 s45, s2, 2\n"
      "s_add_u32 s45, s45, s40\n"
      "s_add_u32 s45, s45, 1510\n"
      "s_mul_i32 s46, s45, 6473\n"
      "s_lshr_b32 s46, s46, 20\n"
      "s_mul_i32 s47, s46, 162\n"
      "s_sub_u32 s47, s45, s47\n"
      "s_mul_i32 s48, s47, 57\n"
      "s_lshr_b32 s48, s48, 9\n"
      "s_mul_i32 s49, s48, 9\n"
      "s_add_u32 m0, s70, 95232\n"
      "s_add_u32 s53, s70, 1024\n"
      "buffer_load_dwordx4 v86, s[4:7], s53 offen sc1 lds\n"
      "s_sub_u32 s49, s47, s49\n"
      "s_mul_i32 s76, s46, 663552\n"
      "s_mul_i32 s50, s49, 72\n"
      "s_add_u32 s50, s50, s48\n"
      "s_lshl_b32 s50, s50, 10\n"
      "s_add_u32 s76, s76, s50\n"
      "s_lshl_b32 s74, s48, 3\n"
      "s_lshl_b32 s49, s49, 7\n"
      "s_sub_u32 s50, s46, 3\n"
      "s_add_u32 m0, s70, 96256\n"
      "s_add_u32 s53, s70, 2048\n"
      "buffer_load_dwordx4 v86, s[4:7], s53 offen sc1 lds\n"
      "s_mul_i32 s50, s50, 1152\n"
      "s_sub_u32 s51, s46, 11\n"
      "s_mul_i32 s51, s51, 576\n"
      "s_mov_b32 s73, 1152\n"
      "s_cmp_lt_u32 s46, 11\n"
      "s_cselect_b32 s50, s50, 0\n"
      "s_cselect_b32 s51, 0, s51\n"
      "s_cselect_b64 s[16:17], s[30:31], s[32:33]\n"
      "s_cselect_b32 s73, 9216, s73\n"
      "s_add_u32 s49, s49, s50\n"
      "s_add_u32 m0, s70, 97280\n"
      "s_add_u32 s53, s70, 3072\n"
      "buffer_load_dwordx4 v86, s[4:7], s53 offen sc1 lds\n"
      "s_add_u32 s74, s74, s51\n"
      "s_add_u32 s75, s74, 4\n"
      "s_mov_b32 s18, 5308416\n"
      "s_and_b32 s17, s17, 0xffff\n"
      "s_mov_b32 s19, 0x20000\n"
      "s_mul_i32 s74, s74, s73\n"
      "s_mul_i32 s75, s75, s73\n"
      "s_add_u32 s74, s74, s49\n"
      "s_add_u32 s75, s75, s49\n"
      "s_add_u32 m0, s70, 98304\n"
      "s_add_u32 s53, s70, 4096\n"
      "buffer_load_dwordx4 v86, s[4:7], s53 offen sc1 lds\n"
      "s_cmp_lt_u32 s45, 3078\n"
      "s_cselect_b32 s18, s18, 0\n"
      "s_cselect_b32 s76, s76, 1073741824\n"
      "s_cselect_b32 s74, s74, 0\n"
      "s_cselect_b32 s75, s75, 0\n"
      "v_bfe_u32 v26, v4, 3, 1\n"
      "v_mul_u32_u24 v26, 144, v26\n"
      "buffer_load_dword v8, v9, s[60:63], 0 offen\n"
      "v_add_u32 v26, v26, v3\n"
      "v_mul_lo_u32 v11, v26, s73\n"
      "v_and_b32 v26, 7, v4\n"
      "v_lshlrev_b32 v27, 1, v3\n"
      "v_xor_b32 v26, v26, v27\n"
      "v_lshl_add_u32 v11, v26, 4, v11\n"
      "s_mul_i32 s73, s73, 288\n"
      "s_mov_b32 s51, 10240\n"
      "s_mul_i32 s44, s40, 2\n"
      "s_mov_b32 exec_hi, 0\n"
      "s_lshl_b32 s44, 3, s44\n"
      "s_and_b32 exec_lo, s44, 127\n"
      "v_mov_b32 v26, 0\n"
      "v_mov_b32 v27, 0\n"
      "buffer_atomic_or_x2 v[26:27], v176, s[4:7], s51 offen sc0 sc1\n"
      "s_mov_b64 exec, -1\n"
      "s_mov_b32 m0, s77\n"
      "v_add_u32 v28, s76, v1\n"
      "buffer_load_dwordx4 v11, s[16:19], s74 offen lds\n"
      "s_add_u32 m0, s77, 1024\n"
      "s_add_u32 s74, s74, s73\n"
      "buffer_load_dwordx4 v11, s[16:19], s75 offen lds\n"
      "s_add_u32 m0, s77, 2048\n"
      "s_add_u32 s75, s75, s73\n"
      "buffer_load_dwordx4 v11, s[16:19], s74 offen lds\n"
      "s_add_u32 m0, s77, 3072\n"
      "s_nop 0\n"
      "buffer_load_dwordx4 v11, s[16:19], s75 offen lds\n"
      "s_mul_i32 s44, s40, 2\n"
      "s_mov_b32 exec_hi, 0\n"
      "s_lshl_b32 s44, 3, s44\n"
      "s_and_b32 exec_lo, s44, 127\n"
      "s_waitcnt vmcnt(4)\n"
      "v_cmp_ne_u64 vcc, s[42:43], v[26:27]\n"
      "s_cbranch_vccz .LD11_%=\n"
      "s_mov_b32 s44, 0\n"
      ".LP10_%=:\n"
      "s_mov_b64 exec, vcc\n"
      "v_mov_b32 v26, 0\n"
      "v_mov_b32 v27, 0\n"
      "buffer_atomic_or_x2 v[26:27], v176, s[4:7], s51 offen sc0 sc1\n"
      "s_waitcnt vmcnt(0)\n"
      "v_cmp_ne_u64 vcc, s[42:43], v[26:27]\n"
      "s_cbranch_vccz .LD11_%=\n"
      "s_add_u32 s44, s44, 1\n"
      "s_cmp_lt_u32 s44, s41\n"
      "s_cbranch_scc1 .LP10_%=\n"
      "s_mov_b32 s41, 0\n"
      ".LD11_%=:\n"
      "s_mov_b64 exec, -1\n"
      "s_barrier\n"
      "s_mov_b32 s69, 3713024\n"
      "buffer_load_dwordx4 v[56:59], v31, s[4:7], s69 offen offset:0 sc1\n"
      "buffer_load_dwordx4 v[60:63], v31, s[4:7], s69 offen offset:1024 sc1\n"
      "buffer_load_dwordx4 v[64:67], v31, s[4:7], s69 offen offset:2048 sc1\n"
      "ds_read_b128 a[0:3], v1 offset:0\n"
      "buffer_load_dwordx4 v[68:71], v31, s[4:7], s69 offen offset:3072 sc1\n"
      "ds_read_b128 a[4:7], v1 offset:18432\n"
      "s_lshr_b32 s48, s39, 1\n"
      "ds_read_b128 a[8:11], v30 offset:0\n"
      "s_mul_i32 s48, s48, 18432\n"
      "ds_read_b128 a[12:15], v30 offset:18432\n"
      "s_and_b32 s49, s39, 1\n"
      "ds_read_b128 v[180:183], v87 offset:0\n"
      "s_mov_b32 s69, 3717120\n"
      "buffer_load_dwordx4 v[76:79], v31, s[4:7], s69 offen offset:0 sc1\n"
      "ds_read_b128 a[16:19], v1 offset:1024\n"
      "ds_read_b128 a[20:23], v1 offset:19456\n"
      "s_lshl_b32 s49, s49, 7\n"
      "ds_read_b128 a[24:27], v30 offset:1024\n"
      "s_add_u32 s48, s48, s49\n"
      "ds_read_b128 a[28:31], v30 offset:19456\n"
      "buffer_load_dwordx4 v[92:95], v31, s[4:7], s69 offen offset:1024 sc1\n"
      "ds_read_b128 v[184:187], v87 offset:1024\n"
      "s_add_u32 s48, s48, 12707840\n"
      "ds_read_b128 a[32:35], v1 offset:2048\n"
      "v_cmp_gt_u32 vcc, 36, v4\n"
      "ds_read_b128 a[36:39], v1 offset:20480\n"
      "ds_read_b128 a[40:43], v30 offset:2048\n"
      "buffer_load_dwordx4 v[128:131], v31, s[4:7], s69 offen offset:2048 sc1\n"
      "ds_read_b128 a[44:47], v30 offset:20480\n"
      "ds_read_b128 v[188:191], v87 offset:2048\n"
      "v_lshrrev_b32 v25, 3, v4\n"
      "ds_read_b128 a[48:51], v1 offset:3072\n"
      "v_add_u32 v27, 548352, v1\n"
      "ds_read_b128 a[52:55], v1 offset:21504\n"
      "buffer_load_dwordx4 v[132:135], v31, s[4:7], s69 offen offset:3072 sc1\n"
      "ds_read_b128 a[56:59], v30 offset:3072\n"
      "ds_read_b128 a[60:63], v30 offset:21504\n"
      "v_mov_b32 v86, 1073741824\n"
      "ds_read_b128 v[192:195], v87 offset:3072\n"
      "ds_read_b128 a[64:67], v1 offset:4096\n"
      "s_mov_b32 s69, 3721216\n"
      "buffer_load_dwordx4 v[136:139], v31, s[4:7], s69 offen offset:0 sc1\n"
      "ds_read_b128 a[68:71], v1 offset:22528\n"
      "ds_read_b128 a[72:75], v30 offset:4096\n"
      "v_cndmask_b32 v86, v86, v27, vcc\n"
      "ds_read_b128 a[76:79], v30 offset:22528\n"
      "ds_read_b128 a[80:83], v1 offset:5120\n"
      "buffer_load_dwordx4 v[140:143], v31, s[4:7], s69 offen offset:1024 sc1\n"
      "ds_read_b128 a[84:87], v1 offset:23552\n"
      "v_cmp_gt_u32 vcc, 32, v4\n"
      "ds_read_b128 a[88:91], v30 offset:5120\n"
      "ds_read_b128 a[92:95], v30 offset:23552\n"
      "v_and_b32 v26, 7, v4\n"
      "ds_read_b128 a[96:99], v1 offset:6144\n"
      "buffer_load_dwordx4 v[144:147], v31, s[4:7], s69 offen offset:2048 sc1\n"
      "ds_read_b128 a[100:103], v1 offset:24576\n"
      "ds_read_b128 a[104:107], v30 offset:6144\n"
      "v_lshl_add_u32 v25, v25, 4, v26\n"
      "ds_read_b128 a[108:111], v30 offset:24576\n"
      "ds_read_b128 a[112:115], v1 offset:7168\n"
      "buffer_load_dwordx4 v[148:151], v31, s[4:7], s69 offen offset:3072 sc1\n"
      "ds_read_b128 a[116:119], v1 offset:25600\n"
      "v_lshl_add_u32 v25, v25, 4, s48\n"
      "ds_read_b128 a[120:123], v30 offset:7168\n"
      "ds_read_b128 a[124:127], v30 offset:25600\n"
      "v_cndmask_b32 v86, v86, v25, vcc\n"
      "s_waitcnt vmcnt(11) lgkmcnt(15)\n"
      "v_mfma_f32_16x16x32_bf16 v[212:215], v[180:183], v[56:59], 0\n"
      "ds_read_b128 v[180:183], v87 offset:4096\n"
      "v_mfma_f32_16x16x32_bf16 v[216:219], v[56:59], v[56:59], 0\n"
      "ds_read_b128 a[128:131], v1 offset:8192\n"
      "v_mfma_f32_16x16x32_bf16 v[196:199], a[0:3], v[56:59], 0\n"
      "ds_read_b128 a[132:135], v1 offset:26624\n"
      "v_mfma_f32_16x16x32_bf16 v[200:203], a[4:7], v[56:59], 0\n"
      "ds_read_b128 a[136:139], v30 offset:8192\n"
      "v_mfma_f32_16x16x32_bf16 v[204:207], a[8:11], v[56:59], 0\n"
      "ds_read_b128 a[140:143], v30 offset:26624\n"
      "v_mfma_f32_16x16x32_bf16 v[208:211], a[12:15], v[56:59], 0\n"
      "s_mov_b32 s69, 3725312\n"
      "buffer_load_dwordx4 v[152:155], v31, s[4:7], s69 offen offset:0 sc1\n"
      "s_waitcnt vmcnt(11) lgkmcnt(15)\n"
      "v_mfma_f32_16x16x32_bf16 v[212:215], v[184:187], v[60:63], v[212:215]\n"
      "ds_read_b128 v[184:187], v87 offset:5120\n"
      "v_mfma_f32_16x16x32_bf16 v[216:219], v[60:63], v[60:63], v[216:219]\n"
      "ds_read_b128 a[144:147], v1 offset:9216\n"
      "v_mfma_f32_16x16x32_bf16 v[196:199], a[16:19], v[60:63], v[196:199]\n"
      "ds_read_b128 a[148:151], v1 offset:27648\n"
      "v_mfma_f32_16x16x32_bf16 v[200:203], a[20:23], v[60:63], v[200:203]\n"
      "ds_read_b128 a[152:155], v30 offset:9216\n"
      "v_mfma_f32_16x16x32_bf16 v[204:207], a[24:27], v[60:63], v[204:207]\n"
      "ds_read_b128 a[156:159], v30 offset:27648\n"
      "v_mfma_f32_16x16x32_bf16 v[208:211], a[28:31], v[60:63], v[208:211]\n"
      "buffer_load_dwordx4 v[156:159], v31, s[4:7], s69 offen offset:1024 sc1\n"
      "s_waitcnt vmcnt(11) lgkmcnt(15)\n"
      "v_mfma_f32_16x16x32_bf16 v[212:215], v[188:191], v[64:67], v[212:215]\n"
      "ds_read_b128 v[188:191], v87 offset:6144\n"
      "v_mfma_f32_16x16x32_bf16 v[216:219], v[64:67], v[64:67], v[216:219]\n"
      "ds_read_b128 a[160:163], v1 offset:10240\n"
      "v_mfma_f32_16x16x32_bf16 v[196:199], a[32:35], v[64:67], v[196:199]\n"
      "ds_read_b128 a[164:167], v1 offset:28672\n"
      "v_mfma_f32_16x16x32_bf16 v[200:203], a[36:39], v[64:67], v[200:203]\n"
      "ds_read_b128 a[168:171], v30 offset:10240\n"
      "v_mfma_f32_16x16x32_bf16 v[204:207], a[40:43], v[64:67], v[204:207]\n"
      "ds_read_b128 a[172:175], v30 offset:28672\n"
      "v_mfma_f32_16x16x32_bf16 v[208:211], a[44:47], v[64:67], v[208:211]\n"
      "buffer_load_dwordx4 v[160:163], v31, s[4:7], s69 offen offset:2048 sc1\n"
      "s_waitcnt vmcnt(11) lgkmcnt(15)\n"
      "v_mfma_f32_16x16x32_bf16 v[212:215], v[192:195], v[68:71], v[212:215]\n"
      "ds_read_b128 v[192:195], v87 offset:7168\n"
      "v_mfma_f32_16x16x32_bf16 v[216:219], v[68:71], v[68:71], v[216:219]\n"
      "ds_read_b128 a[176:179], v1 offset:11264\n"
      "v_mfma_f32_16x16x32_bf16 v[196:199], a[48:51], v[68:71], v[196:199]\n"
      "ds_read_b128 a[180:183], v1 offset:29696\n"
      "v_mfma_f32_16x16x32_bf16 v[200:203], a[52:55], v[68:71], v[200:203]\n"
      "ds_read_b128 a[184:187], v30 offset:11264\n"
      "v_mfma_f32_16x16x32_bf16 v[204:207], a[56:59], v[68:71], v[204:207]\n"
      "ds_read_b128 a[188:191], v30 offset:29696\n"
      "v_mfma_f32_16x16x32_bf16 v[208:211], a[60:63], v[68:71], v[208:211]\n"
      "buffer_load_dwordx4 v[164:167], v31, s[4:7], s69 offen offset:3072 sc1\n"
      "s_waitcnt vmcnt(11) lgkmcnt(15)\n"
      "v_mfma_f32_16x16x32_bf16 v[212:215], v[180:183], v[76:79], v[212:215]\n"
      "ds_read_b128 v[180:183], v87 offset:8192\n"
      "v_mfma_f32_16x16x32_bf16 v[216:219], v[76:79], v[76:79], v[216:219]\n"
      "ds_read_b128 a[192:195], v1 offset:12288\n"
      "v_mfma_f32_16x16x32_bf16 v[196:199], a[64:67], v[76:79], v[196:199]\n"
      "ds_read_b128 a[196:199], v1 offset:30720\n"
      "v_mfma_f32_16x16x32_bf16 v[200:203], a[68:71], v[76:79], v[200:203]\n"
      "ds_read_b128 a[200:203], v30 offset:12288\n"
      "v_mfma_f32_16x16x32_bf16 v[204:207], a[72:75], v[76:79], v[204:207]\n"
      "ds_read_b128 a[204:207], v30 offset:30720\n"
      "v_mfma_f32_16x16x32_bf16 v[208:211], a[76:79], v[76:79], v[208:211]\n"
      "s_mov_b32 s69, 3729408\n"
      "buffer_load_dwordx4 v[168:171], v31, s[4:7], s69 offen offset:0 sc1\n"
      "s_waitcnt vmcnt(11) lgkmcnt(15)\n"
      "v_mfma_f32_16x16x32_bf16 v[212:215], v[184:187], v[92:95], v[212:215]\n"
      "ds_read_b128 v[184:187], v87 offset:9216\n"
      "v_mfma_f32_16x16x32_bf16 v[216:219], v[92:95], v[92:95], v[216:219]\n"
      "ds_read_b128 a[208:211], v1 offset:13312\n"
      "v_mfma_f32_16x16x32_bf16 v[196:199], a[80:83], v[92:95], v[196:199]\n"
      "ds_read_b128 a[212:215], v1 offset:31744\n"
      "v_mfma_f32_16x16x32_bf16 v[200:203], a[84:87], v[92:95], v[200:203]\n"
      "ds_read_b128 a[216:219], v30 offset:13312\n"
      "v_mfma_f32_16x16x32_bf16 v[204:207], a[88:91], v[92:95], v[204:207]\n"
      "ds_read_b128 a[220:223], v30 offset:31744\n"
      "v_mfma_f32_16x16x32_bf16 v[208:211], a[92:95], v[92:95], v[208:211]\n"
      "buffer_load_dwordx4 v[172:175], v31, s[4:7], s69 offen offset:1024 sc1\n"
      "s_waitcnt vmcnt(11) lgkmcnt(15)\n"
      "v_mfma_f32_16x16x32_bf16 v[212:215], v[188:191], v[128:131], v[212:215]\n"
      "ds_read_b128 v[188:191], v87 offset:10240\n"
      "v_mfma_f32_16x16x32_bf16 v[216:219], v[128:131], v[128:131], v[216:219]\n"
      "ds_read_b128 a[224:227], v1 offset:14336\n"
      "v_mfma_f32_16x16x32_bf16 v[196:199], a[96:99], v[128:131], v[196:199]\n"
      "ds_read_b128 a[228:231], v1 offset:32768\n"
      "v_mfma_f32_16x16x32_bf16 v[200:203], a[100:103], v[128:131], v[200:203]\n"
      "ds_read_b128 a[232:235], v30 offset:14336\n"
      "v_mfma_f32_16x16x32_bf16 v[204:207], a[104:107], v[128:131], v[204:207]\n"
      "ds_read_b128 a[236:239], v30 offset:32768\n"
      "v_mfma_f32_16x16x32_bf16 v[208:211], a[108:111], v[128:131], v[208:211]\n"
      "ds_read_b64_tr_b16 v[240:241], v32\n"
      "ds_read_b64_tr_b16 v[242:243], v32 offset:1024\n"
      "ds_read_b64_tr_b16 v[244:245], v33\n"
      "ds_read_b64_tr_b16 v[246:247], v33 offset:1024\n"
      "ds_read_b64_tr_b16 v[248:249], v34\n"
      "ds_read_b64_tr_b16 v[250:251], v34 offset:1024\n"
      "ds_read_b64_tr_b16 v[252:253], v35\n"
      "ds_read_b64_tr_b16 v[254:255], v35 offset:1024\n"
      "s_waitcnt vmcnt(10) lgkmcnt(15)\n"
      "v_mfma_f32_16x16x32_bf16 v[212:215], v[192:195], v[132:135], v[212:215]\n"
      "ds_read_b128 v[192:195], v87 offset:11264\n"
      "v_mfma_f32_16x16x32_bf16 v[216:219], v[132:135], v[132:135], v[216:219]\n"
      "ds_read_b128 a[240:243], v1 offset:15360\n"
      "v_mfma_f32_16x16x32_bf16 v[196:199], a[112:115], v[132:135], v[196:199]\n"
      "ds_read_b128 a[244:247], v1 offset:33792\n"
      "v_mfma_f32_16x16x32_bf16 v[200:203], a[116:119], v[132:135], v[200:203]\n"
      "ds_read_b128 a[248:251], v30 offset:15360\n"
      "v_mfma_f32_16x16x32_bf16 v[204:207], a[120:123], v[132:135], v[204:207]\n"
      "ds_read_b128 a[252:255], v30 offset:33792\n"
      "v_mfma_f32_16x16x32_bf16 v[208:211], a[124:127], v[132:135], v[208:211]\n"
      "s_mov_b32 s52, 10790912\n"
      "s_waitcnt lgkmcnt(11)\n"
      "buffer_store_dwordx4 v[240:243], v28, s[4:7], s52 offen sc1\n"
      "s_waitcnt vmcnt(10)\n"
      "v_mfma_f32_16x16x32_bf16 v[212:215], v[180:183], v[136:139], v[212:215]\n"
      "ds_read_b128 v[180:183], v87 offset:12288\n"
      "v_mfma_f32_16x16x32_bf16 v[216:219], v[136:139], v[136:139], v[216:219]\n"
      "ds_read_b128 v[96:99], v1 offset:16384\n"
      "v_mfma_f32_16x16x32_bf16 v[196:199], a[128:131], v[136:139], v[196:199]\n"
      "ds_read_b128 v[100:103], v1 offset:34816\n"
      "v_mfma_f32_16x16x32_bf16 v[200:203], a[132:135], v[136:139], v[200:203]\n"
      "ds_read_b128 v[104:107], v30 offset:16384\n"
      "v_mfma_f32_16x16x32_bf16 v[204:207], a[136:139], v[136:139], v[204:207]\n"
      "ds_read_b128 v[108:111], v30 offset:34816\n"
      "v_mfma_f32_16x16x32_bf16 v[208:211], a[140:143], v[136:139], v[208:211]\n"
      "s_mov_b32 s52, 10809344\n"
      "s_waitcnt lgkmcnt(14)\n"
      "buffer_store_dwordx4 v[244:247], v28, s[4:7], s52 offen sc1\n"
      "s_waitcnt vmcnt(10)\n"
      "v_mfma_f32_16x16x32_bf16 v[212:215], v[184:187], v[140:143], v[212:215]\n"
      "ds_read_b128 v[184:187], v87 offset:13312\n"
      "v_mfma_f32_16x16x32_bf16 v[216:219], v[140:143], v[140:143], v[216:219]\n"
      "ds_read_b128 v[112:115], v1 offset:17408\n"
      "v_mfma_f32_16x16x32_bf16 v[196:199], a[144:147], v[140:143], v[196:199]\n"
      "ds_read_b128 v[116:119], v1 offset:35840\n"
      "v_mfma_f32_16x16x32_bf16 v[200:203], a[148:151], v[140:143], v[200:203]\n"
      "ds_read_b128 v[120:123], v30 offset:17408\n"
      "v_mfma_f32_16x16x32_bf16 v[204:207], a[152:155], v[140:143], v[204:207]\n"
      "ds_read_b128 v[124:127], v30 offset:35840\n"
      "v_mfma_f32_16x16x32_bf16 v[208:211], a[156:159], v[140:143], v[208:211]\n"
      "s_mov_b32 s52, 10827776\n"
      "s_waitcnt lgkmcnt(15)\n"
      "buffer_store_dwordx4 v[248:251], v28, s[4:7], s52 offen sc1\n"
      "s_waitcnt vmcnt(10)\n"
      "v_mfma_f32_16x16x32_bf16 v[212:215], v[188:191], v[144:147], v[212:215]\n"
      "ds_read_b128 v[188:191], v87 offset:14336\n"
      "v_mfma_f32_16x16x32_bf16 v[216:219], v[144:147], v[144:147], v[216:219]\n"
      "v_mfma_f32_16x16x32_bf16 v[196:199], a[160:163], v[144:147], v[196:199]\n"
      "v_mfma_f32_16x16x32_bf16 v[200:203], a[164:167], v[144:147], v[200:203]\n"
      "s_mov_b32 s52, 10846208\n"
      "s_waitcnt lgkmcnt(15)\n"
      "buffer_store_dwordx4 v[252:255], v28, s[4:7], s52 offen sc1\n"
      "v_mfma_f32_16x16x32_bf16 v[204:207], a[168:171], v[144:147], v[204:207]\n"
      "v_mfma_f32_16x16x32_bf16 v[208:211], a[172:175], v[144:147], v[208:211]\n"
      "s_waitcnt vmcnt(10) lgkmcnt(15)\n"
      "v_mfma_f32_16x16x32_bf16 v[212:215], v[192:195], v[148:151], v[212:215]\n"
      "ds_read_b128 v[192:195], v87 offset:15360\n"
      "v_mfma_f32_16x16x32_bf16 v[216:219], v[148:151], v[148:151], v[216:219]\n"
      "v_mfma_f32_16x16x32_bf16 v[196:199], a[176:179], v[148:151], v[196:199]\n"
      "v_mfma_f32_16x16x32_bf16 v[200:203], a[180:183], v[148:151], v[200:203]\n"
      "v_mfma_f32_16x16x32_bf16 v[204:207], a[184:187], v[148:151], v[204:207]\n"
      "v_mfma_f32_16x16x32_bf16 v[208:211], a[188:191], v[148:151], v[208:211]\n"
      "s_waitcnt vmcnt(9) lgkmcnt(11)\n"
      "v_mfma_f32_16x16x32_bf16 v[212:215], v[180:183], v[152:155], v[212:215]\n"
      "ds_read_b128 v[180:183], v87 offset:16384\n"
      "v_mfma_f32_16x16x32_bf16 v[216:219], v[152:155], v[152:155], v[216:219]\n"
      "v_mfma_f32_16x16x32_bf16 v[196:199], a[192:195], v[152:155], v[196:199]\n"
      "v_mfma_f32_16x16x32_bf16 v[200:203], a[196:199], v[152:155], v[200:203]\n"
      "v_mfma_f32_16x16x32_bf16 v[204:207], a[200:203], v[152:155], v[204:207]\n"
      "v_mfma_f32_16x16x32_bf16 v[208:211], a[204:207], v[152:155], v[208:211]\n"
      "s_waitcnt vmcnt(8) lgkmcnt(7)\n"
      "v_mfma_f32_16x16x32_bf16 v[212:215], v[184:187], v[156:159], v[212:215]\n"
      "ds_read_b128 v[184:187], v87 offset:17408\n"
      "v_mfma_f32_16x16x32_bf16 v[216:219], v[156:159], v[156:159], v[216:219]\n"
      "v_mfma_f32_16x16x32_bf16 v[196:199], a[208:211], v[156:159], v[196:199]\n"
      "v_mfma_f32_16x16x32_bf16 v[200:203], a[212:215], v[156:159], v[200:203]\n"
      "v_mfma_f32_16x16x32_bf16 v[204:207], a[216:219], v[156:159], v[204:207]\n"
      "v_mfma_f32_16x16x32_bf16 v[208:211], a[220:223], v[156:159], v[208:211]\n"
      "s_waitcnt vmcnt(7) lgkmcnt(3)\n"
      "v_mfma_f32_16x16x32_bf16 v[212:215], v[188:191], v[160:163], v[212:215]\n"
      "v_mfma_f32_16x16x32_bf16 v[216:219], v[160:163], v[160:163], v[216:219]\n"
      "v_mfma_f32_16x16x32_bf16 v[196:199], a[224:227], v[160:163], v[196:199]\n"
      "v_mfma_f32_16x16x32_bf16 v[200:203], a[228:231], v[160:163], v[200:203]\n"
      "v_mfma_f32_16x16x32_bf16 v[204:207], a[232:235], v[160:163], v[204:207]\n"
      "v_mfma_f32_16x16x32_bf16 v[208:211], a[236:239], v[160:163], v[208:211]\n"
      "s_waitcnt vmcnt(6) lgkmcnt(2)\n"
      "s_waitcnt lgkmcnt(0)\n"
      "s_barrier\n"
      "v_mfma_f32_16x16x32_bf16 v[212:215], v[192:195], v[164:167], v[212:215]\n"
      "s_add_u32 m0, s66, 0\n"
      "s_add_u32 s53, s65, 1327104\n"
      "buffer_load_dwordx4 v1, s[4:7], s53 offen sc1 lds\n"
      "v_mfma_f32_16x16x32_bf16 v[216:219], v[164:167], v[164:167], v[216:219]\n"
      "v_mfma_f32_16x16x32_bf16 v[196:199], a[240:243], v[164:167], v[196:199]\n"
      "v_mfma_f32_16x16x32_bf16 v[200:203], a[244:247], v[164:167], v[200:203]\n"
      "s_add_u32 m0, s66, 1024\n"
      "s_add_u32 s53, s65, 1328128\n"
      "buffer_load_dwordx4 v1, s[4:7], s53 offen sc1 lds\n"
      "v_mfma_f32_16x16x32_bf16 v[204:207], a[248:251], v[164:167], v[204:207]\n"
      "v_mfma_f32_16x16x32_bf16 v[208:211], a[252:255], v[164:167], v[208:211]\n"
      "s_waitcnt vmcnt(7)\n"
      "v_mfma_f32_16x16x32_bf16 v[212:215], v[180:183], v[168:171], v[212:215]\n"
      "s_add_u32 m0, s66, 2048\n"
      "s_add_u32 s53, s65, 1329152\n"
      "buffer_load_dwordx4 v1, s[4:7], s53 offen sc1 lds\n"
      "v_mfma_f32_16x16x32_bf16 v[216:219], v[168:171], v[168:171], v[216:219]\n"
      "s_add_u32 m0, s66, 3072\n"
      "s_add_u32 s53, s65, 1330176\n"
      "buffer_load_dwordx4 v1, s[4:7], s53 offen sc1 lds\n"
      "s_waitcnt vmcnt(8)\n"
      "v_mfma_f32_16x16x32_bf16 v[212:215], v[184:187], v[172:175], v[212:215]\n"
      "v_mfma_f32_16x16x32_bf16 v[216:219], v[172:175], v[172:175], v[216:219]\n"
      "v_mfma_f32_16x16x32_bf16 v[196:199], v[96:99], v[168:171], v[196:199]\n"
      "s_add_u32 m0, s66, 4096\n"
      "s_add_u32 s53, s65, 1331200\n"
      "buffer_load_dwordx4 v1, s[4:7], s53 offen sc1 lds\n"
      "v_mfma_f32_16x16x32_bf16 v[196:199], v[112:115], v[172:175], v[196:199]\n"
      "s_add_u32 m0, s66, 5120\n"
      "s_add_u32 s53, s65, 1332224\n"
      "buffer_load_dwordx4 v1, s[4:7], s53 offen sc1 lds\n"
      "v_mfma_f32_16x16x32_bf16 v[200:203], v[100:103], v[168:171], v[200:203]\n"
      "v_cndmask_b32 v24, v216, v217, s[54:55]\n"
      "v_mfma_f32_16x16x32_bf16 v[200:203], v[116:119], v[172:175], v[200:203]\n"
      "v_mfma_f32_16x16x32_bf16 v[204:207], v[104:107], v[168:171], v[204:207]\n"
      "v_cndmask_b32 v25, v218, v219, s[54:55]\n"
      "v_mfma_f32_16x16x32_bf16 v[204:207], v[120:123], v[172:175], v[204:207]\n"
      "v_cndmask_b32 v24, v24, v25, s[56:57]\n"
      "v_mfma_f32_16x16x32_bf16 v[208:211], v[108:111], v[168:171], v[208:211]\n"
      "v_mfma_f32_16x16x32_bf16 v[208:211], v[124:127], v[172:175], v[208:211]\n"
      "ds_bpermute_b32 v24, v36, v24\n"
      "s_waitcnt lgkmcnt(0)\n"
      "v_mul_f32 v24, 0x3ae38e39, v24\n"
      "v_add_f32 v40, 0x3727c5ac, v24\n"
      "v_fmamk_f32 v24, v40, 0x3727c5ac, v24\n"
      "v_rsq_f32 v25, v24\n"
      "v_rsq_f32 v40, v40\n"
      "v_mul_f32 v41, 0xc0135761, v25\n"
      "v_mul_f32 v42, v25, v25\n"
      "v_mul_f32 v43, v24, v25\n"
      "v_mul_f32 v42, v42, v41\n"
      "v_mul_f32 v42, 0x3d372713, v42\n"
      "v_mul_f32 v24, v196, v196\n"
      "s_add_u32 m0, s66, 6144\n"
      "s_add_u32 s53, s65, 1333248\n"
      "buffer_load_dwordx4 v1, s[4:7], s53 offen sc1 lds\n"
      "v_mul_f32 v25, v197, v197\n"
      "v_mul_f32 v26, v198, v198\n"
      "v_mul_f32 v27, v199, v199\n"
      "v_fma_f32 v24, v24, v42, v41\n"
      "v_fma_f32 v25, v25, v42, v41\n"
      "v_fma_f32 v26, v26, v42, v41\n"
      "v_fma_f32 v27, v27, v42, v41\n"
      "v_mul_f32 v24, v24, v196\n"
      "v_mul_f32 v25, v25, v197\n"
      "v_mul_f32 v26, v26, v198\n"
      "v_mul_f32 v27, v27, v199\n"
      "v_exp_f32 v24, v24\n"
      "v_exp_f32 v25, v25\n"
      "v_exp_f32 v26, v26\n"
      "v_exp_f32 v27, v27\n"
      "v_fma_f32 v24, v24, v43, v43\n"
      "s_add_u32 m0, s66, 7168\n"
      "s_add_u32 s53, s65, 1334272\n"
      "buffer_load_dwordx4 v1, s[4:7], s53 offen sc1 lds\n"
      "v_fma_f32 v25, v25, v43, v43\n"
      "v_fma_f32 v26, v26, v43, v43\n"
      "v_fma_f32 v27, v27, v43, v43\n"
      "v_rcp_f32 v24, v24\n"
      "v_rcp_f32 v25, v25\n"
      "v_rcp_f32 v26, v26\n"
      "v_rcp_f32 v27, v27\n"
      "v_mul_f32 v196, v196, v24\n"
      "v_mul_f32 v197, v197, v25\n"
      "v_mul_f32 v198, v198, v26\n"
      "v_mul_f32 v199, v199, v27\n"
      "v_mul_f32 v24, v200, v200\n"
      "v_mul_f32 v25, v201, v201\n"
      "v_mul_f32 v26, v202, v202\n"
      "v_mul_f32 v27, v203, v203\n"
      "v_fma_f32 v24, v24, v42, v41\n"
      "v_fma_f32 v25, v25, v42, v41\n"
      "s_add_u32 m0, s66, 8192\n"
      "s_add_u32 s53, s65, 1335296\n"
      "buffer_load_dwordx4 v1, s[4:7], s53 offen sc1 lds\n"
      "v_fma_f32 v26, v26, v42, v41\n"
      "v_fma_f32 v27, v27, v42, v41\n"
      "v_mul_f32 v24, v24, v200\n"
      "v_mul_f32 v25, v25, v201\n"
      "v_mul_f32 v26, v26, v202\n"
      "v_mul_f32 v27, v27, v203\n"
      "v_exp_f32 v24, v24\n"
      "v_exp_f32 v25, v25\n"
      "v_exp_f32 v26, v26\n"
      "v_exp_f32 v27, v27\n"
      "v_fma_f32 v24, v24, v43, v43\n"
      "v_fma_f32 v25, v25, v43, v43\n"
      "v_fma_f32 v26, v26, v43, v43\n"
      "v_fma_f32 v27, v27, v43, v43\n"
      "v_rcp_f32 v24, v24\n"
      "v_rcp_f32 v25, v25\n"
      "v_rcp_f32 v26, v26\n"
      "s_add_u32 m0, s66, 9216\n"
      "s_add_u32 s53, s65, 1336320\n"
      "buffer_load_dwordx4 v1, s[4:7], s53 offen sc1 lds\n"
      "v_rcp_f32 v27, v27\n"
      "v_mul_f32 v200, v200, v24\n"
      "v_mul_f32 v201, v201, v25\n"
      "v_mul_f32 v202, v202, v26\n"
      "v_mul_f32 v203, v203, v27\n"
      "v_mul_f32 v24, v204, v204\n"
      "v_mul_f32 v25, v205, v205\n"
      "v_mul_f32 v26, v206, v206\n"
      "v_mul_f32 v27, v207, v207\n"
      "v_fma_f32 v24, v24, v42, v41\n"
      "v_fma_f32 v25, v25, v42, v41\n"
      "v_fma_f32 v26, v26, v42, v41\n"
      "v_fma_f32 v27, v27, v42, v41\n"
      "v_mul_f32 v24, v24, v204\n"
      "v_mul_f32 v25, v25, v205\n"
      "v_mul_f32 v26, v26, v206\n"
      "s_add_u32 m0, s66, 10240\n"
      "s_add_u32 s53, s65, 1337344\n"
      "buffer_load_dwordx4 v1, s[4:7], s53 offen sc1 lds\n"
      "v_mul_f32 v27, v27, v207\n"
      "v_exp_f32 v24, v24\n"
      "v_exp_f32 v25, v25\n"
      "v_exp_f32 v26, v26\n"
      "v_exp_f32 v27, v27\n"
      "v_fma_f32 v24, v24, v43, v43\n"
      "v_fma_f32 v25, v25, v43, v43\n"
      "v_fma_f32 v26, v26, v43, v43\n"
      "v_fma_f32 v27, v27, v43, v43\n"
      "v_rcp_f32 v24, v24\n"
      "v_rcp_f32 v25, v25\n"
      "v_rcp_f32 v26, v26\n"
      "v_rcp_f32 v27, v27\n"
      "v_mul_f32 v204, v204, v24\n"
      "v_mul_f32 v205, v205, v25\n"
      "v_mul_f32 v206, v206, v26\n"
      "v_mul_f32 v207, v207, v27\n"
      "s_add_u32 m0, s66, 11264\n"
      "s_add_u32 s53, s65, 1338368\n"
      "buffer_load_dwordx4 v1, s[4:7], s53 offen sc1 lds\n"
      "v_mul_f32 v24, v208, v208\n"
      "v_mul_f32 v25, v209, v209\n"
      "v_mul_f32 v26, v210, v210\n"
      "v_mul_f32 v27, v211, v211\n"
      "v_fma_f32 v24, v24, v42, v41\n"
      "v_fma_f32 v25, v25, v42, v41\n"
      "v_fma_f32 v26, v26, v42, v41\n"
      "v_fma_f32 v27, v27, v42, v41\n"
      "v_mul_f32 v24, v24, v208\n"
      "v_mul_f32 v25, v25, v209\n"
      "v_mul_f32 v26, v26, v210\n"
      "v_mul_f32 v27, v27, v211\n"
      "v_exp_f32 v24, v24\n"
      "v_exp_f32 v25, v25\n"
      "v_exp_f32 v26, v26\n"
      "v_exp_f32 v27, v27\n"
      "v_fma_f32 v24, v24, v43, v43\n"
      "v_fma_f32 v25, v25, v43, v43\n"
      "v_fma_f32 v26, v26, v43, v43\n"
      "v_fma_f32 v27, v27, v43, v43\n"
      "v_rcp_f32 v24, v24\n"
      "v_rcp_f32 v25, v25\n"
      "v_rcp_f32 v26, v26\n"
      "v_rcp_f32 v27, v27\n"
      "v_mul_f32 v208, v208, v24\n"
      "v_mul_f32 v209, v209, v25\n"
      "v_mul_f32 v210, v210, v26\n"
      "v_mul_f32 v211, v211, v27\n"
      "v_mul_f32 v24, v212, v212\n"
      "v_mul_f32 v25, v213, v213\n"
      "v_mul_f32 v26, v214, v214\n"
      "v_mul_f32 v27, v215, v215\n"
      "v_fma_f32 v24, v24, v42, v41\n"
      "v_fma_f32 v25, v25, v42, v41\n"
      "v_fma_f32 v26, v26, v42, v41\n"
      "v_fma_f32 v27, v27, v42, v41\n"
      "v_mul_f32 v24, v24, v212\n"
      "v_mul_f32 v25, v25, v213\n"
      "v_mul_f32 v26, v26, v214\n"
      "v_mul_f32 v27, v27, v215\n"
      "v_exp_f32 v24, v24\n"
      "v_exp_f32 v25, v25\n"
      "v_exp_f32 v26, v26\n"
      "v_exp_f32 v27, v27\n"
      "v_fma_f32 v24, v24, v43, v43\n"
      "v_fma_f32 v25, v25, v43, v43\n"
      "v_fma_f32 v26, v26, v43, v43\n"
      "v_fma_f32 v27, v27, v43, v43\n"
      "v_rcp_f32 v24, v24\n"
      "v_rcp_f32 v25, v25\n"
      "v_rcp_f32 v26, v26\n"
      "v_rcp_f32 v27, v27\n"
      "v_mul_f32 v212, v212, v24\n"
      "v_mul_f32 v213, v213, v25\n"
      "v_mul_f32 v214, v214, v26\n"
      "v_mul_f32 v215, v215, v27\n"
      "v_cvt_pk_bf16_f32 v20, v212, v213\n"
      "v_cvt_pk_bf16_f32 v21, v214, v215\n"
      "v_cvt_pk_bf16_f32 v22, v212, v213\n"
      "v_cvt_pk_bf16_f32 v23, v214, v215\n"
      "s_mov_b32 s46, 6072320\n"
      "s_mov_b32 s47, 6076416\n"
      "v_permlane16_swap_b32 v20, v22\n"
      "v_permlane16_swap_b32 v21, v23\n"
      "buffer_store_dwordx4 v[20:23], v74, s[4:7], s46 offen sc1\n"
      "v_cvt_pk_bf16_f32 v12, v196, v197\n"
      "v_cvt_pk_bf16_f32 v13, v198, v199\n"
      "v_cvt_pk_bf16_f32 v14, v200, v201\n"
      "v_cvt_pk_bf16_f32 v15, v202, v203\n"
      "s_nop 0\n"
      "v_permlane16_swap_b32 v12, v14\n"
      "v_permlane16_swap_b32 v13, v15\n"
      "buffer_store_dwordx4 v[12:15], v84, s[4:7], s46 offen sc1\n"
      "v_cvt_pk_bf16_f32 v16, v204, v205\n"
      "v_cvt_pk_bf16_f32 v17, v206, v207\n"
      "v_cvt_pk_bf16_f32 v18, v208, v209\n"
      "v_cvt_pk_bf16_f32 v19, v210, v211\n"
      "s_nop 0\n"
      "v_permlane16_swap_b32 v16, v18\n"
      "v_permlane16_swap_b32 v17, v19\n"
      "buffer_store_dwordx4 v[16:19], v84, s[4:7], s47 offen sc1\n"
      "s_add_u32 m0, s66, 12288\n"
      "s_add_u32 s53, s65, 1339392\n"
      "buffer_load_dwordx4 v1, s[4:7], s53 offen sc1 lds\n"
      "v_mul_f32 v196, v220, v40\n"
      "v_mul_f32 v197, v221, v40\n"
      "v_mul_f32 v198, v222, v40\n"
      "v_mul_f32 v199, v223, v40\n"
      "v_mul_f32 v200, v224, v40\n"
      "v_mul_f32 v201, v225, v40\n"
      "v_mul_f32 v202, v226, v40\n"
      "s_add_u32 m0, s66, 13312\n"
      "s_add_u32 s53, s65, 1340416\n"
      "buffer_load_dwordx4 v1, s[4:7], s53 offen sc1 lds\n"
      "v_mul_f32 v203, v227, v40\n"
      "v_mul_f32 v204, v228, v40\n"
      "v_mul_f32 v205, v229, v40\n"
      "v_mul_f32 v206, v230, v40\n"
      "v_mul_f32 v207, v231, v40\n"
      "v_mul_f32 v208, v232, v40\n"
      "v_mul_f32 v209, v233, v40\n"
      "s_add_u32 m0, s66, 14336\n"
      "s_add_u32 s53, s65, 1341440\n"
      "buffer_load_dwordx4 v1, s[4:7], s53 offen sc1 lds\n"
      "v_mul_f32 v210, v234, v40\n"
      "v_mul_f32 v211, v235, v40\n"
      "v_mul_f32 v212, v236, v40\n"
      "v_mul_f32 v213, v237, v40\n"
      "v_mul_f32 v214, v238, v40\n"
      "v_mul_f32 v215, v239, v40\n"
      "s_add_u32 m0, s66, 15360\n"
      "s_add_u32 s53, s65, 1342464\n"
      "buffer_load_dwordx4 v1, s[4:7], s53 offen sc1 lds\n"
      "v_cmp_gt_u32 vcc, 32, v4\n"
      "s_nop 0\n"
      "s_nop 0\n"
      "v_cndmask_b32 v212, 0, v212, vcc\n"
      "v_cndmask_b32 v213, 0, v213, vcc\n"
      "v_cndmask_b32 v214, 0, v214, vcc\n"
      "v_cndmask_b32 v215, 0, v215, vcc\n"
      "v_add_f32 v220, v220, v29\n"
      "v_add_f32 v221, v221, v29\n"
      "s_add_u32 m0, s66, 16384\n"
      "s_add_u32 s53, s65, 1343488\n"
      "buffer_load_dwordx4 v1, s[4:7], s53 offen sc1 lds\n"
      "v_add_f32 v222, v222, v29\n"
      "v_add_f32 v223, v223, v29\n"
      "v_add_f32 v224, v224, v29\n"
      "v_add_f32 v225, v225, v29\n"
      "v_add_f32 v226, v226, v29\n"
      "v_add_f32 v227, v227, v29\n"
      "s_add_u32 m0, s66, 17408\n"
      "s_add_u32 s53, s65, 1344512\n"
      "buffer_load_dwordx4 v1, s[4:7], s53 offen sc1 lds\n"
      "v_add_f32 v228, v228, v29\n"
      "v_add_f32 v229, v229, v29\n"
      "v_add_f32 v230, v230, v29\n"
      "v_add_f32 v231, v231, v29\n"
      "v_add_f32 v232, v232, v29\n"
      "v_add_f32 v233, v233, v29\n"
      "v_add_f32 v234, v234, v29\n"
      "s_add_u32 m0, s70, 94208\n"
      "s_add_u32 s53, s70, 0\n"
      "buffer_load_dwordx4 v86, s[4:7], s53 offen sc1 lds\n"
      "v_add_f32 v235, v235, v29\n"
      "v_add_f32 v236, v236, v29\n"
      "v_add_f32 v237, v237, v29\n"
      "v_add_f32 v238, v238, v29\n"
      "v_add_f32 v239, v239, v29\n"
      "v_and_b32 v24, 1, v3\n"
      "v_lshl_add_u32 v24, v24, 1, -1\n"
      "s_add_u32 m0, s70, 95232\n"
      "s_add_u32 s53, s70, 1024\n"
      "buffer_load_dwordx4 v86, s[4:7], s53 offen sc1 lds\n"
      "v_lshrrev_b32 v25, 1, v3\n"
      "v_lshlrev_b32 v24, v25, v24\n"
      "s_lshl_b32 s48, s38, 6\n"
      "s_lshl_b32 s49, s40, 4\n"
      "s_add_u32 s48, s48, s49\n"
      "v_add3_u32 v24, v24, v2, s48\n"
      "v_cmp_gt_u32 vcc, 0x800, v24\n"
      "v_lshrrev_b32 v25, 6, v24\n"
      "s_add_u32 m0, s70, 96256\n"
      "s_add_u32 s53, s70, 2048\n"
      "buffer_load_dwordx4 v86, s[4:7], s53 offen sc1 lds\n"
      "v_mul_u32_u24 v25, 73728, v25\n"
      "v_and_b32 v24, 63, v24\n"
      "v_lshl_add_u32 v25, v24, 4, v25\n"
      "v_mul_u32_u24 v24, 18432, v3\n"
      "v_add_u32 v25, v25, v24\n"
      "v_mov_b32 v36, 1073741824\n"
      "v_cndmask_b32 v36, v36, v25, vcc\n"
      "s_add_u32 m0, s70, 97280\n"
      "s_add_u32 s53, s70, 3072\n"
      "buffer_load_dwordx4 v86, s[4:7], s53 offen sc1 lds\n"
      "s_add_u32 s49, s38, 1\n"
      "s_sub_u32 s48, s38, 1\n"
      "s_cselect_b32 s48, 1, s48\n"
      "s_cmp_eq_u32 s38, 31\n"
      "s_cselect_b32 s49, 30, s49\n"
      "v_cmp_gt_u32 vcc, 16, v4\n"
      "v_mov_b32 v24, s38\n"
      "v_mov_b32 v25, s49\n"
      "v_cndmask_b32 v24, v24, v25, vcc\n"
      "s_add_u32 m0, s70, 98304\n"
      "s_add_u32 s53, s70, 4096\n"
      "buffer_load_dwordx4 v86, s[4:7], s53 offen sc1 lds\n"
      "v_cmp_gt_u32 vcc, 8, v4\n"
      "v_mov_b32 v25, s48\n"
      "v_add3_u32 v37, v4, s39, 1\n"
      "v_cndmask_b32 v24, v24, v25, vcc\n"
      "v_and_b32 v37, 7, v37\n"
      "v_mul_u32_u24 v24, 6144, v24\n"
      "v_lshl_add_u32 v37, v37, 8, v24\n"
      "s_waitcnt vmcnt(11)\n"
      "s_barrier\n"
      "s_add_u32 s45, s67, 12288\n"
      "buffer_store_dwordx2 v[6:7], v10, s[4:7], s45 offen sc1\n"
      "s_add_u32 s45, s72, 3072\n"
      "buffer_store_dwordx2 v[6:7], v5, s[4:7], s45 offen sc1\n"
      "s_lshl_b32 s45, s2, 2\n"
      "s_add_u32 s45, s45, s40\n"
      "s_add_u32 s45, s45, 2534\n"
      "s_mul_i32 s46, s45, 6473\n"
      "s_lshr_b32 s46, s46, 20\n"
      "s_mul_i32 s47, s46, 162\n"
      "s_sub_u32 s47, s45, s47\n"
      "s_mul_i32 s48, s47, 57\n"
      "s_lshr_b32 s48, s48, 9\n"
      "s_mul_i32 s49, s48, 9\n"
      "s_sub_u32 s49, s47, s49\n"
      "s_mul_i32 s76, s46, 663552\n"
      "s_mul_i32 s50, s49, 72\n"
      "s_add_u32 s50, s50, s48\n"
      "s_lshl_b32 s50, s50, 10\n"
      "s_add_u32 s76, s76, s50\n"
      "s_lshl_b32 s74, s48, 3\n"
      "s_lshl_b32 s49, s49, 7\n"
      "s_sub_u32 s50, s46, 3\n"
      "s_mul_i32 s50, s50, 1152\n"
      "s_sub_u32 s51, s46, 11\n"
      "s_mul_i32 s51, s51, 576\n"
      "s_mov_b32 s73, 1152\n"
      "s_cmp_lt_u32 s46, 11\n"
      "s_cselect_b32 s50, s50, 0\n"
      "s_cselect_b32 s51, 0, s51\n"
      "s_cselect_b64 s[16:17], s[30:31], s[32:33]\n"
      "s_cselect_b32 s73, 9216, s73\n"
      "s_add_u32 s49, s49, s50\n"
      "s_add_u32 s74, s74, s51\n"
      "s_add_u32 s75, s74, 4\n"
      "s_mov_b32 s18, 5308416\n"
      "s_and_b32 s17, s17, 0xffff\n"
      "s_mov_b32 s19, 0x20000\n"
      "s_mul_i32 s74, s74, s73\n"
      "s_mul_i32 s75, s75, s73\n"
      "s_add_u32 s74, s74, s49\n"
      "s_add_u32 s75, s75, s49\n"
      "s_cmp_lt_u32 s45, 3078\n"
      "s_cselect_b32 s18, s18, 0\n"
      "s_cselect_b32 s76, s76, 1073741824\n"
      "s_cselect_b32 s74, s74, 0\n"
      "s_cselect_b32 s75, s75, 0\n"
      "v_bfe_u32 v26, v4, 3, 1\n"
      "v_mul_u32_u24 v26, 144, v26\n"
      "v_add_u32 v26, v26, v3\n"
      "v_mul_lo_u32 v11, v26, s73\n"
      "v_and_b32 v26, 7, v4\n"
      "v_lshlrev_b32 v27, 1, v3\n"
      "v_xor_b32 v26, v26, v27\n"
      "v_lshl_add_u32 v11, v26, 4, v11\n"
      "s_mul_i32 s73, s73, 288\n"
      "s_mov_b32 s51, 12288\n"
      "s_mul_i32 s44, s40, 6\n"
      "s_mov_b32 exec_hi, 0\n"
      "s_lshl_b32 s44, 63, s44\n"
      "s_and_b32 exec_lo, s44, 8388607\n"
      "v_mov_b32 v26, 0\n"
      "v_mov_b32 v27, 0\n"
      "buffer_atomic_or_x2 v[26:27], v37, s[4:7], s51 offen sc0 sc1\n"
      "s_mov_b64 exec, -1\n"
      "s_mov_b32 m0, s77\n"
      "v_add_u32 v28, s76, v1\n"
      "buffer_load_dwordx4 v11, s[16:19], s74 offen lds\n"
      "s_add_u32 m0, s77, 1024\n"
      "s_add_u32 s74, s74, s73\n"
      "buffer_load_dwordx4 v11, s[16:19], s75 offen lds\n"
      "s_add_u32 m0, s77, 2048\n"
      "s_add_u32 s75, s75, s73\n"
      "buffer_load_dwordx4 v11, s[16:19], s74 offen lds\n"
      "s_add_u32 m0, s77, 3072\n"
      "s_nop 0\n"
      "buffer_load_dwordx4 v11, s[16:19], s75 offen lds\n"
      "s_mul_i32 s44, s40, 6\n"
      "s_mov_b32 exec_hi, 0\n"
      "s_lshl_b32 s44, 63, s44\n"
      "s_and_b32 exec_lo, s44, 8388607\n"
      "s_waitcnt vmcnt(4)\n"
      "v_cmp_ne_u64 vcc, s[42:43], v[26:27]\n"
      "s_cbranch_vccz .LD13_%=\n"
      "s_mov_b32 s44, 0\n"
      ".LP12_%=:\n"
      "s_mov_b64 exec, vcc\n"
      "v_mov_b32 v26, 0\n"
      "v_mov_b32 v27, 0\n"
      "buffer_atomic_or_x2 v[26:27], v37, s[4:7], s51 offen sc0 sc1\n"
      "s_waitcnt vmcnt(0)\n"
      "v_cmp_ne_u64 vcc, s[42:43], v[26:27]\n"
      "s_cbranch_vccz .LD13_%=\n"
      "s_add_u32 s44, s44, 1\n"
      "s_cmp_lt_u32 s44, s41\n"
      "s_cbranch_scc1 .LP12_%=\n"
      "s_mov_b32 s41, 0\n"
      ".LD13_%=:\n"
      "s_mov_b64 exec, -1\n"
      "s_barrier\n"
      "s_mov_b32 s69, 6072320\n"
      "buffer_load_dwordx4 v[56:59], v36, s[4:7], s69 offen offset:0 sc1\n"
      "buffer_load_dwordx4 v[60:63], v36, s[4:7], s69 offen offset:1024 sc1\n"
      "buffer_load_dwordx4 v[64:67], v36, s[4:7], s69 offen offset:2048 sc1\n"
      "ds_read_b128 a[0:3], v1 offset:0\n"
      "buffer_load_dwordx4 v[68:71], v36, s[4:7], s69 offen offset:3072 sc1\n"
      "ds_read_b128 a[4:7], v1 offset:18432\n"
      "ds_read_b128 a[8:11], v30 offset:0\n"
      "ds_read_b128 a[12:15], v30 offset:18432\n"
      "ds_read_b128 v[180:183], v87 offset:0\n"
      "s_mov_b32 s69, 6076416\n"
      "buffer_load_dwordx4 v[76:79], v36, s[4:7], s69 offen offset:0 sc1\n"
      "ds_read_b128 a[16:19], v1 offset:1024\n"
      "ds_read_b128 a[20:23], v1 offset:19456\n"
      "ds_read_b128 a[24:27], v30 offset:1024\n"
      "ds_read_b128 a[28:31], v30 offset:19456\n"
      "buffer_load_dwordx4 v[92:95], v36, s[4:7], s69 offen offset:1024 sc1\n"
      "ds_read_b128 v[184:187], v87 offset:1024\n"
      "ds_read_b128 a[32:35], v1 offset:2048\n"
      "ds_read_b128 a[36:39], v1 offset:20480\n"
      "ds_read_b128 a[40:43], v30 offset:2048\n"
      "buffer_load_dwordx4 v[128:131], v36, s[4:7], s69 offen offset:2048 sc1\n"
      "ds_read_b128 a[44:47], v30 offset:20480\n"
      "ds_read_b128 v[188:191], v87 offset:2048\n"
      "ds_read_b128 a[48:51], v1 offset:3072\n"
      "ds_read_b128 a[52:55], v1 offset:21504\n"
      "buffer_load_dwordx4 v[132:135], v36, s[4:7], s69 offen offset:3072 sc1\n"
      "ds_read_b128 a[56:59], v30 offset:3072\n"
      "ds_read_b128 a[60:63], v30 offset:21504\n"
      "ds_read_b128 v[192:195], v87 offset:3072\n"
      "ds_read_b128 a[64:67], v1 offset:4096\n"
      "s_mov_b32 s69, 6080512\n"
      "buffer_load_dwordx4 v[136:139], v36, s[4:7], s69 offen offset:0 sc1\n"
      "ds_read_b128 a[68:71], v1 offset:22528\n"
      "ds_read_b128 a[72:75], v30 offset:4096\n"
      "ds_read_b128 a[76:79], v30 offset:22528\n"
      "ds_read_b128 a[80:83], v1 offset:5120\n"
      "buffer_load_dwordx4 v[140:143], v36, s[4:7], s69 offen offset:1024 sc1\n"
      "ds_read_b128 a[84:87], v1 offset:23552\n"
      "ds_read_b128 a[88:91], v30 offset:5120\n"
      "ds_read_b128 a[92:95], v30 offset:23552\n"
      "ds_read_b128 a[96:99], v1 offset:6144\n"
      "buffer_load_dwordx4 v[144:147], v36, s[4:7], s69 offen offset:2048 sc1\n"
      "ds_read_b128 a[100:103], v1 offset:24576\n"
      "ds_read_b128 a[104:107], v30 offset:6144\n"
      "ds_read_b128 a[108:111], v30 offset:24576\n"
      "ds_read_b128 a[112:115], v1 offset:7168\n"
      "buffer_load_dwordx4 v[148:151], v36, s[4:7], s69 offen offset:3072 sc1\n"
      "ds_read_b128 a[116:119], v1 offset:25600\n"
      "ds_read_b128 a[120:123], v30 offset:7168\n"
      "ds_read_b128 a[124:127], v30 offset:25600\n"
      "s_waitcnt vmcnt(11) lgkmcnt(15)\n"
      "v_mfma_f32_16x16x32_bf16 v[212:215], v[180:183], v[56:59], v[212:215]\n"
      "ds_read_b128 v[180:183], v87 offset:4096\n"
      "ds_read_b128 a[128:131], v1 offset:8192\n"
      "v_mfma_f32_16x16x32_bf16 v[196:199], a[0:3], v[56:59], v[196:199]\n"
      "ds_read_b128 a[132:135], v1 offset:26624\n"
      "v_mfma_f32_16x16x32_bf16 v[200:203], a[4:7], v[56:59], v[200:203]\n"
      "ds_read_b128 a[136:139], v30 offset:8192\n"
      "v_mfma_f32_16x16x32_bf16 v[204:207], a[8:11], v[56:59], v[204:207]\n"
      "ds_read_b128 a[140:143], v30 offset:26624\n"
      "v_mfma_f32_16x16x32_bf16 v[208:211], a[12:15], v[56:59], v[208:211]\n"
      "s_mov_b32 s69, 6084608\n"
      "buffer_load_dwordx4 v[152:155], v36, s[4:7], s69 offen offset:0 sc1\n"
      "s_waitcnt vmcnt(11) lgkmcnt(15)\n"
      "v_mfma_f32_16x16x32_bf16 v[212:215], v[184:187], v[60:63], v[212:215]\n"
      "ds_read_b128 v[184:187], v87 offset:5120\n"
      "ds_read_b128 a[144:147], v1 offset:9216\n"
      "v_mfma_f32_16x16x32_bf16 v[196:199], a[16:19], v[60:63], v[196:199]\n"
      "ds_read_b128 a[148:151], v1 offset:27648\n"
      "v_mfma_f32_16x16x32_bf16 v[200:203], a[20:23], v[60:63], v[200:203]\n"
      "ds_read_b128 a[152:155], v30 offset:9216\n"
      "v_mfma_f32_16x16x32_bf16 v[204:207], a[24:27], v[60:63], v[204:207]\n"
      "ds_read_b128 a[156:159], v30 offset:27648\n"
      "v_mfma_f32_16x16x32_bf16 v[208:211], a[28:31], v[60:63], v[208:211]\n"
      "buffer_load_dwordx4 v[156:159], v36, s[4:7], s69 offen offset:1024 sc1\n"
      "s_waitcnt vmcnt(11) lgkmcnt(15)\n"
      "v_mfma_f32_16x16x32_bf16 v[212:215], v[188:191], v[64:67], v[212:215]\n"
      "ds_read_b128 v[188:191], v87 offset:6144\n"
      "ds_read_b128 a[160:163], v1 offset:10240\n"
      "v_mfma_f32_16x16x32_bf16 v[196:199], a[32:35], v[64:67], v[196:199]\n"
      "ds_read_b128 a[164:167], v1 offset:28672\n"
      "v_mfma_f32_16x16x32_bf16 v[200:203], a[36:39], v[64:67], v[200:203]\n"
      "ds_read_b128 a[168:171], v30 offset:10240\n"
      "v_mfma_f32_16x16x32_bf16 v[204:207], a[40:43], v[64:67], v[204:207]\n"
      "ds_read_b128 a[172:175], v30 offset:28672\n"
      "v_mfma_f32_16x16x32_bf16 v[208:211], a[44:47], v[64:67], v[208:211]\n"
      "buffer_load_dwordx4 v[160:163], v36, s[4:7], s69 offen offset:2048 sc1\n"
      "s_waitcnt vmcnt(11) lgkmcnt(15)\n"
      "v_mfma_f32_16x16x32_bf16 v[212:215], v[192:195], v[68:71], v[212:215]\n"
      "ds_read_b128 v[192:195], v87 offset:7168\n"
      "ds_read_b128 a[176:179], v1 offset:11264\n"
      "v_mfma_f32_16x16x32_bf16 v[196:199], a[48:51], v[68:71], v[196:199]\n"
      "ds_read_b128 a[180:183], v1 offset:29696\n"
      "v_mfma_f32_16x16x32_bf16 v[200:203], a[52:55], v[68:71], v[200:203]\n"
      "ds_read_b128 a[184:187], v30 offset:11264\n"
      "v_mfma_f32_16x16x32_bf16 v[204:207], a[56:59], v[68:71], v[204:207]\n"
      "ds_read_b128 a[188:191], v30 offset:29696\n"
      "v_mfma_f32_16x16x32_bf16 v[208:211], a[60:63], v[68:71], v[208:211]\n"
      "buffer_load_dwordx4 v[164:167], v36, s[4:7], s69 offen offset:3072 sc1\n"
      "s_waitcnt vmcnt(11) lgkmcnt(15)\n"
      "v_mfma_f32_16x16x32_bf16 v[212:215], v[180:183], v[76:79], v[212:215]\n"
      "ds_read_b128 v[180:183], v87 offset:8192\n"
      "ds_read_b128 a[192:195], v1 offset:12288\n"
      "v_mfma_f32_16x16x32_bf16 v[196:199], a[64:67], v[76:79], v[196:199]\n"
      "ds_read_b128 a[196:199], v1 offset:30720\n"
      "v_mfma_f32_16x16x32_bf16 v[200:203], a[68:71], v[76:79], v[200:203]\n"
      "ds_read_b128 a[200:203], v30 offset:12288\n"
      "v_mfma_f32_16x16x32_bf16 v[204:207], a[72:75], v[76:79], v[204:207]\n"
      "ds_read_b128 a[204:207], v30 offset:30720\n"
      "v_mfma_f32_16x16x32_bf16 v[208:211], a[76:79], v[76:79], v[208:211]\n"
      "s_mov_b32 s69, 6088704\n"
      "buffer_load_dwordx4 v[168:171], v36, s[4:7], s69 offen offset:0 sc1\n"
      "s_waitcnt vmcnt(11) lgkmcnt(15)\n"
      "v_mfma_f32_16x16x32_bf16 v[212:215], v[184:187], v[92:95], v[212:215]\n"
      "ds_read_b128 v[184:187], v87 offset:9216\n"
      "ds_read_b128 a[208:211], v1 offset:13312\n"
      "v_mfma_f32_16x16x32_bf16 v[196:199], a[80:83], v[92:95], v[196:199]\n"
      "ds_read_b128 a[212:215], v1 offset:31744\n"
      "v_mfma_f32_16x16x32_bf16 v[200:203], a[84:87], v[92:95], v[200:203]\n"
      "ds_read_b128 a[216:219], v30 offset:13312\n"
      "v_mfma_f32_16x16x32_bf16 v[204:207], a[88:91], v[92:95], v[204:207]\n"
      "ds_read_b128 a[220:223], v30 offset:31744\n"
      "v_mfma_f32_16x16x32_bf16 v[208:211], a[92:95], v[92:95], v[208:211]\n"
      "buffer_load_dwordx4 v[172:175], v36, s[4:7], s69 offen offset:1024 sc1\n"
      "s_waitcnt vmcnt(11) lgkmcnt(15)\n"
      "v_mfma_f32_16x16x32_bf16 v[212:215], v[188:191], v[128:131], v[212:215]\n"
      "ds_read_b128 v[188:191], v87 offset:10240\n"
      "ds_read_b128 a[224:227], v1 offset:14336\n"
      "v_mfma_f32_16x16x32_bf16 v[196:199], a[96:99], v[128:131], v[196:199]\n"
      "ds_read_b128 a[228:231], v1 offset:32768\n"
      "v_mfma_f32_16x16x32_bf16 v[200:203], a[100:103], v[128:131], v[200:203]\n"
      "ds_read_b128 a[232:235], v30 offset:14336\n"
      "v_mfma_f32_16x16x32_bf16 v[204:207], a[104:107], v[128:131], v[204:207]\n"
      "ds_read_b128 a[236:239], v30 offset:32768\n"
      "v_mfma_f32_16x16x32_bf16 v[208:211], a[108:111], v[128:131], v[208:211]\n"
      "s_mov_b32 s52, 3072\n"
      "buffer_load_dwordx4 v[48:51], v1, s[4:7], s52 offen sc1\n"
      "buffer_load_dwordx4 v[52:55], v1, s[4:7], s52 offen offset:1024 sc1\n"
      "s_waitcnt vmcnt(12) lgkmcnt(15)\n"
      "v_mfma_f32_16x16x32_bf16 v[212:215], v[192:195], v[132:135], v[212:215]\n"
      "ds_read_b128 v[192:195], v87 offset:11264\n"
      "ds_read_b128 a[240:243], v1 offset:15360\n"
      "v_mfma_f32_16x16x32_bf16 v[196:199], a[112:115], v[132:135], v[196:199]\n"
      "ds_read_b128 a[244:247], v1 offset:33792\n"
      "v_mfma_f32_16x16x32_bf16 v[200:203], a[116:119], v[132:135], v[200:203]\n"
      "ds_read_b128 a[248:251], v30 offset:15360\n"
      "v_mfma_f32_16x16x32_bf16 v[204:207], a[120:123], v[132:135], v[204:207]\n"
      "ds_read_b128 a[252:255], v30 offset:33792\n"
      "v_mfma_f32_16x16x32_bf16 v[208:211], a[124:127], v[132:135], v[208:211]\n"
      "buffer_load_dword v8, v9, s[60:63], 0 offen\n"
      "s_waitcnt vmcnt(12) lgkmcnt(15)\n"
      "v_mfma_f32_16x16x32_bf16 v[212:215], v[180:183], v[136:139], v[212:215]\n"
      "ds_read_b128 v[180:183], v87 offset:12288\n"
      "ds_read_b128 v[96:99], v1 offset:16384\n"
      "v_mfma_f32_16x16x32_bf16 v[196:199], a[128:131], v[136:139], v[196:199]\n"
      "ds_read_b128 v[100:103], v1 offset:34816\n"
      "v_mfma_f32_16x16x32_bf16 v[200:203], a[132:135], v[136:139], v[200:203]\n"
      "ds_read_b128 v[104:107], v30 offset:16384\n"
      "v_mfma_f32_16x16x32_bf16 v[204:207], a[136:139], v[136:139], v[204:207]\n"
      "ds_read_b128 v[108:111], v30 offset:34816\n"
      "v_mfma_f32_16x16x32_bf16 v[208:211], a[140:143], v[136:139], v[208:211]\n"
      "ds_read_b64_tr_b16 v[240:241], v32\n"
      "ds_read_b64_tr_b16 v[242:243], v32 offset:1024\n"
      "ds_read_b64_tr_b16 v[244:245], v33\n"
      "ds_read_b64_tr_b16 v[246:247], v33 offset:1024\n"
      "ds_read_b64_tr_b16 v[248:249], v34\n"
      "ds_read_b64_tr_b16 v[250:251], v34 offset:1024\n"
      "ds_read_b64_tr_b16 v[252:253], v35\n"
      "ds_read_b64_tr_b16 v[254:255], v35 offset:1024\n"
      "s_waitcnt vmcnt(11) lgkmcnt(15)\n"
      "v_mfma_f32_16x16x32_bf16 v[212:215], v[184:187], v[140:143], v[212:215]\n"
      "ds_read_b128 v[184:187], v87 offset:13312\n"
      "ds_read_b128 v[112:115], v1 offset:17408\n"
      "v_mfma_f32_16x16x32_bf16 v[196:199], a[144:147], v[140:143], v[196:199]\n"
      "ds_read_b128 v[116:119], v1 offset:35840\n"
      "v_mfma_f32_16x16x32_bf16 v[200:203], a[148:151], v[140:143], v[200:203]\n"
      "ds_read_b128 v[120:123], v30 offset:17408\n"
      "v_mfma_f32_16x16x32_bf16 v[204:207], a[152:155], v[140:143], v[204:207]\n"
      "ds_read_b128 v[124:127], v30 offset:35840\n"
      "v_mfma_f32_16x16x32_bf16 v[208:211], a[156:159], v[140:143], v[208:211]\n"
      "s_mov_b32 s52, 10790912\n"
      "s_waitcnt lgkmcnt(11)\n"
      "buffer_store_dwordx4 v[240:243], v28, s[4:7], s52 offen sc1\n"
      "s_waitcnt vmcnt(11)\n"
      "v_mfma_f32_16x16x32_bf16 v[212:215], v[188:191], v[144:147], v[212:215]\n"
      "ds_read_b128 v[188:191], v87 offset:14336\n"
      "v_mfma_f32_16x16x32_bf16 v[196:199], a[160:163], v[144:147], v[196:199]\n"
      "v_mfma_f32_16x16x32_bf16 v[200:203], a[164:167], v[144:147], v[200:203]\n"
      "s_mov_b32 s52, 10809344\n"
      "s_waitcnt lgkmcnt(10)\n"
      "buffer_store_dwordx4 v[244:247], v28, s[4:7], s52 offen sc1\n"
      "v_mfma_f32_16x16x32_bf16 v[204:207], a[168:171], v[144:147], v[204:207]\n"
      "v_mfma_f32_16x16x32_bf16 v[208:211], a[172:175], v[144:147], v[208:211]\n"
      "s_waitcnt vmcnt(11)\n"
      "v_mfma_f32_16x16x32_bf16 v[212:215], v[192:195], v[148:151], v[212:215]\n"
      "ds_read_b128 v[192:195], v87 offset:15360\n"
      "v_mfma_f32_16x16x32_bf16 v[196:199], a[176:179], v[148:151], v[196:199]\n"
      "v_mfma_f32_16x16x32_bf16 v[200:203], a[180:183], v[148:151], v[200:203]\n"
      "s_mov_b32 s52, 10827776\n"
      "s_waitcnt lgkmcnt(9)\n"
      "buffer_store_dwordx4 v[248:251], v28, s[4:7], s52 offen sc1\n"
      "v_mfma_f32_16x16x32_bf16 v[204:207], a[184:187], v[148:151], v[204:207]\n"
      "v_mfma_f32_16x16x32_bf16 v[208:211], a[188:191], v[148:151], v[208:211]\n"
      "s_waitcnt vmcnt(11)\n"
      "v_mfma_f32_16x16x32_bf16 v[212:215], v[180:183], v[152:155], v[212:215]\n"
      "ds_read_b128 v[180:183], v87 offset:16384\n"
      "v_mfma_f32_16x16x32_bf16 v[196:199], a[192:195], v[152:155], v[196:199]\n"
      "v_mfma_f32_16x16x32_bf16 v[200:203], a[196:199], v[152:155], v[200:203]\n"
      "s_mov_b32 s52, 10846208\n"
      "s_waitcnt lgkmcnt(8)\n"
      "buffer_store_dwordx4 v[252:255], v28, s[4:7], s52 offen sc1\n"
      "v_mfma_f32_16x16x32_bf16 v[204:207], a[200:203], v[152:155], v[204:207]\n"
      "v_mfma_f32_16x16x32_bf16 v[208:211], a[204:207], v[152:155], v[208:211]\n"
      "s_waitcnt vmcnt(11) lgkmcnt(7)\n"
      "v_mfma_f32_16x16x32_bf16 v[212:215], v[184:187], v[156:159], v[212:215]\n"
      "ds_read_b128 v[184:187], v87 offset:17408\n"
      "v_mfma_f32_16x16x32_bf16 v[196:199], a[208:211], v[156:159], v[196:199]\n"
      "v_mfma_f32_16x16x32_bf16 v[200:203], a[212:215], v[156:159], v[200:203]\n"
      "v_mfma_f32_16x16x32_bf16 v[204:207], a[216:219], v[156:159], v[204:207]\n"
      "v_mfma_f32_16x16x32_bf16 v[208:211], a[220:223], v[156:159], v[208:211]\n"
      "s_waitcnt vmcnt(10) lgkmcnt(3)\n"
      "v_mfma_f32_16x16x32_bf16 v[212:215], v[188:191], v[160:163], v[212:215]\n"
      "v_mfma_f32_16x16x32_bf16 v[196:199], a[224:227], v[160:163], v[196:199]\n"
      "v_mfma_f32_16x16x32_bf16 v[200:203], a[228:231], v[160:163], v[200:203]\n"
      "v_mfma_f32_16x16x32_bf16 v[204:207], a[232:235], v[160:163], v[204:207]\n"
      "v_mfma_f32_16x16x32_bf16 v[208:211], a[236:239], v[160:163], v[208:211]\n"
      "s_waitcnt vmcnt(9) lgkmcnt(2)\n"
      "s_waitcnt lgkmcnt(0)\n"
      "s_barrier\n"
      "v_mfma_f32_16x16x32_bf16 v[212:215], v[192:195], v[164:167], v[212:215]\n"
      "v_mfma_f32_16x16x32_bf16 v[196:199], a[240:243], v[164:167], v[196:199]\n"
      "v_mfma_f32_16x16x32_bf16 v[200:203], a[244:247], v[164:167], v[200:203]\n"
      "v_mfma_f32_16x16x32_bf16 v[204:207], a[248:251], v[164:167], v[204:207]\n"
      "v_mfma_f32_16x16x32_bf16 v[208:211], a[252:255], v[164:167], v[208:211]\n"
      "s_waitcnt vmcnt(8)\n"
      "v_mfma_f32_16x16x32_bf16 v[212:215], v[180:183], v[168:171], v[212:215]\n"
      "s_waitcnt vmcnt(7)\n"
      "v_mfma_f32_16x16x32_bf16 v[212:215], v[184:187], v[172:175], v[212:215]\n"
      "v_mfma_f32_16x16x32_bf16 v[196:199], v[96:99], v[168:171], v[196:199]\n"
      "v_mfma_f32_16x16x32_bf16 v[196:199], v[112:115], v[172:175], v[196:199]\n"
      "v_mfma_f32_16x16x32_bf16 v[200:203], v[100:103], v[168:171], v[200:203]\n"
      "s_nop 4\n"
      "v_mov_b32 v24, v212\n"
      "v_mfma_f32_16x16x32_bf16 v[200:203], v[116:119], v[172:175], v[200:203]\n"
      "v_mfma_f32_16x16x32_bf16 v[204:207], v[104:107], v[168:171], v[204:207]\n"
      "v_mov_b32 v25, v212\n"
      "v_mfma_f32_16x16x32_bf16 v[204:207], v[120:123], v[172:175], v[204:207]\n"
      "v_mfma_f32_16x16x32_bf16 v[208:211], v[108:111], v[168:171], v[208:211]\n"
      "v_permlane32_swap_b32 v24, v25\n"
      "v_mfma_f32_16x16x32_bf16 v[208:211], v[124:127], v[172:175], v[208:211]\n"
      "v_sub_f32 v76, v212, v25\n"
      "v_sub_f32 v77, v213, v25\n"
      "v_sub_f32 v78, v214, v25\n"
      "v_sub_f32 v79, v215, v25\n"
      "v_cvt_pk_bf16_f32 v20, v76, v77\n"
      "v_cvt_pk_bf16_f32 v21, v78, v79\n"
      "v_cvt_pk_bf16_f32 v22, v76, v77\n"
      "v_cvt_pk_bf16_f32 v23, v78, v79\n"
      "s_mov_b32 s46, 8431616\n"
      "s_mov_b32 s47, 8435712\n"
      "v_permlane16_swap_b32 v20, v22\n"
      "v_permlane16_swap_b32 v21, v23\n"
      "buffer_store_dwordx4 v[20:23], v74, s[4:7], s46 offen sc1\n"
      "v_sub_f32 v56, v196, v25\n"
      "v_sub_f32 v57, v197, v25\n"
      "v_sub_f32 v58, v198, v25\n"
      "v_sub_f32 v59, v199, v25\n"
      "v_cvt_pk_bf16_f32 v12, v56, v57\n"
      "v_cvt_pk_bf16_f32 v13, v58, v59\n"
      "v_sub_f32 v60, v200, v25\n"
      "v_sub_f32 v61, v201, v25\n"
      "v_sub_f32 v62, v202, v25\n"
      "v_sub_f32 v63, v203, v25\n"
      "v_cvt_pk_bf16_f32 v14, v60, v61\n"
      "v_cvt_pk_bf16_f32 v15, v62, v63\n"
      "s_nop 0\n"
      "v_permlane16_swap_b32 v12, v14\n"
      "v_permlane16_swap_b32 v13, v15\n"
      "buffer_store_dwordx4 v[12:15], v84, s[4:7], s46 offen sc1\n"
      "v_sub_f32 v64, v204, v25\n"
      "v_sub_f32 v65, v205, v25\n"
      "v_sub_f32 v66, v206, v25\n"
      "v_sub_f32 v67, v207, v25\n"
      "v_cvt_pk_bf16_f32 v16, v64, v65\n"
      "v_cvt_pk_bf16_f32 v17, v66, v67\n"
      "v_sub_f32 v68, v208, v25\n"
      "v_sub_f32 v69, v209, v25\n"
      "v_sub_f32 v70, v210, v25\n"
      "v_sub_f32 v71, v211, v25\n"
      "v_cvt_pk_bf16_f32 v18, v68, v69\n"
      "v_cvt_pk_bf16_f32 v19, v70, v71\n"
      "s_nop 0\n"
      "v_permlane16_swap_b32 v16, v18\n"
      "v_permlane16_swap_b32 v17, v19\n"
      "buffer_store_dwordx4 v[16:19], v84, s[4:7], s47 offen sc1\n"
      "s_waitcnt vmcnt(8)\n"
      "s_mov_b32 s44, 0\n"
      ".LP14_%=:\n"
      "v_cmp_ne_u64 vcc, s[42:43], v[48:49]\n"
      "v_cmp_ne_u64 s[58:59], s[42:43], v[50:51]\n"
      "s_or_b64 vcc, vcc, s[58:59]\n"
      "v_cmp_ne_u64 s[58:59], s[42:43], v[52:53]\n"
      "s_or_b64 vcc, vcc, s[58:59]\n"
      "v_cmp_ne_u64 s[58:59], s[42:43], v[54:55]\n"
      "s_or_b64 vcc, vcc, s[58:59]\n"
      "s_cbranch_scc0 .LD15_%=\n"
      "s_mov_b32 s52, 3072\n"
      "buffer_load_dwordx4 v[48:51], v1, s[4:7], s52 offen sc1\n"
      "buffer_load_dwordx4 v[52:55], v1, s[4:7], s52 offen offset:1024 sc1\n"
      "s_waitcnt vmcnt(0)\n"
      "s_add_u32 s44, s44, 1\n"
      "s_cmp_lt_u32 s44, s41\n"
      "s_cbranch_scc1 .LP14_%=\n"
      "s_mov_b32 s41, 0\n"
      ".LD15_%=:\n"
      "s_mul_i32 s65, s39, 663552\n"
      "s_add_u32 s53, s65, 18089984\n"
      "s_mul_i32 s45, s40, 165888\n"
      "s_add_u32 s65, s65, s45\n"
      "s_add_u32 s65, s65, 12781568\n"
      "s_add_u32 s53, s53, s66\n"
      "buffer_load_dwordx4 v[56:59], v1, s[4:7], s65 offen offset:0 sc1\n"
      "v_add_f32 v220, v220, v196\n"
      "v_add_f32 v221, v221, v197\n"
      "v_add_f32 v222, v222, v198\n"
      "v_add_f32 v223, v223, v199\n"
      "v_add_f32 v224, v224, v200\n"
      "v_add_f32 v225, v225, v201\n"
      "v_add_f32 v226, v226, v202\n"
      "buffer_load_dwordx4 v[60:63], v1, s[4:7], s65 offen offset:1024 sc1\n"
      "v_add_f32 v227, v227, v203\n"
      "v_add_f32 v228, v228, v204\n"
      "v_add_f32 v229, v229, v205\n"
      "v_add_f32 v230, v230, v206\n"
      "v_add_f32 v231, v231, v207\n"
      "v_add_f32 v232, v232, v208\n"
      "v_add_f32 v233, v233, v209\n"
      "buffer_load_dwordx4 v[64:67], v1, s[4:7], s65 offen offset:2048 sc1\n"
      "v_add_f32 v234, v234, v210\n"
      "v_add_f32 v235, v235, v211\n"
      "v_add_f32 v236, v236, v212\n"
      "v_add_f32 v237, v237, v213\n"
      "v_add_f32 v238, v238, v214\n"
      "v_add_f32 v239, v239, v215\n"
      "s_lshl_b32 s45, s40, 10\n"
      "s_add_u32 s45, s45, 122880\n"
      "buffer_load_dwordx4 v[68:71], v1, s[4:7], s65 offen offset:3072 sc1\n"
      "v_add_u32 v24, s45, v1\n"
      "s_add_u32 s65, s65, 4096\n"
      "buffer_load_dwordx4 v[76:79], v1, s[4:7], s65 offen offset:0 sc1\n"
      "ds_write_b128 v24, v[220:223] offset:0\n"
      "ds_write_b128 v24, v[224:227] offset:4096\n"
      "ds_write_b128 v24, v[228:231] offset:8192\n"
      "ds_write_b128 v24, v[232:235] offset:12288\n"
      "v_lshrrev_b32 v14, 1, v3\n"
      "v_lshlrev_b32 v14, 10, v14\n"
      "v_and_b32 v15, 1, v3\n"
      "v_lshl_add_u32 v14, v15, 3, v14\n"
      "s_add_u32 s45, s66, -86272\n"
      "v_add_u32 v14, s45, v14\n"
      "v_add_u32 v44, v14, v88\n"
      "v_add_u32 v45, v14, v89\n"
      "v_add_u32 v46, v14, v90\n"
      "buffer_load_dwordx4 v[92:95], v1, s[4:7], s65 offen offset:1024 sc1\n"
      "v_add_u32 v47, v14, v91\n"
      "s_lshl_b32 s73, s38, 3\n"
      "s_add_u32 s73, s73, s39\n"
      "s_mul_i32 s73, s73, 36\n"
      "s_add_u32 s73, s73, s40\n"
      "s_lshl_b32 s73, s73, 11\n"
      "s_add_u32 s73, s73, 23398400\n"
      "s_mul_i32 s77, s38, 4608\n"
      "s_add_u32 s77, s77, 401408\n"
      "s_lshl_b32 s74, s40, 7\n"
      "s_lshl_b32 s47, s39, 3\n"
      "s_add_u32 s74, s74, s47\n"
      "buffer_load_dwordx4 v[128:131], v1, s[4:7], s65 offen offset:2048 sc1\n"
      "s_add_u32 s74, s74, s77\n"
      "v_cmp_eq_u32 vcc, 0, v4\n"
      "v_mov_b32 v11, 1073741824\n"
      "v_lshrrev_b32 v20, 2, v2\n"
      "v_cndmask_b32 v11, v11, v4, vcc\n"
      "v_and_b32 v21, 3, v2\n"
      "v_lshl_add_u32 v20, v20, 3, v21\n"
      "v_lshlrev_b32 v24, 3, v3\n"
      "buffer_load_dwordx4 v[132:135], v1, s[4:7], s65 offen offset:3072 sc1\n"
      "v_sub_u32 v25, v20, v24\n"
      "v_and_b32 v26, 1, v25\n"
      "v_lshlrev_b32 v26, 4, v26\n"
      "v_mov_b32 v27, 0x3f80\n"
      "v_lshlrev_b32 v26, v26, v27\n"
      "v_lshrrev_b32 v25, 1, v25\n"
      "v_cmp_eq_u32 vcc, 0, v25\n"
      "s_add_u32 s65, s65, 4096\n"
      "buffer_load_dwordx4 v[136:139], v1, s[4:7], s65 offen offset:0 sc1\n"
      "v_cndmask_b32 v188, 0, v26, vcc\n"
      "v_cmp_eq_u32 vcc, 1, v25\n"
      "s_nop 1\n"
      "v_cndmask_b32 v189, 0, v26, vcc\n"
      "v_cmp_eq_u32 vcc, 2, v25\n"
      "s_nop 1\n"
      "v_cndmask_b32 v190, 0, v26, vcc\n"
      "v_cmp_eq_u32 vcc, 3, v25\n"
      "buffer_load_dwordx4 v[140:143], v1, s[4:7], s65 offen offset:1024 sc1\n"
      "s_nop 0\n"
      "v_cndmask_b32 v191, 0, v26, vcc\n"
      "v_mov_b32 v192, 0\n"
      "v_mov_b32 v193, 0\n"
      "v_mov_b32 v194, v188\n"
      "v_mov_b32 v195, v189\n"
      "s_lshr_b32 s47, s39, 1\n"
      "s_xor_b32 s47, s47, s40\n"
      "s_and_b32 s48, s47, 1\n"
      "s_lshl_b32 s48, s48, 2\n"
      "buffer_load_dwordx4 v[144:147], v1, s[4:7], s65 offen offset:2048 sc1\n"
      "v_cmp_gt_u32 vcc, 8, v2\n"
      "v_add_u32 v20, s48, v20\n"
      "v_and_b32 v21, 2, v3\n"
      "v_lshl_add_u32 v20, v21, 3, v20\n"
      "v_cndmask_b32 v20, -1, v20, vcc\n"
      "v_lshlrev_b32 v24, 3, v3\n"
      "v_sub_u32 v25, v20, v24\n"
      "buffer_load_dwordx4 v[148:151], v1, s[4:7], s65 offen offset:3072 sc1\n"
      "v_and_b32 v26, 1, v25\n"
      "v_lshlrev_b32 v26, 4, v26\n"
      "v_mov_b32 v27, 0x3f80\n"
      "v_lshlrev_b32 v26, v26, v27\n"
      "v_lshrrev_b32 v25, 1, v25\n"
      "v_cmp_eq_u32 vcc, 0, v25\n"
      "s_nop 1\n"
      "v_cndmask_b32 v212, 0, v26, vcc\n"
      "s_add_u32 s65, s65, 4096\n"
      "buffer_load_dwordx4 v[152:155], v1, s[4:7], s65 offen offset:0 sc1\n"
      "v_cmp_eq_u32 vcc, 1, v25\n"
      "s_nop 1\n"
      "v_cndmask_b32 v213, 0, v26, vcc\n"
      "v_cmp_eq_u32 vcc, 2, v25\n"
      "s_nop 1\n"
      "v_cndmask_b32 v214, 0, v26, vcc\n"
      "v_cmp_eq_u32 vcc, 3, v25\n"
      "s_nop 1\n"
      "v_cndmask_b32 v215, 0, v26, vcc\n"
      "s_waitcnt vmcnt(13)\n"
      "s_waitcnt lgkmcnt(0)\n"
      "s_barrier\n"
      "s_add_u32 s45, s67, 204800\n"
      "buffer_store_dwordx2 v[6:7], v10, s[4:7], s45 offen sc1\n"
      "s_add_u32 s45, s72, 5120\n"
      "buffer_store_dwordx2 v[6:7], v5, s[4:7], s45 offen sc1\n"
      "buffer_load_dwordx4 v[156:159], v1, s[4:7], s65 offen offset:1024 sc1\n"
      "v_lshrrev_b32 v20, 1, v3\n"
      "v_mul_u32_u24 v20, 294912, v20\n"
      "v_and_b32 v21, 0x1f0, v1\n"
      "v_add_u32 v177, v20, v21\n"
      "s_mul_i32 s76, s38, 589824\n"
      "s_add_u32 s76, s76, 23398400\n"
      "s_lshr_b32 s48, s39, 1\n"
      "s_add_u32 s48, s48, 32\n"
      "s_lshl_b32 s75, s48, 11\n"
      "s_add_u32 s75, s75, s76\n"
      "s_lshr_b32 s47, s47, 1\n"
      "s_lshl_b32 s47, s47, 10\n"
      "s_add_u32 s75, s75, s47\n"
      "s_and_b32 s47, s39, 1\n"
      "s_lshl_b32 s47, s47, 9\n"
      "s_add_u32 s75, s75, s47\n"
      "s_lshl_b32 s47, s39, 2\n"
      "s_add_u32 s47, s47, s40\n"
      "s_lshl_b32 s49, s47, 11\n"
      "s_add_u32 s76, s76, s49\n"
      "buffer_load_dwordx4 v[160:163], v1, s[4:7], s65 offen offset:2048 sc1\n"
      "v_and_b32 v20, 8, v4\n"
      "v_cmp_eq_u32 vcc, 0, v20\n"
      "v_and_b32 v20, 3, v4\n"
      "v_add3_u32 v21, v20, s39, 4\n"
      "v_cndmask_b32 v21, v4, v21, vcc\n"
      "v_and_b32 v21, 7, v21\n"
      "v_mov_b32 v22, s47\n"
      "v_mov_b32 v23, s48\n"
      "v_cndmask_b32 v23, v23, v22, vcc\n"
      "v_lshlrev_b32 v21, 3, v21\n"
      "v_lshl_add_u32 v178, v23, 7, v21\n"
      "v_min_u32 v20, 2, v20\n"
      "buffer_load_dwordx4 v[164:167], v1, s[4:7], s65 offen offset:3072 sc1\n"
      "v_add3_u32 v21, v20, s39, 1\n"
      "v_and_b32 v21, 7, v21\n"
      "v_lshlrev_b32 v21, 3, v21\n"
      "v_lshl_add_u32 v179, v22, 7, v21\n"
      "v_and_b32 v20, 1, v3\n"
      "v_xor_b32 v20, s40, v20\n"
      "v_xor_b32 v21, 2, v20\n"
      "v_lshl_add_u32 v20, v20, 4, v2\n"
      "v_lshl_add_u32 v21, v21, 4, v2\n"
      "v_mul_u32_u24 v20, 0x480, v20\n"
      "v_mul_u32_u24 v21, 0x480, v21\n"
      "s_add_u32 s65, s65, 4096\n"
      "buffer_load_dwordx4 v[168:171], v1, s[4:7], s65 offen offset:0 sc1\n"
      "v_lshrrev_b32 v22, 1, v3\n"
      "s_lshl_b32 s47, s47, 5\n"
      "v_lshl_add_u32 v22, v22, 4, s47\n"
      "v_add_u32 v240, v20, v22\n"
      "v_add_u32 v241, v21, v22\n"
      "v_cmp_gt_u32 vcc, 16, v4\n"
      "s_mul_i32 s47, s40, 18432\n"
      "s_lshl_b32 s48, s39, 4\n"
      "s_add_u32 s47, s47, s48\n"
      "s_add_u32 s47, s47, 1024\n"
      "v_mul_u32_u24 v20, 0x480, v2\n"
      "v_add_u32 v20, s47, v20\n"
      "v_mov_b32 v242, 1073741824\n"
      "v_cndmask_b32 v242, v242, v20, vcc\n"
      "buffer_load_dwordx4 v[172:175], v1, s[4:7], s65 offen offset:1024 sc1\n"
      "s_mov_b32 s8, s34\n"
      "s_and_b32 s9, s35, 0xffff\n"
      "s_mov_b32 s10, 2359296\n"
      "s_mov_b32 s11, 0x20000\n"
      "s_add_u32 s71, s68, 8431616\n"
      "s_mul_i32 s45, s40, 5120\n"
      "s_add_u32 s45, s45, 122880\n"
      "v_add_u32 v16, s45, v1\n"
      "ds_read_b128 v[220:223], v16\n"
      "v_xor_b32 v17, 1024, v16\n"
      "ds_read_b128 v[224:227], v17\n"
      "v_xor_b32 v18, 2048, v16\n"
      "ds_read_b128 v[228:231], v18\n"
      "v_xor_b32 v19, 3072, v16\n"
      "ds_read_b128 v[232:235], v19\n"
      "s_mov_b32 s51, 204800\n"
      "s_mul_i32 s44, s40, 2\n"
      "s_mov_b32 exec_hi, 0\n"
      "s_lshl_b32 s44, 3, s44\n"
      "s_and_b32 exec_lo, s44, 127\n"
      "v_mov_b32 v26, 0\n"
      "v_mov_b32 v27, 0\n"
      "buffer_atomic_or_x2 v[26:27], v176, s[4:7], s51 offen sc0 sc1\n"
      "s_mov_b64 exec, -1\n"
      "s_mul_i32 s44, s40, 2\n"
      "s_mov_b32 exec_hi, 0\n"
      "s_lshl_b32 s44, 3, s44\n"
      "s_and_b32 exec_lo, s44, 127\n"
      "s_waitcnt vmcnt(0)\n"
      "v_cmp_ne_u64 vcc, s[42:43], v[26:27]\n"
      "s_cbranch_vccz .LD17_%=\n"
      "s_mov_b32 s44, 0\n"
      ".LP16_%=:\n"
      "s_mov_b64 exec, vcc\n"
      "v_mov_b32 v26, 0\n"
      "v_mov_b32 v27, 0\n"
      "buffer_atomic_or_x2 v[26:27], v176, s[4:7], s51 offen sc0 sc1\n"
      "s_waitcnt vmcnt(0)\n"
      "v_cmp_ne_u64 vcc, s[42:43], v[26:27]\n"
      "s_cbranch_vccz .LD17_%=\n"
      "s_add_u32 s44, s44, 1\n"
      "s_cmp_lt_u32 s44, s41\n"
      "s_cbranch_scc1 .LP16_%=\n"
      "s_mov_b32 s41, 0\n"
      ".LD17_%=:\n"
      "s_mov_b64 exec, -1\n"
      "s_barrier\n"
      "s_add_u32 m0, s66, 0\n"
      "s_add_u32 s69, s71, 0\n"
      "buffer_load_dwordx4 v1, s[4:7], s69 offen sc1 lds\n"
      "buffer_load_dwordx4 v1, s[4:7], s69 offen offset:1024 sc1 lds\n"
      "buffer_load_dwordx4 v1, s[4:7], s69 offen offset:2048 sc1 lds\n"
      "s_add_u32 m0, s66, 3072\n"
      "s_add_u32 s69, s71, 3072\n"
      "buffer_load_dwordx4 v1, s[4:7], s69 offen sc1 lds\n"
      "buffer_load_dwordx4 v1, s[4:7], s69 offen offset:1024 sc1 lds\n"
      "buffer_load_dwordx4 v1, s[4:7], s69 offen offset:2048 sc1 lds\n"
      "s_add_u32 m0, s66, 6144\n"
      "s_add_u32 s69, s71, 6144\n"
      "buffer_load_dwordx4 v1, s[4:7], s69 offen sc1 lds\n"
      "buffer_load_dwordx4 v1, s[4:7], s69 offen offset:1024 sc1 lds\n"
      "buffer_load_dwordx4 v1, s[4:7], s69 offen offset:2048 sc1 lds\n"
      "s_waitcnt vmcnt(6)\n"
      "s_barrier\n"
      "ds_read_b128 a[0:3], v80 offset:0\n"
      "s_add_u32 m0, s66, 9216\n"
      "s_add_u32 s69, s71, 9216\n"
      "buffer_load_dwordx4 v1, s[4:7], s69 offen sc1 lds\n"
      "ds_read_b128 a[4:7], v81 offset:0\n"
      "ds_read_b128 a[8:11], v82 offset:0\n"
      "ds_read_b128 a[12:15], v83 offset:0\n"
      "ds_read_b128 a[16:19], v80 offset:1024\n"
      "buffer_load_dwordx4 v1, s[4:7], s69 offen offset:1024 sc1 lds\n"
      "ds_read_b128 a[20:23], v81 offset:1024\n"
      "ds_read_b128 a[24:27], v82 offset:1024\n"
      "ds_read_b128 a[28:31], v83 offset:1024\n"
      "ds_read_b128 a[32:35], v80 offset:2048\n"
      "buffer_load_dwordx4 v1, s[4:7], s69 offen offset:2048 sc1 lds\n"
      "ds_read_b128 a[36:39], v81 offset:2048\n"
      "ds_read_b128 a[40:43], v82 offset:2048\n"
      "ds_read_b128 a[44:47], v83 offset:2048\n"
      "s_waitcnt vmcnt(6)\n"
      "s_barrier\n"
      "s_waitcnt lgkmcnt(4)\n"
      "v_mfma_f32_16x16x32_bf16 v[216:219], a[0:3], a[0:3], 0\n"
      "ds_read_b128 a[48:51], v80 offset:3072\n"
      "s_add_u32 m0, s66, 12288\n"
      "s_add_u32 s69, s71, 12288\n"
      "buffer_load_dwordx4 v1, s[4:7], s69 offen sc1 lds\n"
      "v_mfma_f32_16x16x32_bf16 v[216:219], a[16:19], a[16:19], v[216:219]\n"
      "ds_read_b128 a[52:55], v81 offset:3072\n"
      "ds_read_b128 a[56:59], v82 offset:3072\n"
      "v_mfma_f32_16x16x32_bf16 v[196:199], v[56:59], a[0:3], 0\n"
      "ds_read_b128 a[60:63], v83 offset:3072\n"
      "ds_read_b128 a[64:67], v80 offset:4096\n"
      "v_mfma_f32_16x16x32_bf16 v[196:199], v[60:63], a[16:19], v[196:199]\n"
      "buffer_load_dwordx4 v1, s[4:7], s69 offen offset:1024 sc1 lds\n"
      "ds_read_b128 a[68:71], v81 offset:4096\n"
      "v_mfma_f32_16x16x32_bf16 v[200:203], v[56:59], a[4:7], 0\n"
      "ds_read_b128 a[72:75], v82 offset:4096\n"
      "ds_read_b128 a[76:79], v83 offset:4096\n"
      "v_mfma_f32_16x16x32_bf16 v[200:203], v[60:63], a[20:23], v[200:203]\n"
      "ds_read_b128 a[80:83], v80 offset:5120\n"
      "buffer_load_dwordx4 v1, s[4:7], s69 offen offset:2048 sc1 lds\n"
      "v_mfma_f32_16x16x32_bf16 v[204:207], v[56:59], a[8:11], 0\n"
      "ds_read_b128 a[84:87], v81 offset:5120\n"
      "ds_read_b128 a[88:91], v82 offset:5120\n"
      "v_mfma_f32_16x16x32_bf16 v[204:207], v[60:63], a[24:27], v[204:207]\n"
      "ds_read_b128 a[92:95], v83 offset:5120\n"
      "v_mfma_f32_16x16x32_bf16 v[208:211], v[56:59], a[12:15], 0\n"
      "v_mfma_f32_16x16x32_bf16 v[208:211], v[60:63], a[28:31], v[208:211]\n"
      "s_waitcnt vmcnt(6)\n"
      "s_barrier\n"
      "s_waitcnt lgkmcnt(0)\n"
      "v_mfma_f32_16x16x32_bf16 v[216:219], a[32:35], a[32:35], v[216:219]\n"
      "ds_read_b128 a[96:99], v80 offset:6144\n"
      "v_mfma_f32_16x16x32_bf16 v[216:219], a[48:51], a[48:51], v[216:219]\n"
      "s_add_u32 m0, s66, 15360\n"
      "s_add_u32 s69, s71, 15360\n"
      "buffer_load_dwordx4 v1, s[4:7], s69 offen sc1 lds\n"
      "v_mfma_f32_16x16x32_bf16 v[196:199], v[64:67], a[32:35], v[196:199]\n"
      "ds_read_b128 a[100:103], v81 offset:6144\n"
      "v_mfma_f32_16x16x32_bf16 v[196:199], v[68:71], a[48:51], v[196:199]\n"
      "ds_read_b128 a[104:107], v82 offset:6144\n"
      "v_mfma_f32_16x16x32_bf16 v[200:203], v[64:67], a[36:39], v[200:203]\n"
      "ds_read_b128 a[108:111], v83 offset:6144\n"
      "v_mfma_f32_16x16x32_bf16 v[200:203], v[68:71], a[52:55], v[200:203]\n"
      "v_mfma_f32_16x16x32_bf16 v[204:207], v[64:67], a[40:43], v[204:207]\n"
      "ds_read_b128 a[112:115], v80 offset:7168\n"
      "v_mfma_f32_16x16x32_bf16 v[204:207], v[68:71], a[56:59], v[204:207]\n"
      "buffer_load_dwordx4 v1, s[4:7], s69 offen offset:1024 sc1 lds\n"
      "v_mfma_f32_16x16x32_bf16 v[208:211], v[64:67], a[44:47], v[208:211]\n"
      "ds_read_b128 a[116:119], v81 offset:7168\n"
      "v_mfma_f32_16x16x32_bf16 v[208:211], v[68:71], a[60:63], v[208:211]\n"
      "ds_read_b128 a[120:123], v82 offset:7168\n"
      "v_mfma_f32_16x16x32_bf16 v[216:219], a[64:67], a[64:67], v[216:219]\n"
      "ds_read_b128 a[124:127], v83 offset:7168\n"
      "v_mfma_f32_16x16x32_bf16 v[216:219], a[80:83], a[80:83], v[216:219]\n"
      "v_mfma_f32_16x16x32_bf16 v[196:199], v[76:79], a[64:67], v[196:199]\n"
      "ds_read_b128 a[128:131], v80 offset:8192\n"
      "v_mfma_f32_16x16x32_bf16 v[196:199], v[92:95], a[80:83], v[196:199]\n"
      "buffer_load_dwordx4 v1, s[4:7], s69 offen offset:2048 sc1 lds\n"
      "v_mfma_f32_16x16x32_bf16 v[200:203], v[76:79], a[68:71], v[200:203]\n"
      "ds_read_b128 a[132:135], v81 offset:8192\n"
      "v_mfma_f32_16x16x32_bf16 v[200:203], v[92:95], a[84:87], v[200:203]\n"
      "ds_read_b128 a[136:139], v82 offset:8192\n"
      "v_mfma_f32_16x16x32_bf16 v[204:207], v[76:79], a[72:75], v[204:207]\n"
      "ds_read_b128 a[140:143], v83 offset:8192\n"
      "v_mfma_f32_16x16x32_bf16 v[204:207], v[92:95], a[88:91], v[204:207]\n"
      "v_mfma_f32_16x16x32_bf16 v[208:211], v[76:79], a[76:79], v[208:211]\n"
      "v_mfma_f32_16x16x32_bf16 v[208:211], v[92:95], a[92:95], v[208:211]\n"
      "s_waitcnt vmcnt(6)\n"
      "s_barrier\n"
      "s_waitcnt lgkmcnt(4)\n"
      "v_mfma_f32_16x16x32_bf16 v[216:219], a[96:99], a[96:99], v[216:219]\n"
      "ds_read_b128 a[144:147], v80 offset:9216\n"
      "s_add_u32 s65, s65, 2048\n"
      "buffer_load_dwordx4 v[56:59], v1, s[4:7], s65 offen offset:0 sc1\n"
      "ds_read_b128 a[148:151], v81 offset:9216\n"
      "v_mfma_f32_16x16x32_bf16 v[216:219], a[112:115], a[112:115], v[216:219]\n"
      "ds_read_b128 a[152:155], v82 offset:9216\n"
      "buffer_load_dwordx4 v[60:63], v1, s[4:7], s65 offen offset:1024 sc1\n"
      "v_mfma_f32_16x16x32_bf16 v[196:199], v[128:131], a[96:99], v[196:199]\n"
      "ds_read_b128 a[156:159], v83 offset:9216\n"
      "ds_read_b128 a[160:163], v80 offset:10240\n"
      "v_mfma_f32_16x16x32_bf16 v[196:199], v[132:135], a[112:115], v[196:199]\n"
      "buffer_load_dwordx4 v[64:67], v1, s[4:7], s65 offen offset:2048 sc1\n"
      "ds_read_b128 a[164:167], v81 offset:10240\n"
      "v_mfma_f32_16x16x32_bf16 v[200:203], v[128:131], a[100:103], v[200:203]\n"
      "ds_read_b128 a[168:171], v82 offset:10240\n"
      "buffer_load_dwordx4 v[68:71], v1, s[4:7], s65 offen offset:3072 sc1\n"
      "ds_read_b128 a[172:175], v83 offset:10240\n"
      "v_mfma_f32_16x16x32_bf16 v[200:203], v[132:135], a[116:119], v[200:203]\n"
      "ds_read_b128 a[176:179], v80 offset:11264\n"
      "s_add_u32 s65, s65, 4096\n"
      "buffer_load_dwordx4 v[76:79], v1, s[4:7], s65 offen offset:0 sc1\n"
      "v_mfma_f32_16x16x32_bf16 v[204:207], v[128:131], a[104:107], v[204:207]\n"
      "ds_read_b128 a[180:183], v81 offset:11264\n"
      "ds_read_b128 a[184:187], v82 offset:11264\n"
      "v_mfma_f32_16x16x32_bf16 v[204:207], v[132:135], a[120:123], v[204:207]\n"
      "buffer_load_dwordx4 v[92:95], v1, s[4:7], s65 offen offset:1024 sc1\n"
      "ds_read_b128 a[188:191], v83 offset:11264\n"
      "v_mfma_f32_16x16x32_bf16 v[208:211], v[128:131], a[108:111], v[208:211]\n"
      "v_mfma_f32_16x16x32_bf16 v[208:211], v[132:135], a[124:127], v[208:211]\n"
      "s_waitcnt vmcnt(9)\n"
      "s_barrier\n"
      "s_waitcnt lgkmcnt(0)\n"
      "v_mfma_f32_16x16x32_bf16 v[216:219], a[128:131], a[128:131], v[216:219]\n"
      "ds_read_b128 a[192:195], v80 offset:12288\n"
      "v_mfma_f32_16x16x32_bf16 v[216:219], a[144:147], a[144:147], v[216:219]\n"
      "buffer_load_dwordx4 v[128:131], v1, s[4:7], s65 offen offset:2048 sc1\n"
      "v_mfma_f32_16x16x32_bf16 v[196:199], v[136:139], a[128:131], v[196:199]\n"
      "ds_read_b128 a[196:199], v81 offset:12288\n"
      "v_mfma_f32_16x16x32_bf16 v[196:199], v[140:143], a[144:147], v[196:199]\n"
      "ds_read_b128 a[200:203], v82 offset:12288\n"
      "v_mfma_f32_16x16x32_bf16 v[200:203], v[136:139], a[132:135], v[200:203]\n"
      "v_mfma_f32_16x16x32_bf16 v[200:203], v[140:143], a[148:151], v[200:203]\n"
      "ds_read_b128 a[204:207], v83 offset:12288\n"
      "v_mfma_f32_16x16x32_bf16 v[204:207], v[136:139], a[136:139], v[204:207]\n"
      "ds_read_b128 a[208:211], v80 offset:13312\n"
      "v_mfma_f32_16x16x32_bf16 v[204:207], v[140:143], a[152:155], v[204:207]\n"
      "ds_read_b128 a[212:215], v81 offset:13312\n"
      "v_mfma_f32_16x16x32_bf16 v[208:211], v[136:139], a[140:143], v[208:211]\n"
      "v_mfma_f32_16x16x32_bf16 v[208:211], v[140:143], a[156:159], v[208:211]\n"
      "ds_read_b128 a[216:219], v82 offset:13312\n"
      "v_mfma_f32_16x16x32_bf16 v[216:219], a[160:163], a[160:163], v[216:219]\n"
      "buffer_load_dwordx4 v[132:135], v1, s[4:7], s65 offen offset:3072 sc1\n"
      "v_mfma_f32_16x16x32_bf16 v[216:219], a[176:179], a[176:179], v[216:219]\n"
      "ds_read_b128 a[220:223], v83 offset:13312\n"
      "v_mfma_f32_16x16x32_bf16 v[196:199], v[144:147], a[160:163], v[196:199]\n"
      "ds_read_b128 a[224:227], v80 offset:14336\n"
      "v_mfma_f32_16x16x32_bf16 v[196:199], v[148:151], a[176:179], v[196:199]\n"
      "v_mfma_f32_16x16x32_bf16 v[200:203], v[144:147], a[164:167], v[200:203]\n"
      "ds_read_b128 a[228:231], v81 offset:14336\n"
      "v_mfma_f32_16x16x32_bf16 v[200:203], v[148:151], a[180:183], v[200:203]\n"
      "ds_read_b128 a[232:235], v82 offset:14336\n"
      "v_mfma_f32_16x16x32_bf16 v[204:207], v[144:147], a[168:171], v[204:207]\n"
      "ds_read_b128 a[236:239], v83 offset:14336\n"
      "v_mfma_f32_16x16x32_bf16 v[204:207], v[148:151], a[184:187], v[204:207]\n"
      "v_mfma_f32_16x16x32_bf16 v[208:211], v[144:147], a[172:175], v[208:211]\n"
      "v_mfma_f32_16x16x32_bf16 v[208:211], v[148:151], a[188:191], v[208:211]\n"
      "s_waitcnt vmcnt(8)\n"
      "s_barrier\n"
      "s_waitcnt lgkmcnt(4)\n"
      "v_mfma_f32_16x16x32_bf16 v[216:219], a[192:195], a[192:195], v[216:219]\n"
      "ds_read_b128 a[240:243], v80 offset:15360\n"
      "s_add_u32 s65, s65, 4096\n"
      "buffer_load_dwordx4 v[136:139], v1, s[4:7], s65 offen offset:0 sc1\n"
      "v_mfma_f32_16x16x32_bf16 v[216:219], a[208:211], a[208:211], v[216:219]\n"
      "ds_read_b128 a[244:247], v81 offset:15360\n"
      "ds_read_b128 a[248:251], v82 offset:15360\n"
      "v_mfma_f32_16x16x32_bf16 v[196:199], v[152:155], a[192:195], v[196:199]\n"
      "ds_read_b128 a[252:255], v83 offset:15360\n"
      "buffer_load_dwordx4 v[140:143], v1, s[4:7], s65 offen offset:1024 sc1\n"
      "v_mfma_f32_16x16x32_bf16 v[196:199], v[156:159], a[208:211], v[196:199]\n"
      "ds_read_b128 v[96:99], v80 offset:16384\n"
      "ds_read_b128 v[100:103], v81 offset:16384\n"
      "v_mfma_f32_16x16x32_bf16 v[200:203], v[152:155], a[196:199], v[200:203]\n"
      "ds_read_b128 v[104:107], v82 offset:16384\n"
      "buffer_load_dwordx4 v[144:147], v1, s[4:7], s65 offen offset:2048 sc1\n"
      "v_mfma_f32_16x16x32_bf16 v[200:203], v[156:159], a[212:215], v[200:203]\n"
      "ds_read_b128 v[108:111], v83 offset:16384\n"
      "ds_read_b128 v[112:115], v80 offset:17408\n"
      "v_mfma_f32_16x16x32_bf16 v[204:207], v[152:155], a[200:203], v[204:207]\n"
      "ds_read_b128 v[116:119], v81 offset:17408\n"
      "buffer_load_dwordx4 v[148:151], v1, s[4:7], s65 offen offset:3072 sc1\n"
      "v_mfma_f32_16x16x32_bf16 v[204:207], v[156:159], a[216:219], v[204:207]\n"
      "ds_read_b128 v[120:123], v82 offset:17408\n"
      "ds_read_b128 v[124:127], v83 offset:17408\n"
      "v_mfma_f32_16x16x32_bf16 v[208:211], v[152:155], a[204:207], v[208:211]\n"
      "v_mfma_f32_16x16x32_bf16 v[208:211], v[156:159], a[220:223], v[208:211]\n"
      "s_waitcnt lgkmcnt(0)\n"
      "v_mfma_f32_16x16x32_bf16 v[216:219], a[224:227], a[224:227], v[216:219]\n"
      "s_add_u32 s65, s65, 4096\n"
      "buffer_load_dwordx4 v[152:155], v1, s[4:7], s65 offen offset:0 sc1\n"
      "v_mfma_f32_16x16x32_bf16 v[216:219], a[240:243], a[240:243], v[216:219]\n"
      "v_mfma_f32_16x16x32_bf16 v[216:219], v[96:99], v[96:99], v[216:219]\n"
      "buffer_load_dwordx4 v[156:159], v1, s[4:7], s65 offen offset:1024 sc1\n"
      "v_mfma_f32_16x16x32_bf16 v[216:219], v[112:115], v[112:115], v[216:219]\n"
      "v_mfma_f32_16x16x32_bf16 v[196:199], v[160:163], a[224:227], v[196:199]\n"
      "v_mfma_f32_16x16x32_bf16 v[196:199], v[164:167], a[240:243], v[196:199]\n"
      "v_mfma_f32_16x16x32_bf16 v[200:203], v[160:163], a[228:231], v[200:203]\n"
      "v_mfma_f32_16x16x32_bf16 v[200:203], v[164:167], a[244:247], v[200:203]\n"
      "s_nop 3\n"
      "v_cndmask_b32 v12, v216, v217, s[54:55]\n"
      "v_cndmask_b32 v13, v218, v219, s[54:55]\n"
      "v_mfma_f32_16x16x32_bf16 v[204:207], v[160:163], a[232:235], v[204:207]\n"
      "v_cndmask_b32 v12, v12, v13, s[56:57]\n"
      "v_mfma_f32_16x16x32_bf16 v[204:207], v[164:167], a[248:251], v[204:207]\n"
      "v_mul_f32 v12, 0x3ae38e39, v12\n"
      "v_add_f32 v12, 0x3727c5ac, v12\n"
      "v_mfma_f32_16x16x32_bf16 v[208:211], v[160:163], a[236:239], v[208:211]\n"
      "buffer_load_dwordx4 v[160:163], v1, s[4:7], s65 offen offset:2048 sc1\n"
      "v_mfma_f32_16x16x32_bf16 v[208:211], v[164:167], a[252:255], v[208:211]\n"
      "buffer_load_dwordx4 v[164:167], v1, s[4:7], s65 offen offset:3072 sc1\n"
      "v_mfma_f32_16x16x32_bf16 v[196:199], v[168:171], v[96:99], v[196:199]\n"
      "s_add_u32 s65, s65, 4096\n"
      "v_rsq_f32 v12, v12\n"
      "v_mfma_f32_16x16x32_bf16 v[196:199], v[172:175], v[112:115], v[196:199]\n"
      "v_mul_f32 v13, 0xc0135761, v12\n"
      "v_mul_f32 v14, v12, v12\n"
      "v_mfma_f32_16x16x32_bf16 v[200:203], v[168:171], v[100:103], v[200:203]\n"
      "v_mul_f32 v14, v14, v13\n"
      "v_mfma_f32_16x16x32_bf16 v[200:203], v[172:175], v[116:119], v[200:203]\n"
      "v_mul_f32 v14, 0x3d372713, v14\n"
      "v_mfma_f32_16x16x32_bf16 v[204:207], v[168:171], v[104:107], v[204:207]\n"
      "s_mov_b32 exec_lo, 15728655\n"
      "s_mov_b32 exec_hi, 4026535680\n"
      "ds_write_b128 v88, v[12:15]\n"
      "s_mov_b64 exec, -1\n"
      "v_mfma_f32_16x16x32_bf16 v[204:207], v[172:175], v[120:123], v[204:207]\n"
      "s_waitcnt vmcnt(14)\n"
      "v_mfma_f32_16x16x32_bf16 v[208:211], v[168:171], v[108:111], v[208:211]\n"
      "buffer_load_dwordx4 v[168:171], v1, s[4:7], s65 offen offset:0 sc1\n"
      "v_mfma_f32_16x16x32_bf16 v[208:211], v[172:175], v[124:127], v[208:211]\n"
      "buffer_load_dwordx4 v[172:175], v1, s[4:7], s65 offen offset:1024 sc1\n"
      "s_waitcnt lgkmcnt(0)\n"
      "s_barrier\n"
      "ds_read_b128 v[72:75], v88\n"
      "ds_read_b128 v[84:87], v89\n"
      "ds_read_b128 v[180:183], v90\n"
      "ds_read_b128 v[184:187], v91\n"
      "v_add_u32 v80, 73728, v80\n"
      "v_add_u32 v81, 73728, v81\n"
      "v_add_u32 v82, 73728, v82\n"
      "v_add_u32 v83, 73728, v83\n"
      "s_add_u32 s45, s53, 589824\n"
      "s_mov_b32 s70, 3\n"
      ".LLP18_%=:\n"
      "v_mfma_f32_16x16x32_bf16 v[28:31], v[56:59], a[0:3], 0\n"
      "s_add_u32 s65, s65, 2048\n"
      "v_mfma_f32_16x16x32_bf16 v[28:31], v[60:63], a[16:19], v[28:31]\n"
      "s_cmp_eq_u32 s70, 0\n"
      "s_cbranch_scc0 .LS19_%=\n"
      "s_mov_b32 s52, 5120\n"
      "buffer_load_dwordx4 v[48:51], v1, s[4:7], s52 offen sc1\n"
      "buffer_load_dwordx4 v[52:55], v1, s[4:7], s52 offen offset:1024 sc1\n"
      ".LS19_%=:\n"
      "v_mfma_f32_16x16x32_bf16 v[32:35], v[56:59], a[4:7], 0\n"
      "v_mul_f32 v12, v196, v196\n"
      "v_mul_f32 v13, v197, v197\n"
      "v_mfma_f32_16x16x32_bf16 v[32:35], v[60:63], a[20:23], v[32:35]\n"
      "v_mul_f32 v14, v198, v198\n"
      "v_mul_f32 v15, v199, v199\n"
      "s_waitcnt lgkmcnt(3)\n"
      "v_fma_f32 v12, v12, v74, v73\n"
      "v_mfma_f32_16x16x32_bf16 v[36:39], v[56:59], a[8:11], 0\n"
      "v_fma_f32 v13, v13, v74, v73\n"
      "v_fma_f32 v14, v14, v74, v73\n"
      "v_mfma_f32_16x16x32_bf16 v[36:39], v[60:63], a[24:27], v[36:39]\n"
      "s_waitcnt vmcnt(14)\n"
      "v_fma_f32 v15, v15, v74, v73\n"
      "v_mul_f32 v12, v12, v196\n"
      "v_mul_f32 v13, v13, v197\n"
      "v_mfma_f32_16x16x32_bf16 v[40:43], v[56:59], a[12:15], 0\n"
      "buffer_load_dwordx4 v[56:59], v1, s[4:7], s65 offen offset:0 sc1\n"
      "v_mul_f32 v14, v14, v198\n"
      "v_mfma_f32_16x16x32_bf16 v[40:43], v[60:63], a[28:31], v[40:43]\n"
      "buffer_load_dwordx4 v[60:63], v1, s[4:7], s65 offen offset:1024 sc1\n"
      "v_mul_f32 v15, v15, v199\n"
      "v_mfma_f32_16x16x32_bf16 v[28:31], v[64:67], a[32:35], v[28:31]\n"
      "v_exp_f32 v12, v12\n"
      "v_exp_f32 v13, v13\n"
      "v_mfma_f32_16x16x32_bf16 v[28:31], v[68:71], a[48:51], v[28:31]\n"
      "v_exp_f32 v14, v14\n"
      "v_mfma_f32_16x16x32_bf16 v[32:35], v[64:67], a[36:39], v[32:35]\n"
      "v_exp_f32 v15, v15\n"
      "v_mfma_f32_16x16x32_bf16 v[32:35], v[68:71], a[52:55], v[32:35]\n"
      "v_add_f32 v12, 1.0, v12\n"
      "v_add_f32 v13, 1.0, v13\n"
      "v_add_f32 v14, 1.0, v14\n"
      "v_mfma_f32_16x16x32_bf16 v[36:39], v[64:67], a[40:43], v[36:39]\n"
      "v_add_f32 v15, 1.0, v15\n"
      "v_rcp_f32 v12, v12\n"
      "v_mfma_f32_16x16x32_bf16 v[36:39], v[68:71], a[56:59], v[36:39]\n"
      "s_waitcnt vmcnt(14)\n"
      "v_rcp_f32 v13, v13\n"
      "v_mfma_f32_16x16x32_bf16 v[40:43], v[64:67], a[44:47], v[40:43]\n"
      "buffer_load_dwordx4 v[64:67], v1, s[4:7], s65 offen offset:2048 sc1\n"
      "v_rcp_f32 v14, v14\n"
      "v_mfma_f32_16x16x32_bf16 v[40:43], v[68:71], a[60:63], v[40:43]\n"
      "buffer_load_dwordx4 v[68:71], v1, s[4:7], s65 offen offset:3072 sc1\n"
      "v_mfma_f32_16x16x32_bf16 v[28:31], v[76:79], a[64:67], v[28:31]\n"
      "s_add_u32 s65, s65, 4096\n"
      "v_rcp_f32 v15, v15\n"
      "v_mul_f32 v12, v12, v196\n"
      "v_mfma_f32_16x16x32_bf16 v[28:31], v[92:95], a[80:83], v[28:31]\n"
      "v_mul_f32 v13, v13, v197\n"
      "v_mul_f32 v14, v14, v198\n"
      "v_mul_f32 v15, v15, v199\n"
      "v_mfma_f32_16x16x32_bf16 v[32:35], v[76:79], a[68:71], v[32:35]\n"
      "v_cvt_pk_bf16_f32 v20, v12, v13\n"
      "v_cvt_pk_bf16_f32 v21, v14, v15\n"
      "v_mfma_f32_16x16x32_bf16 v[32:35], v[92:95], a[84:87], v[32:35]\n"
      "ds_write_b64 v44, v[20:21] offset:0\n"
      "v_mul_f32 v16, v200, v200\n"
      "v_mul_f32 v17, v201, v201\n"
      "v_mfma_f32_16x16x32_bf16 v[36:39], v[76:79], a[72:75], v[36:39]\n"
      "v_mul_f32 v18, v202, v202\n"
      "v_mul_f32 v19, v203, v203\n"
      "v_mfma_f32_16x16x32_bf16 v[36:39], v[92:95], a[88:91], v[36:39]\n"
      "s_waitcnt vmcnt(14) lgkmcnt(3)\n"
      "v_fma_f32 v16, v16, v86, v85\n"
      "v_fma_f32 v17, v17, v86, v85\n"
      "v_fma_f32 v18, v18, v86, v85\n"
      "v_mfma_f32_16x16x32_bf16 v[40:43], v[76:79], a[76:79], v[40:43]\n"
      "buffer_load_dwordx4 v[76:79], v1, s[4:7], s65 offen offset:0 sc1\n"
      "v_fma_f32 v19, v19, v86, v85\n"
      "v_mfma_f32_16x16x32_bf16 v[40:43], v[92:95], a[92:95], v[40:43]\n"
      "buffer_load_dwordx4 v[92:95], v1, s[4:7], s65 offen offset:1024 sc1\n"
      "v_mul_f32 v16, v16, v200\n"
      "v_mul_f32 v17, v17, v201\n"
      "v_mfma_f32_16x16x32_bf16 v[28:31], v[128:131], a[96:99], v[28:31]\n"
      "v_mul_f32 v18, v18, v202\n"
      "v_mul_f32 v19, v19, v203\n"
      "v_mfma_f32_16x16x32_bf16 v[28:31], v[132:135], a[112:115], v[28:31]\n"
      "v_exp_f32 v16, v16\n"
      "v_mfma_f32_16x16x32_bf16 v[32:35], v[128:131], a[100:103], v[32:35]\n"
      "v_exp_f32 v17, v17\n"
      "v_exp_f32 v18, v18\n"
      "v_mfma_f32_16x16x32_bf16 v[32:35], v[132:135], a[116:119], v[32:35]\n"
      "v_exp_f32 v19, v19\n"
      "v_mfma_f32_16x16x32_bf16 v[36:39], v[128:131], a[104:107], v[36:39]\n"
      "v_add_f32 v16, 1.0, v16\n"
      "v_add_f32 v17, 1.0, v17\n"
      "v_mfma_f32_16x16x32_bf16 v[36:39], v[132:135], a[120:123], v[36:39]\n"
      "s_waitcnt vmcnt(14)\n"
      "v_add_f32 v18, 1.0, v18\n"
      "v_add_f32 v19, 1.0, v19\n"
      "v_mfma_f32_16x16x32_bf16 v[40:43], v[128:131], a[108:111], v[40:43]\n"
      "buffer_load_dwordx4 v[128:131], v1, s[4:7], s65 offen offset:2048 sc1\n"
      "v_rcp_f32 v16, v16\n"
      "v_mfma_f32_16x16x32_bf16 v[40:43], v[132:135], a[124:127], v[40:43]\n"
      "buffer_load_dwordx4 v[132:135], v1, s[4:7], s65 offen offset:3072 sc1\n"
      "v_rcp_f32 v17, v17\n"
      "v_mfma_f32_16x16x32_bf16 v[28:31], v[136:139], a[128:131], v[28:31]\n"
      "s_add_u32 s65, s65, 4096\n"
      "v_rcp_f32 v18, v18\n"
      "v_mfma_f32_16x16x32_bf16 v[28:31], v[140:143], a[144:147], v[28:31]\n"
      "v_rcp_f32 v19, v19\n"
      "v_mul_f32 v16, v16, v200\n"
      "v_mfma_f32_16x16x32_bf16 v[32:35], v[136:139], a[132:135], v[32:35]\n"
      "v_mul_f32 v17, v17, v201\n"
      "v_mul_f32 v18, v18, v202\n"
      "v_mfma_f32_16x16x32_bf16 v[32:35], v[140:143], a[148:151], v[32:35]\n"
      "v_mul_f32 v19, v19, v203\n"
      "v_cvt_pk_bf16_f32 v22, v16, v17\n"
      "v_cvt_pk_bf16_f32 v23, v18, v19\n"
      "v_mfma_f32_16x16x32_bf16 v[36:39], v[136:139], a[136:139], v[36:39]\n"
      "ds_write_b64 v45, v[22:23] offset:0\n"
      "v_mul_f32 v12, v204, v204\n"
      "v_mfma_f32_16x16x32_bf16 v[36:39], v[140:143], a[152:155], v[36:39]\n"
      "s_waitcnt vmcnt(14)\n"
      "v_mul_f32 v13, v205, v205\n"
      "v_mul_f32 v14, v206, v206\n"
      "v_mfma_f32_16x16x32_bf16 v[40:43], v[136:139], a[140:143], v[40:43]\n"
      "buffer_load_dwordx4 v[136:139], v1, s[4:7], s65 offen offset:0 sc1\n"
      "v_mul_f32 v15, v207, v207\n"
      "s_waitcnt lgkmcnt(3)\n"
      "v_fma_f32 v12, v12, v182, v181\n"
      "v_mfma_f32_16x16x32_bf16 v[40:43], v[140:143], a[156:159], v[40:43]\n"
      "buffer_load_dwordx4 v[140:143], v1, s[4:7], s65 offen offset:1024 sc1\n"
      "v_fma_f32 v13, v13, v182, v181\n"
      "v_mfma_f32_16x16x32_bf16 v[28:31], v[144:147], a[160:163], v[28:31]\n"
      "v_fma_f32 v14, v14, v182, v181\n"
      "v_fma_f32 v15, v15, v182, v181\n"
      "v_mul_f32 v12, v12, v204\n"
      "v_mfma_f32_16x16x32_bf16 v[28:31], v[148:151], a[176:179], v[28:31]\n"
      "v_mul_f32 v13, v13, v205\n"
      "v_mul_f32 v14, v14, v206\n"
      "v_mfma_f32_16x16x32_bf16 v[32:35], v[144:147], a[164:167], v[32:35]\n"
      "v_mul_f32 v15, v15, v207\n"
      "v_exp_f32 v12, v12\n"
      "v_mfma_f32_16x16x32_bf16 v[32:35], v[148:151], a[180:183], v[32:35]\n"
      "v_exp_f32 v13, v13\n"
      "v_mfma_f32_16x16x32_bf16 v[36:39], v[144:147], a[168:171], v[36:39]\n"
      "v_exp_f32 v14, v14\n"
      "v_mfma_f32_16x16x32_bf16 v[36:39], v[148:151], a[184:187], v[36:39]\n"
      "s_waitcnt vmcnt(14)\n"
      "v_exp_f32 v15, v15\n"
      "v_add_f32 v12, 1.0, v12\n"
      "v_mfma_f32_16x16x32_bf16 v[40:43], v[144:147], a[172:175], v[40:43]\n"
      "buffer_load_dwordx4 v[144:147], v1, s[4:7], s65 offen offset:2048 sc1\n"
      "v_add_f32 v13, 1.0, v13\n"
      "v_add_f32 v14, 1.0, v14\n"
      "v_mfma_f32_16x16x32_bf16 v[40:43], v[148:151], a[188:191], v[40:43]\n"
      "buffer_load_dwordx4 v[148:151], v1, s[4:7], s65 offen offset:3072 sc1\n"
      "v_add_f32 v15, 1.0, v15\n"
      "v_mfma_f32_16x16x32_bf16 v[28:31], v[152:155], a[192:195], v[28:31]\n"
      "s_add_u32 s65, s65, 4096\n"
      "v_rcp_f32 v12, v12\n"
      "v_mfma_f32_16x16x32_bf16 v[28:31], v[156:159], a[208:211], v[28:31]\n"
      "v_rcp_f32 v13, v13\n"
      "v_rcp_f32 v14, v14\n"
      "v_mfma_f32_16x16x32_bf16 v[32:35], v[152:155], a[196:199], v[32:35]\n"
      "v_rcp_f32 v15, v15\n"
      "v_mfma_f32_16x16x32_bf16 v[32:35], v[156:159], a[212:215], v[32:35]\n"
      "v_mul_f32 v12, v12, v204\n"
      "v_mul_f32 v13, v13, v205\n"
      "v_mfma_f32_16x16x32_bf16 v[36:39], v[152:155], a[200:203], v[36:39]\n"
      "v_mul_f32 v14, v14, v206\n"
      "v_mul_f32 v15, v15, v207\n"
      "v_cvt_pk_bf16_f32 v20, v12, v13\n"
      "v_mfma_f32_16x16x32_bf16 v[36:39], v[156:159], a[216:219], v[36:39]\n"
      "s_waitcnt vmcnt(14)\n"
      "v_cvt_pk_bf16_f32 v21, v14, v15\n"
      "v_mfma_f32_16x16x32_bf16 v[40:43], v[152:155], a[204:207], v[40:43]\n"
      "buffer_load_dwordx4 v[152:155], v1, s[4:7], s65 offen offset:0 sc1\n"
      "ds_write_b64 v46, v[20:21] offset:0\n"
      "v_mul_f32 v16, v208, v208\n"
      "v_mul_f32 v17, v209, v209\n"
      "v_mfma_f32_16x16x32_bf16 v[40:43], v[156:159], a[220:223], v[40:43]\n"
      "buffer_load_dwordx4 v[156:159], v1, s[4:7], s65 offen offset:1024 sc1\n"
      "v_mul_f32 v18, v210, v210\n"
      "v_mfma_f32_16x16x32_bf16 v[28:31], v[160:163], a[224:227], v[28:31]\n"
      "v_mul_f32 v19, v211, v211\n"
      "s_waitcnt lgkmcnt(3)\n"
      "v_fma_f32 v16, v16, v186, v185\n"
      "v_fma_f32 v17, v17, v186, v185\n"
      "v_mfma_f32_16x16x32_bf16 v[28:31], v[164:167], a[240:243], v[28:31]\n"
      "v_fma_f32 v18, v18, v186, v185\n"
      "v_fma_f32 v19, v19, v186, v185\n"
      "v_mfma_f32_16x16x32_bf16 v[32:35], v[160:163], a[228:231], v[32:35]\n"
      "v_mul_f32 v16, v16, v208\n"
      "v_mul_f32 v17, v17, v209\n"
      "v_mul_f32 v18, v18, v210\n"
      "v_mfma_f32_16x16x32_bf16 v[32:35], v[164:167], a[244:247], v[32:35]\n"
      "v_mul_f32 v19, v19, v211\n"
      "v_exp_f32 v16, v16\n"
      "v_mfma_f32_16x16x32_bf16 v[36:39], v[160:163], a[232:235], v[36:39]\n"
      "v_exp_f32 v17, v17\n"
      "v_mfma_f32_16x16x32_bf16 v[36:39], v[164:167], a[248:251], v[36:39]\n"
      "s_waitcnt vmcnt(14)\n"
      "v_exp_f32 v18, v18\n"
      "v_mfma_f32_16x16x32_bf16 v[40:43], v[160:163], a[236:239], v[40:43]\n"
      "buffer_load_dwordx4 v[160:163], v1, s[4:7], s65 offen offset:2048 sc1\n"
      "v_exp_f32 v19, v19\n"
      "v_mfma_f32_16x16x32_bf16 v[40:43], v[164:167], a[252:255], v[40:43]\n"
      "buffer_load_dwordx4 v[164:167], v1, s[4:7], s65 offen offset:3072 sc1\n"
      "v_add_f32 v16, 1.0, v16\n"
      "v_mfma_f32_16x16x32_bf16 v[28:31], v[168:171], v[96:99], v[28:31]\n"
      "s_add_u32 s65, s65, 4096\n"
      "v_add_f32 v17, 1.0, v17\n"
      "v_add_f32 v18, 1.0, v18\n"
      "v_add_f32 v19, 1.0, v19\n"
      "v_mfma_f32_16x16x32_bf16 v[28:31], v[172:175], v[112:115], v[28:31]\n"
      "v_rcp_f32 v16, v16\n"
      "v_mfma_f32_16x16x32_bf16 v[32:35], v[168:171], v[100:103], v[32:35]\n"
      "v_rcp_f32 v17, v17\n"
      "v_mfma_f32_16x16x32_bf16 v[32:35], v[172:175], v[116:119], v[32:35]\n"
      "v_rcp_f32 v18, v18\n"
      "v_rcp_f32 v19, v19\n"
      "v_mfma_f32_16x16x32_bf16 v[36:39], v[168:171], v[104:107], v[36:39]\n"
      "v_mul_f32 v16, v16, v208\n"
      "v_mul_f32 v17, v17, v209\n"
      "v_mfma_f32_16x16x32_bf16 v[36:39], v[172:175], v[120:123], v[36:39]\n"
      "s_waitcnt vmcnt(14)\n"
      "v_mul_f32 v18, v18, v210\n"
      "v_mul_f32 v19, v19, v211\n"
      "v_mfma_f32_16x16x32_bf16 v[40:43], v[168:171], v[108:111], v[40:43]\n"
      "buffer_load_dwordx4 v[168:171], v1, s[4:7], s65 offen offset:0 sc1\n"
      "v_cvt_pk_bf16_f32 v22, v16, v17\n"
      "v_cvt_pk_bf16_f32 v23, v18, v19\n"
      "v_mfma_f32_16x16x32_bf16 v[40:43], v[172:175], v[124:127], v[40:43]\n"
      "buffer_load_dwordx4 v[172:175], v1, s[4:7], s65 offen offset:1024 sc1\n"
      "ds_write_b64 v47, v[22:23] offset:0\n"
      "s_cmp_eq_u32 s70, 0\n"
      "s_cbranch_scc0 .LD21_%=\n"
      "s_mov_b32 s44, 0\n"
      ".LP20_%=:\n"
      "v_cmp_ne_u64 vcc, s[42:43], v[48:49]\n"
      "v_cmp_ne_u64 s[58:59], s[42:43], v[50:51]\n"
      "s_or_b64 vcc, vcc, s[58:59]\n"
      "v_cmp_ne_u64 s[58:59], s[42:43], v[52:53]\n"
      "s_or_b64 vcc, vcc, s[58:59]\n"
      "v_cmp_ne_u64 s[58:59], s[42:43], v[54:55]\n"
      "s_or_b64 vcc, vcc, s[58:59]\n"
      "s_cbranch_scc0 .LD21_%=\n"
      "s_mov_b32 s52, 5120\n"
      "buffer_load_dwordx4 v[48:51], v1, s[4:7], s52 offen sc1\n"
      "buffer_load_dwordx4 v[52:55], v1, s[4:7], s52 offen offset:1024 sc1\n"
      "s_waitcnt vmcnt(0)\n"
      "s_add_u32 s44, s44, 1\n"
      "s_cmp_lt_u32 s44, s41\n"
      "s_cbranch_scc1 .LP20_%=\n"
      "s_mov_b32 s41, 0\n"
      ".LD21_%=:\n"
      "v_mfma_f32_16x16x32_bf16 v[196:199], v[56:59], a[0:3], 0\n"
      "s_add_u32 s65, s65, 2048\n"
      "v_mfma_f32_16x16x32_bf16 v[196:199], v[60:63], a[16:19], v[196:199]\n"
      "v_mfma_f32_16x16x32_bf16 v[200:203], v[56:59], a[4:7], 0\n"
      "s_cmp_eq_u32 s70, 0\n"
      "s_cselect_b32 s65, s45, s65\n"
      "v_mul_f32 v12, v28, v28\n"
      "v_mul_f32 v13, v29, v29\n"
      "v_mul_f32 v14, v30, v30\n"
      "v_mfma_f32_16x16x32_bf16 v[200:203], v[60:63], a[20:23], v[200:203]\n"
      "v_mul_f32 v15, v31, v31\n"
      "v_fma_f32 v12, v12, v74, v73\n"
      "v_mfma_f32_16x16x32_bf16 v[204:207], v[56:59], a[8:11], 0\n"
      "v_fma_f32 v13, v13, v74, v73\n"
      "v_fma_f32 v14, v14, v74, v73\n"
      "v_fma_f32 v15, v15, v74, v73\n"
      "v_mfma_f32_16x16x32_bf16 v[204:207], v[60:63], a[24:27], v[204:207]\n"
      "s_waitcnt vmcnt(14)\n"
      "v_mul_f32 v12, v12, v28\n"
      "v_mul_f32 v13, v13, v29\n"
      "v_mfma_f32_16x16x32_bf16 v[208:211], v[56:59], a[12:15], 0\n"
      "buffer_load_dwordx4 v[56:59], v1, s[4:7], s65 offen offset:0 sc1\n"
      "v_mul_f32 v14, v14, v30\n"
      "v_mul_f32 v15, v15, v31\n"
      "v_mfma_f32_16x16x32_bf16 v[208:211], v[60:63], a[28:31], v[208:211]\n"
      "buffer_load_dwordx4 v[60:63], v1, s[4:7], s65 offen offset:1024 sc1\n"
      "v_exp_f32 v12, v12\n"
      "v_mfma_f32_16x16x32_bf16 v[196:199], v[64:67], a[32:35], v[196:199]\n"
      "v_exp_f32 v13, v13\n"
      "v_mfma_f32_16x16x32_bf16 v[196:199], v[68:71], a[48:51], v[196:199]\n"
      "v_exp_f32 v14, v14\n"
      "v_mfma_f32_16x16x32_bf16 v[200:203], v[64:67], a[36:39], v[200:203]\n"
      "v_exp_f32 v15, v15\n"
      "v_add_f32 v12, 1.0, v12\n"
      "v_mfma_f32_16x16x32_bf16 v[200:203], v[68:71], a[52:55], v[200:203]\n"
      "v_add_f32 v13, 1.0, v13\n"
      "v_add_f32 v14, 1.0, v14\n"
      "v_mfma_f32_16x16x32_bf16 v[204:207], v[64:67], a[40:43], v[204:207]\n"
      "v_add_f32 v15, 1.0, v15\n"
      "v_rcp_f32 v12, v12\n"
      "v_mfma_f32_16x16x32_bf16 v[204:207], v[68:71], a[56:59], v[204:207]\n"
      "s_waitcnt vmcnt(14)\n"
      "v_rcp_f32 v13, v13\n"
      "v_mfma_f32_16x16x32_bf16 v[208:211], v[64:67], a[44:47], v[208:211]\n"
      "buffer_load_dwordx4 v[64:67], v1, s[4:7], s65 offen offset:2048 sc1\n"
      "v_rcp_f32 v14, v14\n"
      "v_mfma_f32_16x16x32_bf16 v[208:211], v[68:71], a[60:63], v[208:211]\n"
      "buffer_load_dwordx4 v[68:71], v1, s[4:7], s65 offen offset:3072 sc1\n"
      "v_rcp_f32 v15, v15\n"
      "v_mfma_f32_16x16x32_bf16 v[196:199], v[76:79], a[64:67], v[196:199]\n"
      "s_add_u32 s65, s65, 4096\n"
      "v_mul_f32 v12, v12, v28\n"
      "v_mul_f32 v13, v13, v29\n"
      "v_mfma_f32_16x16x32_bf16 v[196:199], v[92:95], a[80:83], v[196:199]\n"
      "v_mul_f32 v14, v14, v30\n"
      "v_mul_f32 v15, v15, v31\n"
      "v_cvt_pk_bf16_f32 v20, v12, v13\n"
      "v_mfma_f32_16x16x32_bf16 v[200:203], v[76:79], a[68:71], v[200:203]\n"
      "v_cvt_pk_bf16_f32 v21, v14, v15\n"
      "v_mfma_f32_16x16x32_bf16 v[200:203], v[92:95], a[84:87], v[200:203]\n"
      "ds_write_b64 v44, v[20:21] offset:2048\n"
      "v_mul_f32 v16, v32, v32\n"
      "v_mul_f32 v17, v33, v33\n"
      "v_mul_f32 v18, v34, v34\n"
      "v_mfma_f32_16x16x32_bf16 v[204:207], v[76:79], a[72:75], v[204:207]\n"
      "v_mul_f32 v19, v35, v35\n"
      "v_fma_f32 v16, v16, v86, v85\n"
      "v_mfma_f32_16x16x32_bf16 v[204:207], v[92:95], a[88:91], v[204:207]\n"
      "s_waitcnt vmcnt(14)\n"
      "v_fma_f32 v17, v17, v86, v85\n"
      "v_fma_f32 v18, v18, v86, v85\n"
      "v_fma_f32 v19, v19, v86, v85\n"
      "v_mfma_f32_16x16x32_bf16 v[208:211], v[76:79], a[76:79], v[208:211]\n"
      "buffer_load_dwordx4 v[76:79], v1, s[4:7], s65 offen offset:0 sc1\n"
      "v_mul_f32 v16, v16, v32\n"
      "v_mfma_f32_16x16x32_bf16 v[208:211], v[92:95], a[92:95], v[208:211]\n"
      "buffer_load_dwordx4 v[92:95], v1, s[4:7], s65 offen offset:1024 sc1\n"
      "v_mul_f32 v17, v17, v33\n"
      "v_mul_f32 v18, v18, v34\n"
      "v_mfma_f32_16x16x32_bf16 v[196:199], v[128:131], a[96:99], v[196:199]\n"
      "v_mul_f32 v19, v19, v35\n"
      "v_exp_f32 v16, v16\n"
      "v_mfma_f32_16x16x32_bf16 v[196:199], v[132:135], a[112:115], v[196:199]\n"
      "v_exp_f32 v17, v17\n"
      "v_mfma_f32_16x16x32_bf16 v[200:203], v[128:131], a[100:103], v[200:203]\n"
      "v_exp_f32 v18, v18\n"
      "v_mfma_f32_16x16x32_bf16 v[200:203], v[132:135], a[116:119], v[200:203]\n"
      "v_exp_f32 v19, v19\n"
      "v_add_f32 v16, 1.0, v16\n"
      "v_mfma_f32_16x16x32_bf16 v[204:207], v[128:131], a[104:107], v[204:207]\n"
      "v_add_f32 v17, 1.0, v17\n"
      "v_add_f32 v18, 1.0, v18\n"
      "v_add_f32 v19, 1.0, v19\n"
      "v_mfma_f32_16x16x32_bf16 v[204:207], v[132:135], a[120:123], v[204:207]\n"
      "s_waitcnt vmcnt(14)\n"
      "v_rcp_f32 v16, v16\n"
      "v_mfma_f32_16x16x32_bf16 v[208:211], v[128:131], a[108:111], v[208:211]\n"
      "buffer_load_dwordx4 v[128:131], v1, s[4:7], s65 offen offset:2048 sc1\n"
      "v_rcp_f32 v17, v17\n"
      "v_mfma_f32_16x16x32_bf16 v[208:211], v[132:135], a[124:127], v[208:211]\n"
      "buffer_load_dwordx4 v[132:135], v1, s[4:7], s65 offen offset:3072 sc1\n"
      "v_rcp_f32 v18, v18\n"
      "v_mfma_f32_16x16x32_bf16 v[196:199], v[136:139], a[128:131], v[196:199]\n"
      "s_add_u32 s65, s65, 4096\n"
      "v_rcp_f32 v19, v19\n"
      "v_mfma_f32_16x16x32_bf16 v[196:199], v[140:143], a[144:147], v[196:199]\n"
      "v_mul_f32 v16, v16, v32\n"
      "v_mul_f32 v17, v17, v33\n"
      "v_mfma_f32_16x16x32_bf16 v[200:203], v[136:139], a[132:135], v[200:203]\n"
      "v_mul_f32 v18, v18, v34\n"
      "v_mul_f32 v19, v19, v35\n"
      "v_cvt_pk_bf16_f32 v22, v16, v17\n"
      "v_mfma_f32_16x16x32_bf16 v[200:203], v[140:143], a[148:151], v[200:203]\n"
      "v_cvt_pk_bf16_f32 v23, v18, v19\n"
      "v_mfma_f32_16x16x32_bf16 v[204:207], v[136:139], a[136:139], v[204:207]\n"
      "ds_write_b64 v45, v[22:23] offset:2048\n"
      "v_mul_f32 v12, v36, v36\n"
      "v_mul_f32 v13, v37, v37\n"
      "v_mul_f32 v14, v38, v38\n"
      "v_mfma_f32_16x16x32_bf16 v[204:207], v[140:143], a[152:155], v[204:207]\n"
      "s_waitcnt vmcnt(14)\n"
      "v_mul_f32 v15, v39, v39\n"
      "v_fma_f32 v12, v12, v182, v181\n"
      "v_fma_f32 v13, v13, v182, v181\n"
      "v_mfma_f32_16x16x32_bf16 v[208:211], v[136:139], a[140:143], v[208:211]\n"
      "buffer_load_dwordx4 v[136:139], v1, s[4:7], s65 offen offset:0 sc1\n"
      "v_fma_f32 v14, v14, v182, v181\n"
      "v_mfma_f32_16x16x32_bf16 v[208:211], v[140:143], a[156:159], v[208:211]\n"
      "buffer_load_dwordx4 v[140:143], v1, s[4:7], s65 offen offset:1024 sc1\n"
      "v_fma_f32 v15, v15, v182, v181\n"
      "v_mul_f32 v12, v12, v36\n"
      "v_mfma_f32_16x16x32_bf16 v[196:199], v[144:147], a[160:163], v[196:199]\n"
      "v_mul_f32 v13, v13, v37\n"
      "v_mul_f32 v14, v14, v38\n"
      "v_mfma_f32_16x16x32_bf16 v[196:199], v[148:151], a[176:179], v[196:199]\n"
      "v_mul_f32 v15, v15, v39\n"
      "v_exp_f32 v12, v12\n"
      "v_mfma_f32_16x16x32_bf16 v[200:203], v[144:147], a[164:167], v[200:203]\n"
      "v_exp_f32 v13, v13\n"
      "v_mfma_f32_16x16x32_bf16 v[200:203], v[148:151], a[180:183], v[200:203]\n"
      "v_exp_f32 v14, v14\n"
      "v_mfma_f32_16x16x32_bf16 v[204:207], v[144:147], a[168:171], v[204:207]\n"
      "v_exp_f32 v15, v15\n"
      "v_add_f32 v12, 1.0, v12\n"
      "v_mfma_f32_16x16x32_bf16 v[204:207], v[148:151], a[184:187], v[204:207]\n"
      "s_waitcnt vmcnt(14)\n"
      "v_add_f32 v13, 1.0, v13\n"
      "v_add_f32 v14, 1.0, v14\n"
      "v_add_f32 v15, 1.0, v15\n"
      "v_mfma_f32_16x16x32_bf16 v[208:211], v[144:147], a[172:175], v[208:211]\n"
      "buffer_load_dwordx4 v[144:147], v1, s[4:7], s65 offen offset:2048 sc1\n"
      "v_rcp_f32 v12, v12\n"
      "v_mfma_f32_16x16x32_bf16 v[208:211], v[148:151], a[188:191], v[208:211]\n"
      "buffer_load_dwordx4 v[148:151], v1, s[4:7], s65 offen offset:3072 sc1\n"
      "v_mfma_f32_16x16x32_bf16 v[196:199], v[152:155], a[192:195], v[196:199]\n"
      "s_add_u32 s65, s65, 4096\n"
      "v_rcp_f32 v13, v13\n"
      "v_rcp_f32 v14, v14\n"
      "v_mfma_f32_16x16x32_bf16 v[196:199], v[156:159], a[208:211], v[196:199]\n"
      "v_rcp_f32 v15, v15\n"
      "v_mfma_f32_16x16x32_bf16 v[200:203], v[152:155], a[196:199], v[200:203]\n"
      "v_mul_f32 v12, v12, v36\n"
      "v_mul_f32 v13, v13, v37\n"
      "v_mul_f32 v14, v14, v38\n"
      "v_mfma_f32_16x16x32_bf16 v[200:203], v[156:159], a[212:215], v[200:203]\n"
      "v_mul_f32 v15, v15, v39\n"
      "v_cvt_pk_bf16_f32 v20, v12, v13\n"
      "v_mfma_f32_16x16x32_bf16 v[204:207], v[152:155], a[200:203], v[204:207]\n"
      "v_cvt_pk_bf16_f32 v21, v14, v15\n"
      "v_mfma_f32_16x16x32_bf16 v[204:207], v[156:159], a[216:219], v[204:207]\n"
      "s_waitcnt vmcnt(14)\n"
      "ds_write_b64 v46, v[20:21] offset:2048\n"
      "v_mul_f32 v16, v40, v40\n"
      "v_mul_f32 v17, v41, v41\n"
      "v_mul_f32 v18, v42, v42\n"
      "v_mfma_f32_16x16x32_bf16 v[208:211], v[152:155], a[204:207], v[208:211]\n"
      "buffer_load_dwordx4 v[152:155], v1, s[4:7], s65 offen offset:0 sc1\n"
      "v_mul_f32 v19, v43, v43\n"
      "v_fma_f32 v16, v16, v186, v185\n"
      "v_mfma_f32_16x16x32_bf16 v[208:211], v[156:159], a[220:223], v[208:211]\n"
      "buffer_load_dwordx4 v[156:159], v1, s[4:7], s65 offen offset:1024 sc1\n"
      "v_fma_f32 v17, v17, v186, v185\n"
      "v_mfma_f32_16x16x32_bf16 v[196:199], v[160:163], a[224:227], v[196:199]\n"
      "v_fma_f32 v18, v18, v186, v185\n"
      "v_fma_f32 v19, v19, v186, v185\n"
      "v_mul_f32 v16, v16, v40\n"
      "v_mfma_f32_16x16x32_bf16 v[196:199], v[164:167], a[240:243], v[196:199]\n"
      "v_mul_f32 v17, v17, v41\n"
      "v_mul_f32 v18, v18, v42\n"
      "v_mfma_f32_16x16x32_bf16 v[200:203], v[160:163], a[228:231], v[200:203]\n"
      "v_mul_f32 v19, v19, v43\n"
      "v_exp_f32 v16, v16\n"
      "v_mfma_f32_16x16x32_bf16 v[200:203], v[164:167], a[244:247], v[200:203]\n"
      "v_exp_f32 v17, v17\n"
      "v_mfma_f32_16x16x32_bf16 v[204:207], v[160:163], a[232:235], v[204:207]\n"
      "v_exp_f32 v18, v18\n"
      "v_exp_f32 v19, v19\n"
      "v_mfma_f32_16x16x32_bf16 v[204:207], v[164:167], a[248:251], v[204:207]\n"
      "s_waitcnt vmcnt(14)\n"
      "v_add_f32 v16, 1.0, v16\n"
      "v_add_f32 v17, 1.0, v17\n"
      "v_mfma_f32_16x16x32_bf16 v[208:211], v[160:163], a[236:239], v[208:211]\n"
      "buffer_load_dwordx4 v[160:163], v1, s[4:7], s65 offen offset:2048 sc1\n"
      "v_add_f32 v18, 1.0, v18\n"
      "v_mfma_f32_16x16x32_bf16 v[208:211], v[164:167], a[252:255], v[208:211]\n"
      "buffer_load_dwordx4 v[164:167], v1, s[4:7], s65 offen offset:3072 sc1\n"
      "v_add_f32 v19, 1.0, v19\n"
      "v_mfma_f32_16x16x32_bf16 v[196:199], v[168:171], v[96:99], v[196:199]\n"
      "s_add_u32 s65, s65, 4096\n"
      "v_rcp_f32 v16, v16\n"
      "v_rcp_f32 v17, v17\n"
      "v_mfma_f32_16x16x32_bf16 v[196:199], v[172:175], v[112:115], v[196:199]\n"
      "v_rcp_f32 v18, v18\n"
      "v_mfma_f32_16x16x32_bf16 v[200:203], v[168:171], v[100:103], v[200:203]\n"
      "v_rcp_f32 v19, v19\n"
      "v_mfma_f32_16x16x32_bf16 v[200:203], v[172:175], v[116:119], v[200:203]\n"
      "v_mul_f32 v16, v16, v40\n"
      "v_mul_f32 v17, v17, v41\n"
      "v_mul_f32 v18, v18, v42\n"
      "v_mfma_f32_16x16x32_bf16 v[204:207], v[168:171], v[104:107], v[204:207]\n"
      "v_mul_f32 v19, v19, v43\n"
      "v_cvt_pk_bf16_f32 v22, v16, v17\n"
      "v_mfma_f32_16x16x32_bf16 v[204:207], v[172:175], v[120:123], v[204:207]\n"
      "s_waitcnt vmcnt(14)\n"
      "v_cvt_pk_bf16_f32 v23, v18, v19\n"
      "v_mfma_f32_16x16x32_bf16 v[208:211], v[168:171], v[108:111], v[208:211]\n"
      "buffer_load_dwordx4 v[168:171], v1, s[4:7], s65 offen offset:0 sc1\n"
      "ds_write_b64 v47, v[22:23] offset:2048\n"
      "v_add_u32 v44, 4096, v44\n"
      "v_add_u32 v45, 4096, v45\n"
      "v_mfma_f32_16x16x32_bf16 v[208:211], v[172:175], v[124:127], v[208:211]\n"
      "buffer_load_dwordx4 v[172:175], v1, s[4:7], s65 offen offset:1024 sc1\n"
      "v_add_u32 v46, 4096, v46\n"
      "v_add_u32 v47, 4096, v47\n"
      "s_sub_u32 s70, s70, 1\n"
      "s_cbranch_scc0 .LLP18_%=\n"
      "s_waitcnt lgkmcnt(0)\n"
      "s_barrier\n"
      "ds_read_b128 a[0:3], v80 offset:0\n"
      "ds_read_b128 a[16:19], v80 offset:1024\n"
      "ds_read_b128 a[4:7], v81 offset:0\n"
      "ds_read_b128 a[20:23], v81 offset:1024\n"
      "ds_read_b128 a[8:11], v82 offset:0\n"
      "ds_read_b128 a[24:27], v82 offset:1024\n"
      "ds_read_b128 a[12:15], v83 offset:0\n"
      "ds_read_b128 a[28:31], v83 offset:1024\n"
      "ds_read_b128 a[32:35], v80 offset:2048\n"
      "ds_read_b128 a[48:51], v80 offset:3072\n"
      "ds_read_b128 a[36:39], v81 offset:2048\n"
      "ds_read_b128 a[52:55], v81 offset:3072\n"
      "ds_read_b128 a[40:43], v82 offset:2048\n"
      "ds_read_b128 a[56:59], v82 offset:3072\n"
      "ds_read_b128 a[44:47], v83 offset:2048\n"
      "ds_read_b128 a[60:63], v83 offset:3072\n"
      "s_lshl_b32 s46, s39, 2\n"
      "s_mov_b32 s45, 32\n"
      "s_mov_b32 s78, s46\n"
      "s_mov_b32 s79, s46\n"
      "v_mul_f32 v12, v196, v196\n"
      "v_mul_f32 v13, v197, v197\n"
      "v_mul_f32 v14, v198, v198\n"
      "v_mul_f32 v15, v199, v199\n"
      "v_fma_f32 v12, v12, v74, v73\n"
      "v_fma_f32 v13, v13, v74, v73\n"
      "v_fma_f32 v14, v14, v74, v73\n"
      "v_fma_f32 v15, v15, v74, v73\n"
      "v_mul_f32 v12, v12, v196\n"
      "v_mul_f32 v13, v13, v197\n"
      "v_mul_f32 v14, v14, v198\n"
      "v_mul_f32 v15, v15, v199\n"
      "v_exp_f32 v12, v12\n"
      "v_exp_f32 v13, v13\n"
      "v_exp_f32 v14, v14\n"
      "v_exp_f32 v15, v15\n"
      "v_add_f32 v12, 1.0, v12\n"
      "v_add_f32 v13, 1.0, v13\n"
      "v_add_f32 v14, 1.0, v14\n"
      "v_add_f32 v15, 1.0, v15\n"
      "v_rcp_f32 v12, v12\n"
      "v_rcp_f32 v13, v13\n"
      "v_rcp_f32 v14, v14\n"
      "v_rcp_f32 v15, v15\n"
      "v_mul_f32 v12, v12, v196\n"
      "v_mul_f32 v13, v13, v197\n"
      "v_mul_f32 v14, v14, v198\n"
      "v_mul_f32 v15, v15, v199\n"
      "s_waitcnt lgkmcnt(8)\n"
      "v_mfma_f32_16x16x32_bf16 v[28:31], v[56:59], a[0:3], 0\n"
      "s_mov_b32 s80, s79\n"
      "s_mov_b32 s79, s78\n"
      "ds_read_b128 a[64:67], v80 offset:4096\n"
      "ds_read_b128 a[80:83], v80 offset:5120\n"
      "v_cvt_pk_bf16_f32 v20, v12, v13\n"
      "v_mfma_f32_16x16x32_bf16 v[28:31], v[60:63], a[16:19], v[28:31]\n"
      "s_add_u32 s46, s46, 4\n"
      "s_and_b32 s46, s46, 31\n"
      "ds_read_b128 a[68:71], v81 offset:4096\n"
      "v_cvt_pk_bf16_f32 v21, v14, v15\n"
      "v_mfma_f32_16x16x32_bf16 v[32:35], v[56:59], a[4:7], 0\n"
      "s_mov_b32 s78, s45\n"
      "ds_read_b128 a[84:87], v81 offset:5120\n"
      "ds_read_b128 a[72:75], v82 offset:4096\n"
      "ds_write_b64 v44, v[20:21] offset:0\n"
      "v_mul_f32 v16, v200, v200\n"
      "v_mfma_f32_16x16x32_bf16 v[32:35], v[60:63], a[20:23], v[32:35]\n"
      "s_mov_b32 s45, s46\n"
      "s_mul_i32 s65, s46, 18432\n"
      "ds_read_b128 a[88:91], v82 offset:5120\n"
      "v_mul_f32 v17, v201, v201\n"
      "v_mfma_f32_16x16x32_bf16 v[36:39], v[56:59], a[8:11], 0\n"
      "s_add_u32 s65, s65, s53\n"
      "ds_read_b128 a[76:79], v83 offset:4096\n"
      "v_mul_f32 v18, v202, v202\n"
      "v_mul_f32 v19, v203, v203\n"
      "v_mfma_f32_16x16x32_bf16 v[36:39], v[60:63], a[24:27], v[36:39]\n"
      "s_waitcnt vmcnt(14)\n"
      "ds_read_b128 a[92:95], v83 offset:5120\n"
      "v_fma_f32 v16, v16, v86, v85\n"
      "v_fma_f32 v17, v17, v86, v85\n"
      "v_fma_f32 v18, v18, v86, v85\n"
      "v_mfma_f32_16x16x32_bf16 v[40:43], v[56:59], a[12:15], 0\n"
      "buffer_load_dwordx4 v[56:59], v1, s[4:7], s65 offen offset:0 sc1\n"
      "v_fma_f32 v19, v19, v86, v85\n"
      "v_mul_f32 v16, v16, v200\n"
      "v_mfma_f32_16x16x32_bf16 v[40:43], v[60:63], a[28:31], v[40:43]\n"
      "s_waitcnt lgkmcnt(9)\n"
      "buffer_load_dwordx4 v[60:63], v1, s[4:7], s65 offen offset:1024 sc1\n"
      "v_mul_f32 v17, v17, v201\n"
      "v_mul_f32 v18, v18, v202\n"
      "v_mfma_f32_16x16x32_bf16 v[28:31], v[64:67], a[32:35], v[28:31]\n"
      "ds_read_b128 a[96:99], v80 offset:6144\n"
      "ds_read_b128 a[112:115], v80 offset:7168\n"
      "v_mul_f32 v19, v19, v203\n"
      "v_exp_f32 v16, v16\n"
      "v_mfma_f32_16x16x32_bf16 v[28:31], v[68:71], a[48:51], v[28:31]\n"
      "ds_read_b128 a[100:103], v81 offset:6144\n"
      "v_exp_f32 v17, v17\n"
      "v_mfma_f32_16x16x32_bf16 v[32:35], v[64:67], a[36:39], v[32:35]\n"
      "ds_read_b128 a[116:119], v81 offset:7168\n"
      "ds_read_b128 a[104:107], v82 offset:6144\n"
      "v_exp_f32 v18, v18\n"
      "v_mfma_f32_16x16x32_bf16 v[32:35], v[68:71], a[52:55], v[32:35]\n"
      "ds_read_b128 a[120:123], v82 offset:7168\n"
      "v_exp_f32 v19, v19\n"
      "v_mfma_f32_16x16x32_bf16 v[36:39], v[64:67], a[40:43], v[36:39]\n"
      "ds_read_b128 a[108:111], v83 offset:6144\n"
      "v_add_f32 v16, 1.0, v16\n"
      "v_add_f32 v17, 1.0, v17\n"
      "v_add_f32 v18, 1.0, v18\n"
      "v_mfma_f32_16x16x32_bf16 v[36:39], v[68:71], a[56:59], v[36:39]\n"
      "s_waitcnt vmcnt(14)\n"
      "ds_read_b128 a[124:127], v83 offset:7168\n"
      "v_add_f32 v19, 1.0, v19\n"
      "v_mfma_f32_16x16x32_bf16 v[40:43], v[64:67], a[44:47], v[40:43]\n"
      "buffer_load_dwordx4 v[64:67], v1, s[4:7], s65 offen offset:2048 sc1\n"
      "v_rcp_f32 v16, v16\n"
      "v_rcp_f32 v17, v17\n"
      "v_mfma_f32_16x16x32_bf16 v[40:43], v[68:71], a[60:63], v[40:43]\n"
      "s_waitcnt lgkmcnt(8)\n"
      "buffer_load_dwordx4 v[68:71], v1, s[4:7], s65 offen offset:3072 sc1\n"
      "v_rcp_f32 v18, v18\n"
      "v_mfma_f32_16x16x32_bf16 v[28:31], v[76:79], a[64:67], v[28:31]\n"
      "s_add_u32 s65, s65, 4096\n"
      "ds_read_b128 a[128:131], v80 offset:8192\n"
      "ds_read_b128 a[144:147], v80 offset:9216\n"
      "v_mfma_f32_16x16x32_bf16 v[28:31], v[92:95], a[80:83], v[28:31]\n"
      "ds_read_b128 a[132:135], v81 offset:8192\n"
      "v_rcp_f32 v19, v19\n"
      "v_mul_f32 v16, v16, v200\n"
      "v_mul_f32 v17, v17, v201\n"
      "v_mfma_f32_16x16x32_bf16 v[32:35], v[76:79], a[68:71], v[32:35]\n"
      "ds_read_b128 a[148:151], v81 offset:9216\n"
      "ds_read_b128 a[136:139], v82 offset:8192\n"
      "v_mul_f32 v18, v18, v202\n"
      "v_mfma_f32_16x16x32_bf16 v[32:35], v[92:95], a[84:87], v[32:35]\n"
      "ds_read_b128 a[152:155], v82 offset:9216\n"
      "v_mul_f32 v19, v19, v203\n"
      "v_cvt_pk_bf16_f32 v22, v16, v17\n"
      "v_cvt_pk_bf16_f32 v23, v18, v19\n"
      "v_mfma_f32_16x16x32_bf16 v[36:39], v[76:79], a[72:75], v[36:39]\n"
      "ds_read_b128 a[140:143], v83 offset:8192\n"
      "ds_write_b64 v45, v[22:23] offset:0\n"
      "v_mul_f32 v12, v204, v204\n"
      "v_mul_f32 v13, v205, v205\n"
      "v_mfma_f32_16x16x32_bf16 v[36:39], v[92:95], a[88:91], v[36:39]\n"
      "s_waitcnt vmcnt(14)\n"
      "ds_read_b128 a[156:159], v83 offset:9216\n"
      "v_mul_f32 v14, v206, v206\n"
      "v_mul_f32 v15, v207, v207\n"
      "v_mfma_f32_16x16x32_bf16 v[40:43], v[76:79], a[76:79], v[40:43]\n"
      "buffer_load_dwordx4 v[76:79], v1, s[4:7], s65 offen offset:0 sc1\n"
      "v_fma_f32 v12, v12, v182, v181\n"
      "v_fma_f32 v13, v13, v182, v181\n"
      "v_mfma_f32_16x16x32_bf16 v[40:43], v[92:95], a[92:95], v[40:43]\n"
      "s_waitcnt lgkmcnt(9)\n"
      "buffer_load_dwordx4 v[92:95], v1, s[4:7], s65 offen offset:1024 sc1\n"
      "v_fma_f32 v14, v14, v182, v181\n"
      "v_fma_f32 v15, v15, v182, v181\n"
      "v_mul_f32 v12, v12, v204\n"
      "v_mfma_f32_16x16x32_bf16 v[28:31], v[128:131], a[96:99], v[28:31]\n"
      "ds_read_b128 a[160:163], v80 offset:10240\n"
      "ds_read_b128 a[176:179], v80 offset:11264\n"
      "v_mul_f32 v13, v13, v205\n"
      "v_mfma_f32_16x16x32_bf16 v[28:31], v[132:135], a[112:115], v[28:31]\n"
      "ds_read_b128 a[164:167], v81 offset:10240\n"
      "v_mul_f32 v14, v14, v206\n"
      "v_mul_f32 v15, v15, v207\n"
      "v_exp_f32 v12, v12\n"
      "v_mfma_f32_16x16x32_bf16 v[32:35], v[128:131], a[100:103], v[32:35]\n"
      "ds_read_b128 a[180:183], v81 offset:11264\n"
      "ds_read_b128 a[168:171], v82 offset:10240\n"
      "v_mfma_f32_16x16x32_bf16 v[32:35], v[132:135], a[116:119], v[32:35]\n"
      "ds_read_b128 a[184:187], v82 offset:11264\n"
      "v_exp_f32 v13, v13\n"
      "v_exp_f32 v14, v14\n"
      "v_mfma_f32_16x16x32_bf16 v[36:39], v[128:131], a[104:107], v[36:39]\n"
      "ds_read_b128 a[172:175], v83 offset:10240\n"
      "v_exp_f32 v15, v15\n"
      "v_mfma_f32_16x16x32_bf16 v[36:39], v[132:135], a[120:123], v[36:39]\n"
      "s_waitcnt vmcnt(14)\n"
      "ds_read_b128 a[188:191], v83 offset:11264\n"
      "v_add_f32 v12, 1.0, v12\n"
      "v_add_f32 v13, 1.0, v13\n"
      "v_mfma_f32_16x16x32_bf16 v[40:43], v[128:131], a[108:111], v[40:43]\n"
      "buffer_load_dwordx4 v[128:131], v1, s[4:7], s65 offen offset:2048 sc1\n"
      "v_add_f32 v14, 1.0, v14\n"
      "v_add_f32 v15, 1.0, v15\n"
      "v_mfma_f32_16x16x32_bf16 v[40:43], v[132:135], a[124:127], v[40:43]\n"
      "s_waitcnt lgkmcnt(8)\n"
      "buffer_load_dwordx4 v[132:135], v1, s[4:7], s65 offen offset:3072 sc1\n"
      "v_rcp_f32 v12, v12\n"
      "v_rcp_f32 v13, v13\n"
      "v_mfma_f32_16x16x32_bf16 v[28:31], v[136:139], a[128:131], v[28:31]\n"
      "s_add_u32 s65, s65, 4096\n"
      "ds_read_b128 a[192:195], v80 offset:12288\n"
      "ds_read_b128 a[208:211], v80 offset:13312\n"
      "v_mfma_f32_16x16x32_bf16 v[28:31], v[140:143], a[144:147], v[28:31]\n"
      "ds_read_b128 a[196:199], v81 offset:12288\n"
      "v_rcp_f32 v14, v14\n"
      "v_mfma_f32_16x16x32_bf16 v[32:35], v[136:139], a[132:135], v[32:35]\n"
      "ds_read_b128 a[212:215], v81 offset:13312\n"
      "ds_read_b128 a[200:203], v82 offset:12288\n"
      "v_rcp_f32 v15, v15\n"
      "v_mul_f32 v12, v12, v204\n"
      "v_mfma_f32_16x16x32_bf16 v[32:35], v[140:143], a[148:151], v[32:35]\n"
      "ds_read_b128 a[216:219], v82 offset:13312\n"
      "v_mul_f32 v13, v13, v205\n"
      "v_mul_f32 v14, v14, v206\n"
      "v_mfma_f32_16x16x32_bf16 v[36:39], v[136:139], a[136:139], v[36:39]\n"
      "ds_read_b128 a[204:207], v83 offset:12288\n"
      "v_mul_f32 v15, v15, v207\n"
      "v_cvt_pk_bf16_f32 v20, v12, v13\n"
      "v_cvt_pk_bf16_f32 v21, v14, v15\n"
      "v_mfma_f32_16x16x32_bf16 v[36:39], v[140:143], a[152:155], v[36:39]\n"
      "s_waitcnt vmcnt(14)\n"
      "ds_read_b128 a[220:223], v83 offset:13312\n"
      "ds_write_b64 v46, v[20:21] offset:0\n"
      "v_mul_f32 v16, v208, v208\n"
      "v_mfma_f32_16x16x32_bf16 v[40:43], v[136:139], a[140:143], v[40:43]\n"
      "buffer_load_dwordx4 v[136:139], v1, s[4:7], s65 offen offset:0 sc1\n"
      "v_mul_f32 v17, v209, v209\n"
      "v_mul_f32 v18, v210, v210\n"
      "v_mul_f32 v19, v211, v211\n"
      "v_mfma_f32_16x16x32_bf16 v[40:43], v[140:143], a[156:159], v[40:43]\n"
      "s_waitcnt lgkmcnt(9)\n"
      "buffer_load_dwordx4 v[140:143], v1, s[4:7], s65 offen offset:1024 sc1\n"
      "v_fma_f32 v16, v16, v186, v185\n"
      "v_fma_f32 v17, v17, v186, v185\n"
      "v_mfma_f32_16x16x32_bf16 v[28:31], v[144:147], a[160:163], v[28:31]\n"
      "ds_read_b128 a[224:227], v80 offset:14336\n"
      "ds_read_b128 a[240:243], v80 offset:15360\n"
      "v_fma_f32 v18, v18, v186, v185\n"
      "v_fma_f32 v19, v19, v186, v185\n"
      "v_mfma_f32_16x16x32_bf16 v[28:31], v[148:151], a[176:179], v[28:31]\n"
      "ds_read_b128 a[228:231], v81 offset:14336\n"
      "v_mul_f32 v16, v16, v208\n"
      "v_mul_f32 v17, v17, v209\n"
      "v_mfma_f32_16x16x32_bf16 v[32:35], v[144:147], a[164:167], v[32:35]\n"
      "ds_read_b128 a[244:247], v81 offset:15360\n"
      "ds_read_b128 a[232:235], v82 offset:14336\n"
      "v_mul_f32 v18, v18, v210\n"
      "v_mul_f32 v19, v19, v211\n"
      "v_mfma_f32_16x16x32_bf16 v[32:35], v[148:151], a[180:183], v[32:35]\n"
      "ds_read_b128 a[248:251], v82 offset:15360\n"
      "v_exp_f32 v16, v16\n"
      "v_mfma_f32_16x16x32_bf16 v[36:39], v[144:147], a[168:171], v[36:39]\n"
      "ds_read_b128 a[236:239], v83 offset:14336\n"
      "v_exp_f32 v17, v17\n"
      "v_exp_f32 v18, v18\n"
      "v_mfma_f32_16x16x32_bf16 v[36:39], v[148:151], a[184:187], v[36:39]\n"
      "s_waitcnt vmcnt(14)\n"
      "ds_read_b128 a[252:255], v83 offset:15360\n"
      "v_exp_f32 v19, v19\n"
      "v_mfma_f32_16x16x32_bf16 v[40:43], v[144:147], a[172:175], v[40:43]\n"
      "buffer_load_dwordx4 v[144:147], v1, s[4:7], s65 offen offset:2048 sc1\n"
      "v_add_f32 v16, 1.0, v16\n"
      "v_add_f32 v17, 1.0, v17\n"
      "v_mfma_f32_16x16x32_bf16 v[40:43], v[148:151], a[188:191], v[40:43]\n"
      "s_waitcnt lgkmcnt(9)\n"
      "buffer_load_dwordx4 v[148:151], v1, s[4:7], s65 offen offset:3072 sc1\n"
      "v_add_f32 v18, 1.0, v18\n"
      "v_add_f32 v19, 1.0, v19\n"
      "v_mfma_f32_16x16x32_bf16 v[28:31], v[152:155], a[192:195], v[28:31]\n"
      "s_add_u32 s65, s65, 4096\n"
      "v_rcp_f32 v16, v16\n"
      "v_rcp_f32 v17, v17\n"
      "v_mfma_f32_16x16x32_bf16 v[28:31], v[156:159], a[208:211], v[28:31]\n"
      "v_rcp_f32 v18, v18\n"
      "v_rcp_f32 v19, v19\n"
      "v_mfma_f32_16x16x32_bf16 v[32:35], v[152:155], a[196:199], v[32:35]\n"
      "v_mul_f32 v16, v16, v208\n"
      "v_mul_f32 v17, v17, v209\n"
      "v_mul_f32 v18, v18, v210\n"
      "v_mfma_f32_16x16x32_bf16 v[32:35], v[156:159], a[212:215], v[32:35]\n"
      "v_mul_f32 v19, v19, v211\n"
      "v_cvt_pk_bf16_f32 v22, v16, v17\n"
      "v_cvt_pk_bf16_f32 v23, v18, v19\n"
      "ds_write_b64 v47, v[22:23] offset:0\n"
      "v_mfma_f32_16x16x32_bf16 v[36:39], v[152:155], a[200:203], v[36:39]\n"
      "v_mfma_f32_16x16x32_bf16 v[36:39], v[156:159], a[216:219], v[36:39]\n"
      "s_waitcnt vmcnt(14)\n"
      "v_mfma_f32_16x16x32_bf16 v[40:43], v[152:155], a[204:207], v[40:43]\n"
      "buffer_load_dwordx4 v[152:155], v1, s[4:7], s65 offen offset:0 sc1\n"
      "v_mfma_f32_16x16x32_bf16 v[40:43], v[156:159], a[220:223], v[40:43]\n"
      "s_waitcnt lgkmcnt(1)\n"
      "buffer_load_dwordx4 v[156:159], v1, s[4:7], s65 offen offset:1024 sc1\n"
      "s_waitcnt lgkmcnt(0)\n"
      "s_barrier\n"
      "v_mfma_f32_16x16x32_bf16 v[28:31], v[160:163], a[224:227], v[28:31]\n"
      "ds_read_b128 v[96:99], v80 offset:16384\n"
      "ds_read_b128 v[112:115], v80 offset:17408\n"
      "v_mfma_f32_16x16x32_bf16 v[28:31], v[164:167], a[240:243], v[28:31]\n"
      "ds_read_b128 v[100:103], v81 offset:16384\n"
      "ds_read_b128 v[116:119], v81 offset:17408\n"
      "v_mfma_f32_16x16x32_bf16 v[32:35], v[160:163], a[228:231], v[32:35]\n"
      "ds_read_b128 v[104:107], v82 offset:16384\n"
      "ds_read_b128 v[120:123], v82 offset:17408\n"
      "v_mfma_f32_16x16x32_bf16 v[32:35], v[164:167], a[244:247], v[32:35]\n"
      "ds_read_b128 v[108:111], v83 offset:16384\n"
      "ds_read_b128 v[124:127], v83 offset:17408\n"
      "v_mfma_f32_16x16x32_bf16 v[36:39], v[160:163], a[232:235], v[36:39]\n"
      "v_mfma_f32_16x16x32_bf16 v[36:39], v[164:167], a[248:251], v[36:39]\n"
      "s_waitcnt vmcnt(14)\n"
      "v_mfma_f32_16x16x32_bf16 v[40:43], v[160:163], a[236:239], v[40:43]\n"
      "buffer_load_dwordx4 v[160:163], v1, s[4:7], s65 offen offset:2048 sc1\n"
      "v_mfma_f32_16x16x32_bf16 v[40:43], v[164:167], a[252:255], v[40:43]\n"
      "s_waitcnt lgkmcnt(0)\n"
      "buffer_load_dwordx4 v[164:167], v1, s[4:7], s65 offen offset:3072 sc1\n"
      "v_mfma_f32_16x16x32_bf16 v[28:31], v[168:171], v[96:99], v[28:31]\n"
      "s_add_u32 s65, s65, 4096\n"
      "v_mfma_f32_16x16x32_bf16 v[28:31], v[172:175], v[112:115], v[28:31]\n"
      "v_mfma_f32_16x16x32_bf16 v[32:35], v[168:171], v[100:103], v[32:35]\n"
      "v_mfma_f32_16x16x32_bf16 v[32:35], v[172:175], v[116:119], v[32:35]\n"
      "v_mfma_f32_16x16x32_bf16 v[36:39], v[168:171], v[104:107], v[36:39]\n"
      "v_mfma_f32_16x16x32_bf16 v[36:39], v[172:175], v[120:123], v[36:39]\n"
      "s_waitcnt vmcnt(14)\n"
      "v_mfma_f32_16x16x32_bf16 v[40:43], v[168:171], v[108:111], v[40:43]\n"
      "buffer_load_dwordx4 v[168:171], v1, s[4:7], s65 offen offset:0 sc1\n"
      "v_mfma_f32_16x16x32_bf16 v[40:43], v[172:175], v[124:127], v[40:43]\n"
      "buffer_load_dwordx4 v[172:175], v1, s[4:7], s65 offen offset:1024 sc1\n"
      "s_mov_b32 s70, 2\n"
      ".LLP22_%=:\n"
      "v_mfma_f32_16x16x32_bf16 v[196:199], v[56:59], a[0:3], 0\n"
      "s_mov_b32 s80, s79\n"
      "s_mov_b32 s79, s78\n"
      "v_mfma_f32_16x16x32_bf16 v[196:199], v[60:63], a[16:19], v[196:199]\n"
      "s_add_u32 s46, s46, 4\n"
      "s_and_b32 s46, s46, 31\n"
      "v_mfma_f32_16x16x32_bf16 v[200:203], v[56:59], a[4:7], 0\n"
      "s_mov_b32 s78, s45\n"
      "v_cvt_pk_bf16_f32 v12, v28, v29\n"
      "v_cvt_pk_bf16_f32 v13, v30, v31\n"
      "v_mfma_f32_16x16x32_bf16 v[200:203], v[60:63], a[20:23], v[200:203]\n"
      "s_mov_b32 s45, s46\n"
      "s_mul_i32 s65, s46, 18432\n"
      "v_cvt_pk_bf16_f32 v14, v32, v33\n"
      "v_mfma_f32_16x16x32_bf16 v[204:207], v[56:59], a[8:11], 0\n"
      "s_add_u32 s65, s65, s53\n"
      "v_cvt_pk_bf16_f32 v15, v34, v35\n"
      "v_cvt_pk_bf16_f32 v16, v36, v37\n"
      "v_mfma_f32_16x16x32_bf16 v[204:207], v[60:63], a[24:27], v[204:207]\n"
      "s_waitcnt vmcnt(14)\n"
      "v_cvt_pk_bf16_f32 v17, v38, v39\n"
      "v_cvt_pk_bf16_f32 v18, v40, v41\n"
      "v_mfma_f32_16x16x32_bf16 v[208:211], v[56:59], a[12:15], 0\n"
      "buffer_load_dwordx4 v[56:59], v1, s[4:7], s65 offen offset:0 sc1\n"
      "v_mfma_f32_16x16x32_bf16 v[208:211], v[60:63], a[28:31], v[208:211]\n"
      "buffer_load_dwordx4 v[60:63], v1, s[4:7], s65 offen offset:1024 sc1\n"
      "v_cvt_pk_bf16_f32 v19, v42, v43\n"
      "v_mfma_f32_16x16x32_bf16 v[196:199], v[64:67], a[32:35], v[196:199]\n"
      "s_lshl_b32 s47, s79, 11\n"
      "s_add_u32 s47, s47, s73\n"
      "v_mfma_f32_16x16x32_bf16 v[196:199], v[68:71], a[48:51], v[196:199]\n"
      "buffer_store_dwordx4 v[12:15], v1, s[4:7], s47 offen sc1\n"
      "v_mfma_f32_16x16x32_bf16 v[200:203], v[64:67], a[36:39], v[200:203]\n"
      "buffer_store_dwordx4 v[16:19], v1, s[4:7], s47 offen offset:1024 sc1\n"
      "v_mfma_f32_16x16x32_bf16 v[200:203], v[68:71], a[52:55], v[200:203]\n"
      "v_mfma_f32_16x16x32_bf16 v[204:207], v[64:67], a[40:43], v[204:207]\n"
      "v_mfma_f32_16x16x32_bf16 v[204:207], v[68:71], a[56:59], v[204:207]\n"
      "s_waitcnt vmcnt(16)\n"
      "v_mfma_f32_16x16x32_bf16 v[208:211], v[64:67], a[44:47], v[208:211]\n"
      "buffer_load_dwordx4 v[64:67], v1, s[4:7], s65 offen offset:2048 sc1\n"
      "v_mfma_f32_16x16x32_bf16 v[208:211], v[68:71], a[60:63], v[208:211]\n"
      "buffer_load_dwordx4 v[68:71], v1, s[4:7], s65 offen offset:3072 sc1\n"
      "v_mfma_f32_16x16x32_bf16 v[196:199], v[76:79], a[64:67], v[196:199]\n"
      "s_add_u32 s65, s65, 4096\n"
      "v_mfma_f32_16x16x32_bf16 v[196:199], v[92:95], a[80:83], v[196:199]\n"
      "v_mfma_f32_16x16x32_bf16 v[200:203], v[76:79], a[68:71], v[200:203]\n"
      "v_mfma_f32_16x16x32_bf16 v[200:203], v[92:95], a[84:87], v[200:203]\n"
      "v_mfma_f32_16x16x32_bf16 v[204:207], v[76:79], a[72:75], v[204:207]\n"
      "v_mfma_f32_16x16x32_bf16 v[204:207], v[92:95], a[88:91], v[204:207]\n"
      "s_waitcnt vmcnt(16)\n"
      "v_mfma_f32_16x16x32_bf16 v[208:211], v[76:79], a[76:79], v[208:211]\n"
      "buffer_load_dwordx4 v[76:79], v1, s[4:7], s65 offen offset:0 sc1\n"
      "v_mfma_f32_16x16x32_bf16 v[208:211], v[92:95], a[92:95], v[208:211]\n"
      "buffer_load_dwordx4 v[92:95], v1, s[4:7], s65 offen offset:1024 sc1\n"
      "v_mfma_f32_16x16x32_bf16 v[196:199], v[128:131], a[96:99], v[196:199]\n"
      "v_mfma_f32_16x16x32_bf16 v[196:199], v[132:135], a[112:115], v[196:199]\n"
      "v_mfma_f32_16x16x32_bf16 v[200:203], v[128:131], a[100:103], v[200:203]\n"
      "v_mfma_f32_16x16x32_bf16 v[200:203], v[132:135], a[116:119], v[200:203]\n"
      "v_mfma_f32_16x16x32_bf16 v[204:207], v[128:131], a[104:107], v[204:207]\n"
      "v_mfma_f32_16x16x32_bf16 v[204:207], v[132:135], a[120:123], v[204:207]\n"
      "s_waitcnt vmcnt(16)\n"
      "v_mfma_f32_16x16x32_bf16 v[208:211], v[128:131], a[108:111], v[208:211]\n"
      "buffer_load_dwordx4 v[128:131], v1, s[4:7], s65 offen offset:2048 sc1\n"
      "v_mfma_f32_16x16x32_bf16 v[208:211], v[132:135], a[124:127], v[208:211]\n"
      "buffer_load_dwordx4 v[132:135], v1, s[4:7], s65 offen offset:3072 sc1\n"
      "v_mfma_f32_16x16x32_bf16 v[196:199], v[136:139], a[128:131], v[196:199]\n"
      "s_add_u32 s65, s65, 4096\n"
      "v_mfma_f32_16x16x32_bf16 v[196:199], v[140:143], a[144:147], v[196:199]\n"
      "s_lshl_b32 s47, s80, 7\n"
      "s_add_u32 s47, s47, s74\n"
      "v_mfma_f32_16x16x32_bf16 v[200:203], v[136:139], a[132:135], v[200:203]\n"
      "buffer_store_dwordx2 v[6:7], v11, s[4:7], s47 offen sc1\n"
      "v_mfma_f32_16x16x32_bf16 v[200:203], v[140:143], a[148:151], v[200:203]\n"
      "v_mfma_f32_16x16x32_bf16 v[204:207], v[136:139], a[136:139], v[204:207]\n"
      "v_mfma_f32_16x16x32_bf16 v[204:207], v[140:143], a[152:155], v[204:207]\n"
      "s_waitcnt vmcnt(17)\n"
      "v_mfma_f32_16x16x32_bf16 v[208:211], v[136:139], a[140:143], v[208:211]\n"
      "buffer_load_dwordx4 v[136:139], v1, s[4:7], s65 offen offset:0 sc1\n"
      "v_mfma_f32_16x16x32_bf16 v[208:211], v[140:143], a[156:159], v[208:211]\n"
      "buffer_load_dwordx4 v[140:143], v1, s[4:7], s65 offen offset:1024 sc1\n"
      "v_mfma_f32_16x16x32_bf16 v[196:199], v[144:147], a[160:163], v[196:199]\n"
      "v_mfma_f32_16x16x32_bf16 v[196:199], v[148:151], a[176:179], v[196:199]\n"
      "v_mfma_f32_16x16x32_bf16 v[200:203], v[144:147], a[164:167], v[200:203]\n"
      "v_mfma_f32_16x16x32_bf16 v[200:203], v[148:151], a[180:183], v[200:203]\n"
      "v_mfma_f32_16x16x32_bf16 v[204:207], v[144:147], a[168:171], v[204:207]\n"
      "v_mfma_f32_16x16x32_bf16 v[204:207], v[148:151], a[184:187], v[204:207]\n"
      "s_waitcnt vmcnt(17)\n"
      "v_mfma_f32_16x16x32_bf16 v[208:211], v[144:147], a[172:175], v[208:211]\n"
      "buffer_load_dwordx4 v[144:147], v1, s[4:7], s65 offen offset:2048 sc1\n"
      "v_mfma_f32_16x16x32_bf16 v[208:211], v[148:151], a[188:191], v[208:211]\n"
      "buffer_load_dwordx4 v[148:151], v1, s[4:7], s65 offen offset:3072 sc1\n"
      "v_mfma_f32_16x16x32_bf16 v[196:199], v[152:155], a[192:195], v[196:199]\n"
      "s_add_u32 s65, s65, 4096\n"
      "v_mfma_f32_16x16x32_bf16 v[196:199], v[156:159], a[208:211], v[196:199]\n"
      "v_mfma_f32_16x16x32_bf16 v[200:203], v[152:155], a[196:199], v[200:203]\n"
      "v_mfma_f32_16x16x32_bf16 v[200:203], v[156:159], a[212:215], v[200:203]\n"
      "v_mfma_f32_16x16x32_bf16 v[204:207], v[152:155], a[200:203], v[204:207]\n"
      "v_mfma_f32_16x16x32_bf16 v[204:207], v[156:159], a[216:219], v[204:207]\n"
      "s_waitcnt vmcnt(17)\n"
      "v_mfma_f32_16x16x32_bf16 v[208:211], v[152:155], a[204:207], v[208:211]\n"
      "buffer_load_dwordx4 v[152:155], v1, s[4:7], s65 offen offset:0 sc1\n"
      "v_mfma_f32_16x16x32_bf16 v[208:211], v[156:159], a[220:223], v[208:211]\n"
      "buffer_load_dwordx4 v[156:159], v1, s[4:7], s65 offen offset:1024 sc1\n"
      "v_mfma_f32_16x16x32_bf16 v[196:199], v[160:163], a[224:227], v[196:199]\n"
      "v_mfma_f32_16x16x32_bf16 v[196:199], v[164:167], a[240:243], v[196:199]\n"
      "v_mfma_f32_16x16x32_bf16 v[200:203], v[160:163], a[228:231], v[200:203]\n"
      "v_mfma_f32_16x16x32_bf16 v[200:203], v[164:167], a[244:247], v[200:203]\n"
      "v_mfma_f32_16x16x32_bf16 v[204:207], v[160:163], a[232:235], v[204:207]\n"
      "v_mfma_f32_16x16x32_bf16 v[204:207], v[164:167], a[248:251], v[204:207]\n"
      "s_waitcnt vmcnt(17)\n"
      "v_mfma_f32_16x16x32_bf16 v[208:211], v[160:163], a[236:239], v[208:211]\n"
      "buffer_load_dwordx4 v[160:163], v1, s[4:7], s65 offen offset:2048 sc1\n"
      "v_mfma_f32_16x16x32_bf16 v[208:211], v[164:167], a[252:255], v[208:211]\n"
      "buffer_load_dwordx4 v[164:167], v1, s[4:7], s65 offen offset:3072 sc1\n"
      "v_mfma_f32_16x16x32_bf16 v[196:199], v[168:171], v[96:99], v[196:199]\n"
      "s_add_u32 s65, s65, 4096\n"
      "v_mfma_f32_16x16x32_bf16 v[196:199], v[172:175], v[112:115], v[196:199]\n"
      "v_mfma_f32_16x16x32_bf16 v[200:203], v[168:171], v[100:103], v[200:203]\n"
      "v_mfma_f32_16x16x32_bf16 v[200:203], v[172:175], v[116:119], v[200:203]\n"
      "v_mfma_f32_16x16x32_bf16 v[204:207], v[168:171], v[104:107], v[204:207]\n"
      "v_mfma_f32_16x16x32_bf16 v[204:207], v[172:175], v[120:123], v[204:207]\n"
      "s_waitcnt vmcnt(17)\n"
      "v_mfma_f32_16x16x32_bf16 v[208:211], v[168:171], v[108:111], v[208:211]\n"
      "buffer_load_dwordx4 v[168:171], v1, s[4:7], s65 offen offset:0 sc1\n"
      "v_mfma_f32_16x16x32_bf16 v[208:211], v[172:175], v[124:127], v[208:211]\n"
      "buffer_load_dwordx4 v[172:175], v1, s[4:7], s65 offen offset:1024 sc1\n"
      "v_mfma_f32_16x16x32_bf16 v[28:31], v[56:59], a[0:3], 0\n"
      "s_mov_b32 s80, s79\n"
      "s_mov_b32 s79, s78\n"
      "v_mfma_f32_16x16x32_bf16 v[28:31], v[60:63], a[16:19], v[28:31]\n"
      "s_add_u32 s46, s46, 4\n"
      "s_and_b32 s46, s46, 31\n"
      "v_mfma_f32_16x16x32_bf16 v[32:35], v[56:59], a[4:7], 0\n"
      "s_mov_b32 s78, s45\n"
      "v_cvt_pk_bf16_f32 v12, v196, v197\n"
      "v_cvt_pk_bf16_f32 v13, v198, v199\n"
      "v_mfma_f32_16x16x32_bf16 v[32:35], v[60:63], a[20:23], v[32:35]\n"
      "s_mov_b32 s45, s46\n"
      "s_mul_i32 s65, s46, 18432\n"
      "v_cvt_pk_bf16_f32 v14, v200, v201\n"
      "v_mfma_f32_16x16x32_bf16 v[36:39], v[56:59], a[8:11], 0\n"
      "s_add_u32 s65, s65, s53\n"
      "v_cvt_pk_bf16_f32 v15, v202, v203\n"
      "v_cvt_pk_bf16_f32 v16, v204, v205\n"
      "v_mfma_f32_16x16x32_bf16 v[36:39], v[60:63], a[24:27], v[36:39]\n"
      "s_waitcnt vmcnt(15)\n"
      "v_cvt_pk_bf16_f32 v17, v206, v207\n"
      "v_cvt_pk_bf16_f32 v18, v208, v209\n"
      "v_mfma_f32_16x16x32_bf16 v[40:43], v[56:59], a[12:15], 0\n"
      "buffer_load_dwordx4 v[56:59], v1, s[4:7], s65 offen offset:0 sc1\n"
      "v_mfma_f32_16x16x32_bf16 v[40:43], v[60:63], a[28:31], v[40:43]\n"
      "buffer_load_dwordx4 v[60:63], v1, s[4:7], s65 offen offset:1024 sc1\n"
      "v_cvt_pk_bf16_f32 v19, v210, v211\n"
      "v_mfma_f32_16x16x32_bf16 v[28:31], v[64:67], a[32:35], v[28:31]\n"
      "s_lshl_b32 s47, s79, 11\n"
      "s_add_u32 s47, s47, s73\n"
      "v_mfma_f32_16x16x32_bf16 v[28:31], v[68:71], a[48:51], v[28:31]\n"
      "buffer_store_dwordx4 v[12:15], v1, s[4:7], s47 offen sc1\n"
      "v_mfma_f32_16x16x32_bf16 v[32:35], v[64:67], a[36:39], v[32:35]\n"
      "buffer_store_dwordx4 v[16:19], v1, s[4:7], s47 offen offset:1024 sc1\n"
      "v_mfma_f32_16x16x32_bf16 v[32:35], v[68:71], a[52:55], v[32:35]\n"
      "v_mfma_f32_16x16x32_bf16 v[36:39], v[64:67], a[40:43], v[36:39]\n"
      "v_mfma_f32_16x16x32_bf16 v[36:39], v[68:71], a[56:59], v[36:39]\n"
      "s_waitcnt vmcnt(17)\n"
      "v_mfma_f32_16x16x32_bf16 v[40:43], v[64:67], a[44:47], v[40:43]\n"
      "buffer_load_dwordx4 v[64:67], v1, s[4:7], s65 offen offset:2048 sc1\n"
      "v_mfma_f32_16x16x32_bf16 v[40:43], v[68:71], a[60:63], v[40:43]\n"
      "buffer_load_dwordx4 v[68:71], v1, s[4:7], s65 offen offset:3072 sc1\n"
      "v_mfma_f32_16x16x32_bf16 v[28:31], v[76:79], a[64:67], v[28:31]\n"
      "s_add_u32 s65, s65, 4096\n"
      "v_mfma_f32_16x16x32_bf16 v[28:31], v[92:95], a[80:83], v[28:31]\n"
      "v_mfma_f32_16x16x32_bf16 v[32:35], v[76:79], a[68:71], v[32:35]\n"
      "v_mfma_f32_16x16x32_bf16 v[32:35], v[92:95], a[84:87], v[32:35]\n"
      "v_mfma_f32_16x16x32_bf16 v[36:39], v[76:79], a[72:75], v[36:39]\n"
      "v_mfma_f32_16x16x32_bf16 v[36:39], v[92:95], a[88:91], v[36:39]\n"
      "s_waitcnt vmcnt(17)\n"
      "v_mfma_f32_16x16x32_bf16 v[40:43], v[76:79], a[76:79], v[40:43]\n"
      "buffer_load_dwordx4 v[76:79], v1, s[4:7], s65 offen offset:0 sc1\n"
      "v_mfma_f32_16x16x32_bf16 v[40:43], v[92:95], a[92:95], v[40:43]\n"
      "buffer_load_dwordx4 v[92:95], v1, s[4:7], s65 offen offset:1024 sc1\n"
      "v_mfma_f32_16x16x32_bf16 v[28:31], v[128:131], a[96:99], v[28:31]\n"
      "v_mfma_f32_16x16x32_bf16 v[28:31], v[132:135], a[112:115], v[28:31]\n"
      "v_mfma_f32_16x16x32_bf16 v[32:35], v[128:131], a[100:103], v[32:35]\n"
      "v_mfma_f32_16x16x32_bf16 v[32:35], v[132:135], a[116:119], v[32:35]\n"
      "v_mfma_f32_16x16x32_bf16 v[36:39], v[128:131], a[104:107], v[36:39]\n"
      "v_mfma_f32_16x16x32_bf16 v[36:39], v[132:135], a[120:123], v[36:39]\n"
      "s_waitcnt vmcnt(16)\n"
      "v_mfma_f32_16x16x32_bf16 v[40:43], v[128:131], a[108:111], v[40:43]\n"
      "buffer_load_dwordx4 v[128:131], v1, s[4:7], s65 offen offset:2048 sc1\n"
      "v_mfma_f32_16x16x32_bf16 v[40:43], v[132:135], a[124:127], v[40:43]\n"
      "buffer_load_dwordx4 v[132:135], v1, s[4:7], s65 offen offset:3072 sc1\n"
      "v_mfma_f32_16x16x32_bf16 v[28:31], v[136:139], a[128:131], v[28:31]\n"
      "s_add_u32 s65, s65, 4096\n"
      "v_mfma_f32_16x16x32_bf16 v[28:31], v[140:143], a[144:147], v[28:31]\n"
      "s_lshl_b32 s47, s80, 7\n"
      "s_add_u32 s47, s47, s74\n"
      "v_mfma_f32_16x16x32_bf16 v[32:35], v[136:139], a[132:135], v[32:35]\n"
      "buffer_store_dwordx2 v[6:7], v11, s[4:7], s47 offen sc1\n"
      "v_mfma_f32_16x16x32_bf16 v[32:35], v[140:143], a[148:151], v[32:35]\n"
      "v_mfma_f32_16x16x32_bf16 v[36:39], v[136:139], a[136:139], v[36:39]\n"
      "v_mfma_f32_16x16x32_bf16 v[36:39], v[140:143], a[152:155], v[36:39]\n"
      "s_waitcnt vmcnt(17)\n"
      "v_mfma_f32_16x16x32_bf16 v[40:43], v[136:139], a[140:143], v[40:43]\n"
      "buffer_load_dwordx4 v[136:139], v1, s[4:7], s65 offen offset:0 sc1\n"
      "v_mfma_f32_16x16x32_bf16 v[40:43], v[140:143], a[156:159], v[40:43]\n"
      "buffer_load_dwordx4 v[140:143], v1, s[4:7], s65 offen offset:1024 sc1\n"
      "v_mfma_f32_16x16x32_bf16 v[28:31], v[144:147], a[160:163], v[28:31]\n"
      "v_mfma_f32_16x16x32_bf16 v[28:31], v[148:151], a[176:179], v[28:31]\n"
      "v_mfma_f32_16x16x32_bf16 v[32:35], v[144:147], a[164:167], v[32:35]\n"
      "v_mfma_f32_16x16x32_bf16 v[32:35], v[148:151], a[180:183], v[32:35]\n"
      "v_mfma_f32_16x16x32_bf16 v[36:39], v[144:147], a[168:171], v[36:39]\n"
      "v_mfma_f32_16x16x32_bf16 v[36:39], v[148:151], a[184:187], v[36:39]\n"
      "s_waitcnt vmcnt(17)\n"
      "v_mfma_f32_16x16x32_bf16 v[40:43], v[144:147], a[172:175], v[40:43]\n"
      "buffer_load_dwordx4 v[144:147], v1, s[4:7], s65 offen offset:2048 sc1\n"
      "v_mfma_f32_16x16x32_bf16 v[40:43], v[148:151], a[188:191], v[40:43]\n"
      "buffer_load_dwordx4 v[148:151], v1, s[4:7], s65 offen offset:3072 sc1\n"
      "v_mfma_f32_16x16x32_bf16 v[28:31], v[152:155], a[192:195], v[28:31]\n"
      "s_add_u32 s65, s65, 4096\n"
      "v_mfma_f32_16x16x32_bf16 v[28:31], v[156:159], a[208:211], v[28:31]\n"
      "v_mfma_f32_16x16x32_bf16 v[32:35], v[152:155], a[196:199], v[32:35]\n"
      "v_mfma_f32_16x16x32_bf16 v[32:35], v[156:159], a[212:215], v[32:35]\n"
      "v_mfma_f32_16x16x32_bf16 v[36:39], v[152:155], a[200:203], v[36:39]\n"
      "v_mfma_f32_16x16x32_bf16 v[36:39], v[156:159], a[216:219], v[36:39]\n"
      "s_waitcnt vmcnt(17)\n"
      "v_mfma_f32_16x16x32_bf16 v[40:43], v[152:155], a[204:207], v[40:43]\n"
      "buffer_load_dwordx4 v[152:155], v1, s[4:7], s65 offen offset:0 sc1\n"
      "v_mfma_f32_16x16x32_bf16 v[40:43], v[156:159], a[220:223], v[40:43]\n"
      "buffer_load_dwordx4 v[156:159], v1, s[4:7], s65 offen offset:1024 sc1\n"
      "v_mfma_f32_16x16x32_bf16 v[28:31], v[160:163], a[224:227], v[28:31]\n"
      "v_mfma_f32_16x16x32_bf16 v[28:31], v[164:167], a[240:243], v[28:31]\n"
      "v_mfma_f32_16x16x32_bf16 v[32:35], v[160:163], a[228:231], v[32:35]\n"
      "v_mfma_f32_16x16x32_bf16 v[32:35], v[164:167], a[244:247], v[32:35]\n"
      "v_mfma_f32_16x16x32_bf16 v[36:39], v[160:163], a[232:235], v[36:39]\n"
      "v_mfma_f32_16x16x32_bf16 v[36:39], v[164:167], a[248:251], v[36:39]\n"
      "s_waitcnt vmcnt(17)\n"
      "v_mfma_f32_16x16x32_bf16 v[40:43], v[160:163], a[236:239], v[40:43]\n"
      "buffer_load_dwordx4 v[160:163], v1, s[4:7], s65 offen offset:2048 sc1\n"
      "v_mfma_f32_16x16x32_bf16 v[40:43], v[164:167], a[252:255], v[40:43]\n"
      "buffer_load_dwordx4 v[164:167], v1, s[4:7], s65 offen offset:3072 sc1\n"
      "v_mfma_f32_16x16x32_bf16 v[28:31], v[168:171], v[96:99], v[28:31]\n"
      "s_add_u32 s65, s65, 4096\n"
      "v_mfma_f32_16x16x32_bf16 v[28:31], v[172:175], v[112:115], v[28:31]\n"
      "v_mfma_f32_16x16x32_bf16 v[32:35], v[168:171], v[100:103], v[32:35]\n"
      "v_mfma_f32_16x16x32_bf16 v[32:35], v[172:175], v[116:119], v[32:35]\n"
      "v_mfma_f32_16x16x32_bf16 v[36:39], v[168:171], v[104:107], v[36:39]\n"
      "v_mfma_f32_16x16x32_bf16 v[36:39], v[172:175], v[120:123], v[36:39]\n"
      "s_waitcnt vmcnt(17)\n"
      "v_mfma_f32_16x16x32_bf16 v[40:43], v[168:171], v[108:111], v[40:43]\n"
      "buffer_load_dwordx4 v[168:171], v1, s[4:7], s65 offen offset:0 sc1\n"
      "v_mfma_f32_16x16x32_bf16 v[40:43], v[172:175], v[124:127], v[40:43]\n"
      "buffer_load_dwordx4 v[172:175], v1, s[4:7], s65 offen offset:1024 sc1\n"
      "s_sub_u32 s70, s70, 1\n"
      "s_cbranch_scc0 .LLP22_%=\n"
      "v_mfma_f32_16x16x32_bf16 v[196:199], v[56:59], a[0:3], 0\n"
      "s_mov_b32 s80, s79\n"
      "s_mov_b32 s79, s78\n"
      "v_mfma_f32_16x16x32_bf16 v[196:199], v[60:63], a[16:19], v[196:199]\n"
      "s_add_u32 s46, s46, 4\n"
      "s_and_b32 s46, s46, 31\n"
      "v_mfma_f32_16x16x32_bf16 v[200:203], v[56:59], a[4:7], 0\n"
      "s_mov_b32 s78, s45\n"
      "v_cvt_pk_bf16_f32 v12, v28, v29\n"
      "v_cvt_pk_bf16_f32 v13, v30, v31\n"
      "v_mfma_f32_16x16x32_bf16 v[200:203], v[60:63], a[20:23], v[200:203]\n"
      "s_mov_b32 s45, s46\n"
      "s_mul_i32 s65, s46, 18432\n"
      "v_cvt_pk_bf16_f32 v14, v32, v33\n"
      "v_mfma_f32_16x16x32_bf16 v[204:207], v[56:59], a[8:11], 0\n"
      "s_add_u32 s65, s65, s53\n"
      "v_cvt_pk_bf16_f32 v15, v34, v35\n"
      "v_cvt_pk_bf16_f32 v16, v36, v37\n"
      "v_mfma_f32_16x16x32_bf16 v[204:207], v[60:63], a[24:27], v[204:207]\n"
      "s_waitcnt vmcnt(15)\n"
      "v_cvt_pk_bf16_f32 v17, v38, v39\n"
      "v_cvt_pk_bf16_f32 v18, v40, v41\n"
      "v_mfma_f32_16x16x32_bf16 v[208:211], v[56:59], a[12:15], 0\n"
      "buffer_load_dwordx4 v[56:59], v1, s[4:7], s65 offen offset:0 sc1\n"
      "v_mfma_f32_16x16x32_bf16 v[208:211], v[60:63], a[28:31], v[208:211]\n"
      "buffer_load_dwordx4 v[60:63], v1, s[4:7], s65 offen offset:1024 sc1\n"
      "v_cvt_pk_bf16_f32 v19, v42, v43\n"
      "v_mfma_f32_16x16x32_bf16 v[196:199], v[64:67], a[32:35], v[196:199]\n"
      "s_lshl_b32 s47, s79, 11\n"
      "s_add_u32 s47, s47, s73\n"
      "v_mfma_f32_16x16x32_bf16 v[196:199], v[68:71], a[48:51], v[196:199]\n"
      "buffer_store_dwordx4 v[12:15], v1, s[4:7], s47 offen sc1\n"
      "v_mfma_f32_16x16x32_bf16 v[200:203], v[64:67], a[36:39], v[200:203]\n"
      "buffer_store_dwordx4 v[16:19], v1, s[4:7], s47 offen offset:1024 sc1\n"
      "v_mfma_f32_16x16x32_bf16 v[200:203], v[68:71], a[52:55], v[200:203]\n"
      "v_mfma_f32_16x16x32_bf16 v[204:207], v[64:67], a[40:43], v[204:207]\n"
      "v_mfma_f32_16x16x32_bf16 v[204:207], v[68:71], a[56:59], v[204:207]\n"
      "s_waitcnt vmcnt(17)\n"
      "v_mfma_f32_16x16x32_bf16 v[208:211], v[64:67], a[44:47], v[208:211]\n"
      "buffer_load_dwordx4 v[64:67], v1, s[4:7], s65 offen offset:2048 sc1\n"
      "v_mfma_f32_16x16x32_bf16 v[208:211], v[68:71], a[60:63], v[208:211]\n"
      "buffer_load_dwordx4 v[68:71], v1, s[4:7], s65 offen offset:3072 sc1\n"
      "v_mfma_f32_16x16x32_bf16 v[196:199], v[76:79], a[64:67], v[196:199]\n"
      "s_add_u32 s65, s65, 4096\n"
      "v_mfma_f32_16x16x32_bf16 v[196:199], v[92:95], a[80:83], v[196:199]\n"
      "v_mfma_f32_16x16x32_bf16 v[200:203], v[76:79], a[68:71], v[200:203]\n"
      "v_mfma_f32_16x16x32_bf16 v[200:203], v[92:95], a[84:87], v[200:203]\n"
      "v_mfma_f32_16x16x32_bf16 v[204:207], v[76:79], a[72:75], v[204:207]\n"
      "v_mfma_f32_16x16x32_bf16 v[204:207], v[92:95], a[88:91], v[204:207]\n"
      "s_waitcnt vmcnt(17)\n"
      "v_mfma_f32_16x16x32_bf16 v[208:211], v[76:79], a[76:79], v[208:211]\n"
      "buffer_load_dwordx4 v[76:79], v1, s[4:7], s65 offen offset:0 sc1\n"
      "v_mfma_f32_16x16x32_bf16 v[208:211], v[92:95], a[92:95], v[208:211]\n"
      "buffer_load_dwordx4 v[92:95], v1, s[4:7], s65 offen offset:1024 sc1\n"
      "v_mfma_f32_16x16x32_bf16 v[196:199], v[128:131], a[96:99], v[196:199]\n"
      "v_mfma_f32_16x16x32_bf16 v[196:199], v[132:135], a[112:115], v[196:199]\n"
      "v_mfma_f32_16x16x32_bf16 v[200:203], v[128:131], a[100:103], v[200:203]\n"
      "v_mfma_f32_16x16x32_bf16 v[200:203], v[132:135], a[116:119], v[200:203]\n"
      "v_mfma_f32_16x16x32_bf16 v[204:207], v[128:131], a[104:107], v[204:207]\n"
      "v_mfma_f32_16x16x32_bf16 v[204:207], v[132:135], a[120:123], v[204:207]\n"
      "s_waitcnt vmcnt(16)\n"
      "v_mfma_f32_16x16x32_bf16 v[208:211], v[128:131], a[108:111], v[208:211]\n"
      "buffer_load_dwordx4 v[128:131], v1, s[4:7], s65 offen offset:2048 sc1\n"
      "v_mfma_f32_16x16x32_bf16 v[208:211], v[132:135], a[124:127], v[208:211]\n"
      "buffer_load_dwordx4 v[132:135], v1, s[4:7], s65 offen offset:3072 sc1\n"
      "v_mfma_f32_16x16x32_bf16 v[196:199], v[136:139], a[128:131], v[196:199]\n"
      "s_add_u32 s65, s65, 4096\n"
      "v_mfma_f32_16x16x32_bf16 v[196:199], v[140:143], a[144:147], v[196:199]\n"
      "s_lshl_b32 s47, s80, 7\n"
      "s_add_u32 s47, s47, s74\n"
      "v_mfma_f32_16x16x32_bf16 v[200:203], v[136:139], a[132:135], v[200:203]\n"
      "buffer_store_dwordx2 v[6:7], v11, s[4:7], s47 offen sc1\n"
      "v_mfma_f32_16x16x32_bf16 v[200:203], v[140:143], a[148:151], v[200:203]\n"
      "v_mfma_f32_16x16x32_bf16 v[204:207], v[136:139], a[136:139], v[204:207]\n"
      "v_mfma_f32_16x16x32_bf16 v[204:207], v[140:143], a[152:155], v[204:207]\n"
      "s_waitcnt vmcnt(17)\n"
      "v_mfma_f32_16x16x32_bf16 v[208:211], v[136:139], a[140:143], v[208:211]\n"
      "buffer_load_dwordx4 v[136:139], v1, s[4:7], s65 offen offset:0 sc1\n"
      "v_mfma_f32_16x16x32_bf16 v[208:211], v[140:143], a[156:159], v[208:211]\n"
      "buffer_load_dwordx4 v[140:143], v1, s[4:7], s65 offen offset:1024 sc1\n"
      "v_mfma_f32_16x16x32_bf16 v[196:199], v[144:147], a[160:163], v[196:199]\n"
      "v_mfma_f32_16x16x32_bf16 v[196:199], v[148:151], a[176:179], v[196:199]\n"
      "v_mfma_f32_16x16x32_bf16 v[200:203], v[144:147], a[164:167], v[200:203]\n"
      "v_mfma_f32_16x16x32_bf16 v[200:203], v[148:151], a[180:183], v[200:203]\n"
      "v_mfma_f32_16x16x32_bf16 v[204:207], v[144:147], a[168:171], v[204:207]\n"
      "v_mfma_f32_16x16x32_bf16 v[204:207], v[148:151], a[184:187], v[204:207]\n"
      "s_waitcnt vmcnt(17)\n"
      "v_mfma_f32_16x16x32_bf16 v[208:211], v[144:147], a[172:175], v[208:211]\n"
      "buffer_load_dwordx4 v[144:147], v1, s[4:7], s65 offen offset:2048 sc1\n"
      "v_mfma_f32_16x16x32_bf16 v[208:211], v[148:151], a[188:191], v[208:211]\n"
      "buffer_load_dwordx4 v[148:151], v1, s[4:7], s65 offen offset:3072 sc1\n"
      "v_mfma_f32_16x16x32_bf16 v[196:199], v[152:155], a[192:195], v[196:199]\n"
      "s_add_u32 s65, s65, 4096\n"
      "v_mfma_f32_16x16x32_bf16 v[196:199], v[156:159], a[208:211], v[196:199]\n"
      "v_mfma_f32_16x16x32_bf16 v[200:203], v[152:155], a[196:199], v[200:203]\n"
      "v_mfma_f32_16x16x32_bf16 v[200:203], v[156:159], a[212:215], v[200:203]\n"
      "v_mfma_f32_16x16x32_bf16 v[204:207], v[152:155], a[200:203], v[204:207]\n"
      "v_mfma_f32_16x16x32_bf16 v[204:207], v[156:159], a[216:219], v[204:207]\n"
      "s_waitcnt vmcnt(17)\n"
      "v_mfma_f32_16x16x32_bf16 v[208:211], v[152:155], a[204:207], v[208:211]\n"
      "buffer_load_dwordx4 v[152:155], v1, s[4:7], s65 offen offset:0 sc1\n"
      "v_mfma_f32_16x16x32_bf16 v[208:211], v[156:159], a[220:223], v[208:211]\n"
      "buffer_load_dwordx4 v[156:159], v1, s[4:7], s65 offen offset:1024 sc1\n"
      "v_mfma_f32_16x16x32_bf16 v[196:199], v[160:163], a[224:227], v[196:199]\n"
      "v_mfma_f32_16x16x32_bf16 v[196:199], v[164:167], a[240:243], v[196:199]\n"
      "v_mfma_f32_16x16x32_bf16 v[200:203], v[160:163], a[228:231], v[200:203]\n"
      "v_mfma_f32_16x16x32_bf16 v[200:203], v[164:167], a[244:247], v[200:203]\n"
      "v_mfma_f32_16x16x32_bf16 v[204:207], v[160:163], a[232:235], v[204:207]\n"
      "v_mfma_f32_16x16x32_bf16 v[204:207], v[164:167], a[248:251], v[204:207]\n"
      "s_waitcnt vmcnt(17)\n"
      "v_mfma_f32_16x16x32_bf16 v[208:211], v[160:163], a[236:239], v[208:211]\n"
      "buffer_load_dwordx4 v[160:163], v1, s[4:7], s65 offen offset:2048 sc1\n"
      "v_mfma_f32_16x16x32_bf16 v[208:211], v[164:167], a[252:255], v[208:211]\n"
      "buffer_load_dwordx4 v[164:167], v1, s[4:7], s65 offen offset:3072 sc1\n"
      "v_mfma_f32_16x16x32_bf16 v[196:199], v[168:171], v[96:99], v[196:199]\n"
      "s_add_u32 s65, s65, 4096\n"
      "v_mfma_f32_16x16x32_bf16 v[196:199], v[172:175], v[112:115], v[196:199]\n"
      "v_mfma_f32_16x16x32_bf16 v[200:203], v[168:171], v[100:103], v[200:203]\n"
      "v_mfma_f32_16x16x32_bf16 v[200:203], v[172:175], v[116:119], v[200:203]\n"
      "s_mov_b32 exec_hi, 0\n"
      "s_mov_b32 exec_lo, 65535\n"
      "v_mov_b32 v24, 0\n"
      "v_mov_b32 v25, 0\n"
      "buffer_atomic_or_x2 v[24:25], v178, s[4:7], s77 offen sc0 sc1\n"
      "s_mov_b64 exec, -1\n"
      "v_mfma_f32_16x16x32_bf16 v[204:207], v[168:171], v[104:107], v[204:207]\n"
      "v_mfma_f32_16x16x32_bf16 v[204:207], v[172:175], v[120:123], v[204:207]\n"
      "s_waitcnt vmcnt(18)\n"
      "v_mfma_f32_16x16x32_bf16 v[208:211], v[168:171], v[108:111], v[208:211]\n"
      "buffer_load_dwordx4 v[168:171], v1, s[4:7], s65 offen offset:0 sc1\n"
      "v_mfma_f32_16x16x32_bf16 v[208:211], v[172:175], v[124:127], v[208:211]\n"
      "buffer_load_dwordx4 v[172:175], v1, s[4:7], s65 offen offset:1024 sc1\n"
      "v_mfma_f32_16x16x32_bf16 v[28:31], v[56:59], a[0:3], 0\n"
      "v_mfma_f32_16x16x32_bf16 v[28:31], v[60:63], a[16:19], v[28:31]\n"
      "s_mov_b32 s80, s79\n"
      "s_mov_b32 s79, s78\n"
      "v_mfma_f32_16x16x32_bf16 v[32:35], v[56:59], a[4:7], 0\n"
      "v_cvt_pk_bf16_f32 v12, v196, v197\n"
      "v_mfma_f32_16x16x32_bf16 v[32:35], v[60:63], a[20:23], v[32:35]\n"
      "v_cvt_pk_bf16_f32 v13, v198, v199\n"
      "v_cvt_pk_bf16_f32 v14, v200, v201\n"
      "v_mfma_f32_16x16x32_bf16 v[36:39], v[56:59], a[8:11], 0\n"
      "v_cvt_pk_bf16_f32 v15, v202, v203\n"
      "v_mfma_f32_16x16x32_bf16 v[36:39], v[60:63], a[24:27], v[36:39]\n"
      "s_waitcnt vmcnt(16)\n"
      "v_cvt_pk_bf16_f32 v16, v204, v205\n"
      "v_mfma_f32_16x16x32_bf16 v[40:43], v[56:59], a[12:15], 0\n"
      "v_cvt_pk_bf16_f32 v17, v206, v207\n"
      "v_cvt_pk_bf16_f32 v18, v208, v209\n"
      "v_mfma_f32_16x16x32_bf16 v[40:43], v[60:63], a[28:31], v[40:43]\n"
      "v_cvt_pk_bf16_f32 v19, v210, v211\n"
      "v_mfma_f32_16x16x32_bf16 v[28:31], v[64:67], a[32:35], v[28:31]\n"
      "s_lshl_b32 s47, s79, 11\n"
      "s_add_u32 s47, s47, s73\n"
      "v_mfma_f32_16x16x32_bf16 v[28:31], v[68:71], a[48:51], v[28:31]\n"
      "buffer_store_dwordx4 v[12:15], v1, s[4:7], s47 offen sc1\n"
      "v_mfma_f32_16x16x32_bf16 v[32:35], v[64:67], a[36:39], v[32:35]\n"
      "buffer_store_dwordx4 v[16:19], v1, s[4:7], s47 offen offset:1024 sc1\n"
      "v_mfma_f32_16x16x32_bf16 v[32:35], v[68:71], a[52:55], v[32:35]\n"
      "v_mfma_f32_16x16x32_bf16 v[36:39], v[64:67], a[40:43], v[36:39]\n"
      "v_mfma_f32_16x16x32_bf16 v[36:39], v[68:71], a[56:59], v[36:39]\n"
      "s_waitcnt vmcnt(16)\n"
      "v_mfma_f32_16x16x32_bf16 v[40:43], v[64:67], a[44:47], v[40:43]\n"
      "v_mfma_f32_16x16x32_bf16 v[40:43], v[68:71], a[60:63], v[40:43]\n"
      "v_mfma_f32_16x16x32_bf16 v[28:31], v[76:79], a[64:67], v[28:31]\n"
      "v_mfma_f32_16x16x32_bf16 v[28:31], v[92:95], a[80:83], v[28:31]\n"
      "v_mfma_f32_16x16x32_bf16 v[32:35], v[76:79], a[68:71], v[32:35]\n"
      "v_mfma_f32_16x16x32_bf16 v[32:35], v[92:95], a[84:87], v[32:35]\n"
      "v_mfma_f32_16x16x32_bf16 v[36:39], v[76:79], a[72:75], v[36:39]\n"
      "v_mfma_f32_16x16x32_bf16 v[36:39], v[92:95], a[88:91], v[36:39]\n"
      "s_waitcnt vmcnt(14)\n"
      "v_mfma_f32_16x16x32_bf16 v[40:43], v[76:79], a[76:79], v[40:43]\n"
      "v_mfma_f32_16x16x32_bf16 v[40:43], v[92:95], a[92:95], v[40:43]\n"
      "v_mfma_f32_16x16x32_bf16 v[28:31], v[128:131], a[96:99], v[28:31]\n"
      "v_mfma_f32_16x16x32_bf16 v[28:31], v[132:135], a[112:115], v[28:31]\n"
      "v_mfma_f32_16x16x32_bf16 v[32:35], v[128:131], a[100:103], v[32:35]\n"
      "v_mfma_f32_16x16x32_bf16 v[32:35], v[132:135], a[116:119], v[32:35]\n"
      "v_mfma_f32_16x16x32_bf16 v[36:39], v[128:131], a[104:107], v[36:39]\n"
      "v_mfma_f32_16x16x32_bf16 v[36:39], v[132:135], a[120:123], v[36:39]\n"
      "s_waitcnt vmcnt(11)\n"
      "v_mfma_f32_16x16x32_bf16 v[40:43], v[128:131], a[108:111], v[40:43]\n"
      "v_mfma_f32_16x16x32_bf16 v[40:43], v[132:135], a[124:127], v[40:43]\n"
      "v_mfma_f32_16x16x32_bf16 v[28:31], v[136:139], a[128:131], v[28:31]\n"
      "v_mfma_f32_16x16x32_bf16 v[28:31], v[140:143], a[144:147], v[28:31]\n"
      "s_lshl_b32 s47, s80, 7\n"
      "s_add_u32 s47, s47, s74\n"
      "v_mfma_f32_16x16x32_bf16 v[32:35], v[136:139], a[132:135], v[32:35]\n"
      "buffer_store_dwordx2 v[6:7], v11, s[4:7], s47 offen sc1\n"
      "v_mfma_f32_16x16x32_bf16 v[32:35], v[140:143], a[148:151], v[32:35]\n"
      "v_mfma_f32_16x16x32_bf16 v[36:39], v[136:139], a[136:139], v[36:39]\n"
      "v_mfma_f32_16x16x32_bf16 v[36:39], v[140:143], a[152:155], v[36:39]\n"
      "s_waitcnt vmcnt(10)\n"
      "v_mfma_f32_16x16x32_bf16 v[40:43], v[136:139], a[140:143], v[40:43]\n"
      "v_mfma_f32_16x16x32_bf16 v[40:43], v[140:143], a[156:159], v[40:43]\n"
      "s_mov_b32 exec_hi, 0\n"
      "s_mov_b32 exec_lo, 65535\n"
      "s_waitcnt vmcnt(5)\n"
      "v_cmp_ne_u64 vcc, s[42:43], v[24:25]\n"
      "s_cbranch_vccz .LD24_%=\n"
      "s_mov_b32 s44, 0\n"
      ".LP23_%=:\n"
      "s_mov_b64 exec, vcc\n"
      "v_mov_b32 v24, 0\n"
      "v_mov_b32 v25, 0\n"
      "buffer_atomic_or_x2 v[24:25], v178, s[4:7], s77 offen sc0 sc1\n"
      "s_waitcnt vmcnt(0)\n"
      "v_cmp_ne_u64 vcc, s[42:43], v[24:25]\n"
      "s_cbranch_vccz .LD24_%=\n"
      "s_add_u32 s44, s44, 1\n"
      "s_cmp_lt_u32 s44, s41\n"
      "s_cbranch_scc1 .LP23_%=\n"
      "s_mov_b32 s41, 0\n"
      ".LD24_%=:\n"
      "s_mov_b64 exec, -1\n"
      "s_add_u32 s48, s39, 7\n"
      "s_and_b32 s48, s48, 7\n"
      "s_mul_i32 s48, s48, 73728\n"
      "s_add_u32 s48, s48, s76\n"
      "buffer_load_dwordx4 v[56:59], v1, s[4:7], s48 offen sc1\n"
      "buffer_load_dwordx4 v[60:63], v1, s[4:7], s48 offen offset:1024 sc1\n"
      "v_mfma_f32_16x16x32_bf16 v[28:31], v[144:147], a[160:163], v[28:31]\n"
      "v_mfma_f32_16x16x32_bf16 v[28:31], v[148:151], a[176:179], v[28:31]\n"
      "v_mfma_f32_16x16x32_bf16 v[32:35], v[144:147], a[164:167], v[32:35]\n"
      "v_mfma_f32_16x16x32_bf16 v[32:35], v[148:151], a[180:183], v[32:35]\n"
      "s_add_u32 s48, s39, 6\n"
      "s_and_b32 s48, s48, 7\n"
      "s_mul_i32 s48, s48, 73728\n"
      "s_add_u32 s48, s48, s76\n"
      "buffer_load_dwordx4 v[64:67], v1, s[4:7], s48 offen sc1\n"
      "buffer_load_dwordx4 v[68:71], v1, s[4:7], s48 offen offset:1024 sc1\n"
      "v_mfma_f32_16x16x32_bf16 v[36:39], v[144:147], a[168:171], v[36:39]\n"
      "v_mfma_f32_16x16x32_bf16 v[36:39], v[148:151], a[184:187], v[36:39]\n"
      "v_mfma_f32_16x16x32_bf16 v[40:43], v[144:147], a[172:175], v[40:43]\n"
      "v_mfma_f32_16x16x32_bf16 v[40:43], v[148:151], a[188:191], v[40:43]\n"
      "s_add_u32 s48, s39, 5\n"
      "s_and_b32 s48, s48, 7\n"
      "s_mul_i32 s48, s48, 73728\n"
      "s_add_u32 s48, s48, s76\n"
      "buffer_load_dwordx4 v[76:79], v1, s[4:7], s48 offen sc1\n"
      "buffer_load_dwordx4 v[92:95], v1, s[4:7], s48 offen offset:1024 sc1\n"
      "v_mfma_f32_16x16x32_bf16 v[28:31], v[152:155], a[192:195], v[28:31]\n"
      "v_mfma_f32_16x16x32_bf16 v[28:31], v[156:159], a[208:211], v[28:31]\n"
      "v_mfma_f32_16x16x32_bf16 v[32:35], v[152:155], a[196:199], v[32:35]\n"
      "v_mfma_f32_16x16x32_bf16 v[32:35], v[156:159], a[212:215], v[32:35]\n"
      "s_add_u32 s48, s39, 4\n"
      "s_and_b32 s48, s48, 7\n"
      "s_mul_i32 s48, s48, 73728\n"
      "s_add_u32 s48, s48, s76\n"
      "buffer_load_dwordx4 v[128:131], v1, s[4:7], s48 offen sc1\n"
      "buffer_load_dwordx4 v[132:135], v1, s[4:7], s48 offen offset:1024 sc1\n"
      "v_mfma_f32_16x16x32_bf16 v[36:39], v[152:155], a[200:203], v[36:39]\n"
      "v_mfma_f32_16x16x32_bf16 v[36:39], v[156:159], a[216:219], v[36:39]\n"
      "v_mfma_f32_16x16x32_bf16 v[40:43], v[152:155], a[204:207], v[40:43]\n"
      "v_mfma_f32_16x16x32_bf16 v[40:43], v[156:159], a[220:223], v[40:43]\n"
      "buffer_load_dwordx4 v[136:139], v177, s[4:7], s75 offen sc1\n"
      "s_add_u32 s75, s75, 73728\n"
      "buffer_load_dwordx4 v[140:143], v177, s[4:7], s75 offen sc1\n"
      "s_add_u32 s75, s75, 73728\n"
      "v_mfma_f32_16x16x32_bf16 v[28:31], v[160:163], a[224:227], v[28:31]\n"
      "v_mfma_f32_16x16x32_bf16 v[28:31], v[164:167], a[240:243], v[28:31]\n"
      "v_mfma_f32_16x16x32_bf16 v[32:35], v[160:163], a[228:231], v[32:35]\n"
      "v_mfma_f32_16x16x32_bf16 v[32:35], v[164:167], a[244:247], v[32:35]\n"
      "buffer_load_dwordx4 v[144:147], v177, s[4:7], s75 offen sc1\n"
      "s_add_u32 s75, s75, 73728\n"
      "buffer_load_dwordx4 v[148:151], v177, s[4:7], s75 offen sc1\n"
      "s_add_u32 s75, s75, 73728\n"
      "v_mfma_f32_16x16x32_bf16 v[36:39], v[160:163], a[232:235], v[36:39]\n"
      "v_mfma_f32_16x16x32_bf16 v[36:39], v[164:167], a[248:251], v[36:39]\n"
      "s_waitcnt vmcnt(15)\n"
      "v_mfma_f32_16x16x32_bf16 v[40:43], v[160:163], a[236:239], v[40:43]\n"
      "v_mfma_f32_16x16x32_bf16 v[40:43], v[164:167], a[252:255], v[40:43]\n"
      "v_mfma_f32_16x16x32_bf16 v[28:31], v[168:171], v[96:99], v[28:31]\n"
      "v_mfma_f32_16x16x32_bf16 v[28:31], v[172:175], v[112:115], v[28:31]\n"
      "v_mfma_f32_16x16x32_bf16 v[32:35], v[168:171], v[100:103], v[32:35]\n"
      "v_mfma_f32_16x16x32_bf16 v[32:35], v[172:175], v[116:119], v[32:35]\n"
      "v_mfma_f32_16x16x32_bf16 v[36:39], v[168:171], v[104:107], v[36:39]\n"
      "v_mfma_f32_16x16x32_bf16 v[36:39], v[172:175], v[120:123], v[36:39]\n"
      "v_mfma_f32_16x16x32_bf16 v[40:43], v[168:171], v[108:111], v[40:43]\n"
      "v_mfma_f32_16x16x32_bf16 v[40:43], v[172:175], v[124:127], v[40:43]\n"
      "s_waitcnt vmcnt(13)\n"
      "s_lshl_b32 s47, s79, 7\n"
      "s_add_u32 s47, s47, s74\n"
      "buffer_store_dwordx2 v[6:7], v11, s[4:7], s47 offen sc1\n"
      "s_waitcnt vmcnt(12)\n"
      "v_mfma_f32_16x16x32_bf16 v[28:31], v[188:191], v[56:59], v[28:31]\n"
      "v_mfma_f32_16x16x32_bf16 v[32:35], v[192:195], v[56:59], v[32:35]\n"
      "s_waitcnt vmcnt(11)\n"
      "v_mfma_f32_16x16x32_bf16 v[36:39], v[188:191], v[60:63], v[36:39]\n"
      "v_mfma_f32_16x16x32_bf16 v[40:43], v[192:195], v[60:63], v[40:43]\n"
      "s_waitcnt vmcnt(10)\n"
      "v_mfma_f32_16x16x32_bf16 v[28:31], v[188:191], v[64:67], v[28:31]\n"
      "v_mfma_f32_16x16x32_bf16 v[32:35], v[192:195], v[64:67], v[32:35]\n"
      "s_waitcnt vmcnt(9)\n"
      "v_mfma_f32_16x16x32_bf16 v[36:39], v[188:191], v[68:71], v[36:39]\n"
      "v_mfma_f32_16x16x32_bf16 v[40:43], v[192:195], v[68:71], v[40:43]\n"
      "s_waitcnt vmcnt(8)\n"
      "v_mfma_f32_16x16x32_bf16 v[28:31], v[188:191], v[76:79], v[28:31]\n"
      "v_mfma_f32_16x16x32_bf16 v[32:35], v[192:195], v[76:79], v[32:35]\n"
      "s_waitcnt vmcnt(7)\n"
      "v_mfma_f32_16x16x32_bf16 v[36:39], v[188:191], v[92:95], v[36:39]\n"
      "v_mfma_f32_16x16x32_bf16 v[40:43], v[192:195], v[92:95], v[40:43]\n"
      "s_waitcnt vmcnt(6)\n"
      "v_mfma_f32_16x16x32_bf16 v[28:31], v[188:191], v[128:131], v[28:31]\n"
      "v_mfma_f32_16x16x32_bf16 v[32:35], v[192:195], v[128:131], v[32:35]\n"
      "s_waitcnt vmcnt(5)\n"
      "v_mfma_f32_16x16x32_bf16 v[36:39], v[188:191], v[132:135], v[36:39]\n"
      "v_mfma_f32_16x16x32_bf16 v[40:43], v[192:195], v[132:135], v[40:43]\n"
      "s_waitcnt vmcnt(4)\n"
      "v_mfma_f32_16x16x32_bf16 v[216:219], v[212:215], v[136:139], 0\n"
      "s_waitcnt vmcnt(3)\n"
      "v_mfma_f32_16x16x32_bf16 v[216:219], v[212:215], v[140:143], v[216:219]\n"
      "s_waitcnt vmcnt(2)\n"
      "v_mfma_f32_16x16x32_bf16 v[216:219], v[212:215], v[144:147], v[216:219]\n"
      "s_waitcnt vmcnt(1)\n"
      "v_mfma_f32_16x16x32_bf16 v[216:219], v[212:215], v[148:151], v[216:219]\n"
      "s_mov_b32 exec_hi, 0\n"
      "s_mov_b32 exec_lo, 7\n"
      "v_mov_b32 v24, 0\n"
      "v_mov_b32 v25, 0\n"
      "buffer_atomic_or_x2 v[24:25], v179, s[4:7], s77 offen sc0 sc1\n"
      "s_mov_b64 exec, -1\n"
      "s_mul_i32 s45, s38, 73728\n"
      "s_nop 0\n"
      "v_fma_f32 v236, v216, v72, v236\n"
      "v_fma_f32 v237, v217, v72, v237\n"
      "v_fma_f32 v238, v218, v72, v238\n"
      "v_fma_f32 v239, v219, v72, v239\n"
      "v_cvt_pk_bf16_f32 v20, v236, v237\n"
      "v_cvt_pk_bf16_f32 v21, v238, v239\n"
      "v_cvt_pk_bf16_f32 v22, v236, v237\n"
      "v_cvt_pk_bf16_f32 v23, v238, v239\n"
      "s_nop 0\n"
      "v_permlane16_swap_b32 v20, v22\n"
      "v_permlane16_swap_b32 v21, v23\n"
      "s_mov_b32 exec_hi, 0\n"
      "s_mov_b32 exec_lo, 7\n"
      "s_waitcnt vmcnt(0)\n"
      "v_cmp_ne_u64 vcc, s[42:43], v[24:25]\n"
      "s_cbranch_vccz .LD26_%=\n"
      "s_mov_b32 s44, 0\n"
      ".LP25_%=:\n"
      "s_mov_b64 exec, vcc\n"
      "v_mov_b32 v24, 0\n"
      "v_mov_b32 v25, 0\n"
      "buffer_atomic_or_x2 v[24:25], v179, s[4:7], s77 offen sc0 sc1\n"
      "s_waitcnt vmcnt(0)\n"
      "v_cmp_ne_u64 vcc, s[42:43], v[24:25]\n"
      "s_cbranch_vccz .LD26_%=\n"
      "s_add_u32 s44, s44, 1\n"
      "s_cmp_lt_u32 s44, s41\n"
      "s_cbranch_scc1 .LP25_%=\n"
      "s_mov_b32 s41, 0\n"
      ".LD26_%=:\n"
      "s_mov_b64 exec, -1\n"
      "s_add_u32 s48, s39, 3\n"
      "s_and_b32 s48, s48, 7\n"
      "s_mul_i32 s48, s48, 73728\n"
      "s_add_u32 s48, s48, s76\n"
      "buffer_load_dwordx4 v[168:171], v1, s[4:7], s48 offen sc1\n"
      "buffer_load_dwordx4 v[172:175], v1, s[4:7], s48 offen offset:1024 sc1\n"
      "s_add_u32 s48, s39, 2\n"
      "s_and_b32 s48, s48, 7\n"
      "s_mul_i32 s48, s48, 73728\n"
      "s_add_u32 s48, s48, s76\n"
      "buffer_load_dwordx4 v[160:163], v1, s[4:7], s48 offen sc1\n"
      "buffer_load_dwordx4 v[164:167], v1, s[4:7], s48 offen offset:1024 sc1\n"
      "s_add_u32 s48, s39, 1\n"
      "s_and_b32 s48, s48, 7\n"
      "s_mul_i32 s48, s48, 73728\n"
      "s_add_u32 s48, s48, s76\n"
      "buffer_load_dwordx4 v[152:155], v1, s[4:7], s48 offen sc1\n"
      "buffer_load_dwordx4 v[156:159], v1, s[4:7], s48 offen offset:1024 sc1\n"
      "buffer_store_dwordx4 v[20:23], v242, s[8:11], s45 offen\n"
      "s_waitcnt vmcnt(6)\n"
      "v_mfma_f32_16x16x32_bf16 v[28:31], v[188:191], v[168:171], v[28:31]\n"
      "v_mfma_f32_16x16x32_bf16 v[32:35], v[192:195], v[168:171], v[32:35]\n"
      "s_waitcnt vmcnt(5)\n"
      "v_mfma_f32_16x16x32_bf16 v[36:39], v[188:191], v[172:175], v[36:39]\n"
      "v_mfma_f32_16x16x32_bf16 v[40:43], v[192:195], v[172:175], v[40:43]\n"
      "s_waitcnt vmcnt(4)\n"
      "v_mfma_f32_16x16x32_bf16 v[28:31], v[188:191], v[160:163], v[28:31]\n"
      "v_mfma_f32_16x16x32_bf16 v[32:35], v[192:195], v[160:163], v[32:35]\n"
      "s_waitcnt vmcnt(3)\n"
      "v_mfma_f32_16x16x32_bf16 v[36:39], v[188:191], v[164:167], v[36:39]\n"
      "v_mfma_f32_16x16x32_bf16 v[40:43], v[192:195], v[164:167], v[40:43]\n"
      "s_waitcnt vmcnt(2)\n"
      "v_mfma_f32_16x16x32_bf16 v[28:31], v[188:191], v[152:155], v[28:31]\n"
      "v_mfma_f32_16x16x32_bf16 v[32:35], v[192:195], v[152:155], v[32:35]\n"
      "s_waitcnt vmcnt(1)\n"
      "v_mfma_f32_16x16x32_bf16 v[36:39], v[188:191], v[156:159], v[36:39]\n"
      "v_mfma_f32_16x16x32_bf16 v[40:43], v[192:195], v[156:159], v[40:43]\n"
      "s_nop 3\n"
      "v_fma_f32 v220, v28, v72, v220\n"
      "v_fma_f32 v221, v29, v72, v221\n"
      "v_fma_f32 v222, v30, v72, v222\n"
      "v_fma_f32 v223, v31, v72, v223\n"
      "v_fma_f32 v224, v32, v84, v224\n"
      "v_fma_f32 v225, v33, v84, v225\n"
      "v_fma_f32 v226, v34, v84, v226\n"
      "v_fma_f32 v227, v35, v84, v227\n"
      "v_cvt_pk_bf16_f32 v12, v220, v221\n"
      "v_cvt_pk_bf16_f32 v13, v222, v223\n"
      "v_cvt_pk_bf16_f32 v14, v224, v225\n"
      "v_cvt_pk_bf16_f32 v15, v226, v227\n"
      "s_nop 0\n"
      "v_permlane16_swap_b32 v12, v14\n"
      "v_permlane16_swap_b32 v13, v15\n"
      "buffer_store_dwordx4 v[12:15], v240, s[8:11], s45 offen\n"
      "v_fma_f32 v228, v36, v180, v228\n"
      "v_fma_f32 v229, v37, v180, v229\n"
      "v_fma_f32 v230, v38, v180, v230\n"
      "v_fma_f32 v231, v39, v180, v231\n"
      "v_fma_f32 v232, v40, v184, v232\n"
      "v_fma_f32 v233, v41, v184, v233\n"
      "v_fma_f32 v234, v42, v184, v234\n"
      "v_fma_f32 v235, v43, v184, v235\n"
      "v_cvt_pk_bf16_f32 v16, v228, v229\n"
      "v_cvt_pk_bf16_f32 v17, v230, v231\n"
      "v_cvt_pk_bf16_f32 v18, v232, v233\n"
      "v_cvt_pk_bf16_f32 v19, v234, v235\n"
      "s_nop 0\n"
      "v_permlane16_swap_b32 v16, v18\n"
      "v_permlane16_swap_b32 v17, v19\n"
      "buffer_store_dwordx4 v[16:19], v241, s[8:11], s45 offen\n"
      ".LCODE1_%=:\n"
      : "+{v0}"(tid), "+{s[0:1]}"(ka), "+{s2}"(bid)
      :
      : "v1", "v2", "v3", "v4", "v5", "v6", "v7", "v8", "v9", "v10", "v11", "v12", "v13", "v14", "v15", "v16", "v17", "v18", "v19", "v20", "v21", "v22", "v23", "v24", "v25", "v26", "v27", "v28", "v29", "v30", "v31", "v32", "v33", "v34", "v35", "v36", "v37", "v38", "v39", "v40", "v41", "v42", "v43", "v44", "v45", "v46", "v47", "v48", "v49", "v50", "v51", "v52", "v53", "v54", "v55", "v56", "v57", "v58", "v59", "v60", "v61", "v62", "v63", "v64", "v65", "v66", "v67", "v68", "v69", "v70", "v71", "v72", "v73", "v74", "v75", "v76", "v77", "v78", "v79", "v80", "v81", "v82", "v83", "v84", "v85", "v86", "v87", "v88", "v89", "v90", "v91", "v92", "v93", "v94", "v95", "v96", "v97", "v98", "v99", "v100", "v101", "v102", "v103", "v104", "v105", "v106", "v107", "v108", "v109", "v110", "v111", "v112", "v113", "v114", "v115", "v116", "v117", "v118", "v119", "v120", "v121", "v122", "v123", "v124", "v125", "v126", "v127", "v128", "v129", "v130", "v131", "v132", "v133", "v134", "v135", "v136", "v137", "v138", "v139", "v140", "v141", "v142", "v143", "v144", "v145", "v146", "v147", "v148", "v149", "v150", "v151", "v152", "v153", "v154", "v155", "v156", "v157", "v158", "v159", "v160", "v161", "v162", "v163", "v164", "v165", "v166", "v167", "v168", "v169", "v170", "v171", "v172", "v173", "v174", "v175", "v176", "v177", "v178", "v179", "v180", "v181", "v182", "v183", "v184", "v185", "v186", "v187", "v188", "v189", "v190", "v191", "v192", "v193", "v194", "v195", "v196", "v197", "v198", "v199", "v200", "v201", "v202", "v203", "v204", "v205", "v206", "v207", "v208", "v209", "v210", "v211", "v212", "v213", "v214", "v215", "v216", "v217", "v218", "v219", "v220", "v221", "v222", "v223", "v224", "v225", "v226", "v227", "v228", "v229", "v230", "v231", "v232", "v233", "v234", "v235", "v236", "v237", "v238", "v239", "v240", "v241", "v242", "v243", "v244", "v245", "v246", "v247", "v248", "v249", "v250", "v251", "v252", "v253", "v254", "v255", "a0", "a1", "a2", "a3", "a4", "a5", "a6", "a7", "a8", "a9", "a10", "a11", "a12", "a13", "a14", "a15", "a16", "a17", "a18", "a19", "a20", "a21", "a22", "a23", "a24", "a25", "a26", "a27", "a28", "a29", "a30", "a31", "a32", "a33", "a34", "a35", "a36", "a37", "a38", "a39", "a40", "a41", "a42", "a43", "a44", "a45", "a46", "a47", "a48", "a49", "a50", "a51", "a52", "a53", "a54", "a55", "a56", "a57", "a58", "a59", "a60", "a61", "a62", "a63", "a64", "a65", "a66", "a67", "a68", "a69", "a70", "a71", "a72", "a73", "a74", "a75", "a76", "a77", "a78", "a79", "a80", "a81", "a82", "a83", "a84", "a85", "a86", "a87", "a88", "a89", "a90", "a91", "a92", "a93", "a94", "a95", "a96", "a97", "a98", "a99", "a100", "a101", "a102", "a103", "a104", "a105", "a106", "a107", "a108", "a109", "a110", "a111", "a112", "a113", "a114", "a115", "a116", "a117", "a118", "a119", "a120", "a121", "a122", "a123", "a124", "a125", "a126", "a127", "a128", "a129", "a130", "a131", "a132", "a133", "a134", "a135", "a136", "a137", "a138", "a139", "a140", "a141", "a142", "a143", "a144", "a145", "a146", "a147", "a148", "a149", "a150", "a151", "a152", "a153", "a154", "a155", "a156", "a157", "a158", "a159", "a160", "a161", "a162", "a163", "a164", "a165", "a166", "a167", "a168", "a169", "a170", "a171", "a172", "a173", "a174", "a175", "a176", "a177", "a178", "a179", "a180", "a181", "a182", "a183", "a184", "a185", "a186", "a187", "a188", "a189", "a190", "a191", "a192", "a193", "a194", "a195", "a196", "a197", "a198", "a199", "a200", "a201", "a202", "a203", "a204", "a205", "a206", "a207", "a208", "a209", "a210", "a211", "a212", "a213", "a214", "a215", "a216", "a217", "a218", "a219", "a220", "a221", "a222", "a223", "a224", "a225", "a226", "a227", "a228", "a229", "a230", "a231", "a232", "a233", "a234", "a235", "a236", "a237", "a238", "a239", "a240", "a241", "a242", "a243", "a244", "a245", "a246", "a247", "a248", "a249", "a250", "a251", "a252", "a253", "a254", "a255", "s3", "s4", "s5", "s6", "s7", "s8", "s9", "s10", "s11", "s12", "s13", "s14", "s15", "s16", "s17", "s18", "s19", "s20", "s21", "s22", "s23", "s24", "s25", "s26", "s27", "s28", "s29", "s30", "s31", "s33", "s34", "s35", "s36", "s37", "s38", "s39", "s40", "s41", "s42", "s43", "s44", "s45", "s46", "s47", "s48", "s49", "s50", "s51", "s52", "s53", "s54", "s55", "s56", "s57", "s58", "s59", "s60", "s61", "s62", "s63", "s64", "s65", "s66", "s67", "s68", "s69", "s70", "s71", "s72", "s73", "s74", "s75", "s76", "s77", "s78", "s79", "s80", "s81", "s82", "s83", "s84", "s85", "s86", "s87", "s88", "s89", "s90", "s91", "s92", "s93", "s94", "s95", "s96", "s97", "s98", "s99", "vcc", "scc", "memory");
}
}

extern "C" void kernel_launch(void *const *d_in, const int *in_sizes, int n_in, void *d_out, int out_size, void *d_ws, size_t ws_size,
                              hipStream_t stream) {
  mk::Params p;
  p.x = d_in[0], p.wqkv = d_in[1], p.wo = d_in[2], p.w1 = d_in[4], p.w2 = d_in[5], p.w3 = d_in[6], p.w4 = d_in[7];
  p.out = d_out, p.ws = d_ws;
  (void)hipFuncSetAttribute((const void *)mk::fused, hipFuncAttributeMaxDynamicSharedMemorySize, 163840);
  mk::fused<<<256, 256, 163840, stream>>>(p);
}
```
